```python
import torch

def _softmax(x, axis=-1):
    return torch.softmax(x, dim=axis)
def _gelu(x):
    return torch.nn.functional.gelu(x, approximate="tanh")
def _silu(x):
    return torch.nn.functional.silu(x)
def _sigmoid(x):
    return torch.sigmoid(x)
def _layernorm(x, axis=-1, eps=1e-5):
    m = x.mean(dim=axis, keepdim=True)
    v = x.var(dim=axis, keepdim=True, unbiased=False)
    return (x - m) / torch.sqrt(v + eps)
def _scatter_add(tgt, idx, src, axis=0):
    return tgt.clone().index_add_(axis, idx, src)
def _segment_sum(src, seg, n):
    out = torch.zeros((n,) + src.shape[1:], dtype=src.dtype)
    return out.index_add_(0, seg, src)
def _pad(x, pw):
    flat = tuple(v for pair in reversed(pw) for v in pair)
    return torch.nn.functional.pad(x, flat)

kernel_name = 'gated_mix__grouped_kv_mqa__mha_block__prompt'

def setup_inputs():

    _w = (lambda a: (lambda t: t.to(torch.bfloat16) if t.is_floating_point() else t)(torch.as_tensor(a)))
    def _A_setup():
        B, S, D, H = 1, 2048, 640, 32
        Dh = D // H
        return {
            "x": _w((lambda *s: torch.randn(*s, dtype=torch.bfloat16) * 0.1)(B, S, D)),
            "wq": _w((lambda *s: torch.randn(*s, dtype=torch.bfloat16) * 0.4)(D, H * Dh)),
            "wk": _w((lambda *s: torch.randn(*s, dtype=torch.bfloat16) * 0.4)(D, Dh)),
            "wv": _w((lambda *s: torch.randn(*s, dtype=torch.bfloat16) * 0.4)(D, Dh)),
            "wo": _w((lambda *s: torch.randn(*s, dtype=torch.bfloat16) * 0.4)(H * Dh, D)),
        }
    _A_in = _A_setup()

    def _B_setup():
        B, S, D, H = 1, 2048, 640, 32
        return {
            "x": _w((lambda *s: torch.randn(*s, dtype=torch.bfloat16) * 0.1)(B, S, D)),
            "wqkv": _w((lambda *s: torch.randn(*s, dtype=torch.bfloat16) * 0.4)(D, 3 * D)),
            "wo": _w((lambda *s: torch.randn(*s, dtype=torch.bfloat16) * 0.2)(D, D)),
        }
    _B_in = _B_setup()

    return {
        "A_x": _A_in["x"],
        "A_wq": _A_in["wq"],
        "A_wk": _A_in["wk"],
        "A_wv": _A_in["wv"],
        "A_wo": _A_in["wo"],
        "B_x": _B_in["x"],
        "B_wqkv": _B_in["wqkv"],
        "B_wo": _B_in["wo"],
        "gate_w": _w((lambda *s: torch.randn(*s, dtype=torch.bfloat16) * 0.1)(640) * 16.0),
    }

def reference(A_x, A_wq, A_wk, A_wv, A_wo, B_x, B_wqkv, B_wo, gate_w):
    def _A_ref(x, wq, wk, wv, wo):
        B, S, D = x.shape
        H = 32
        Dh = D // H
        q = (lambda x, *d: x.permute(*d))((x @ wq).reshape(B, S, H, Dh), 0, 2, 1, 3)
        k = x @ wk
        v = x @ wv
        scores = torch.einsum("bhqd,bkd->bhqk", q, k) * (Dh ** -0.5)
        p = _softmax(scores, axis=-1)
        o = torch.einsum("bhqk,bkd->bhqd", p, v)
        return (lambda x, *d: x.permute(*d))(o, 0, 2, 1, 3).reshape(B, S, H * Dh) @ wo

    def _B_ref(x, wqkv, wo):
        B, S, D = x.shape
        H = 32
        Dh = D // H
        qkv = (lambda x, *d: x.permute(*d))((x @ wqkv).reshape(B, S, 3, H, Dh), 2, 0, 3, 1, 4)
        q, k, v = qkv[0], qkv[1], qkv[2]
        scores = torch.einsum("bhqd,bhkd->bhqk", q, k) * (Dh ** -0.5)
        p = _softmax(scores, axis=-1)
        o = torch.einsum("bhqk,bhkd->bhqd", p, v)
        return (lambda x, *d: x.permute(*d))(o, 0, 2, 1, 3).reshape(B, S, D) @ wo

    x = A_x
    g = _sigmoid((x * gate_w).sum(-1, keepdims=True))
    a = _A_ref(x, A_wq, A_wk, A_wv, A_wo)
    b = _B_ref(x, B_wqkv, B_wo)
    return g * a + (1 - g) * b
```

```cpp
#include <hip/hip_runtime.h>
#include <hip/hip_bf16.h>
#include <stdint.h>

typedef __attribute__((ext_vector_type(8))) short bf16x8;
typedef __attribute__((ext_vector_type(4))) short s16x4;
typedef __attribute__((ext_vector_type(4))) float f32x4;
typedef __attribute__((ext_vector_type(16))) float f32x16;
typedef __attribute__((ext_vector_type(32))) float f32x32;
typedef __attribute__((ext_vector_type(2))) unsigned u32x2;
typedef __attribute__((ext_vector_type(4))) unsigned u32x4;
typedef __attribute__((ext_vector_type(2))) __bf16 bf16x2_t;
typedef __attribute__((ext_vector_type(2))) float f32x2;
#define LDSP __attribute__((address_space(3)))
#define DI __device__ __forceinline__

constexpr int SEQ = 2048, DM = 640;
constexpr size_t Q_HEAD = 32 * 2560;
constexpr size_t KV_HEAD = 32 * 5120;
constexpr size_t WS_Q = 0;
constexpr size_t WS_KV = WS_Q + 64 * Q_HEAD;
constexpr size_t WS_G = WS_KV + 33 * KV_HEAD;
constexpr size_t WS_O1 = WS_G + 2048 * 4;
constexpr size_t WS_O2 = WS_O1 + 64 * 65536;
constexpr size_t WS_WO = WS_O2 + 32 * 2048 * 16;

DI unsigned pk_bf16(float a, float b) {
    f32x2 v = {a, b};
    return __builtin_bit_cast(unsigned, __builtin_convertvector(v, bf16x2_t));
}
DI s16x4 tr_read(const LDSP char* p) {
    typedef short v4i16_t __attribute__((ext_vector_type(4)));
    return __builtin_amdgcn_ds_read_tr16_b64_v4i16((LDSP v4i16_t*)p);
}
DI bf16x8 cat8(s16x4 lo, s16x4 hi) { return __builtin_shufflevector(lo, hi, 0, 1, 2, 3, 4, 5, 6, 7); }
#define MFMA16(a, b, c) __builtin_amdgcn_mfma_f32_16x16x32_bf16((a), (b), (c), 0, 0, 0)
#define MFMA32(a, b, c) __builtin_amdgcn_mfma_f32_32x32x16_bf16((a), (b), (c), 0, 0, 0)

constexpr int P_SLOT = 16384 + 20480 + 2560;

template <bool VT>
DI void proj_body(char* lds, const char* xb, const char* wb, int ldw2, const char* eb, int estride, int enl,
                  int ekind, int ets, f32x4 (&acc)[4][5], f32x4& eacc) {
    const int tid = threadIdx.x, l = tid & 63, w = __builtin_amdgcn_readfirstlane(tid >> 6);
    const int wm = w >> 1, wn = w & 1, r = l & 15, g = l >> 4;
    const unsigned lds0 = (unsigned)(uintptr_t)lds;
    unsigned xo[4], wo[5];
#pragma unroll
    for (int n = 0; n < 4; ++n) { const int row = 8 * (w + 4 * n) + (l >> 3); xo[n] = row * (DM * 2) + (((l & 7) ^ ((row >> 1) & 7)) << 4); }
#pragma unroll
    for (int n = 0; n < 5; ++n) { const int cid = 64 * (w + 4 * n) + l, k = cid / 20, c = cid - 20 * k; wo[n] = k * ldw2 + ((c ^ (((k >> 3) & 1) << 1)) << 4); }
    const unsigned eo = (w * enl + l) * 16;
    const unsigned long long emask = (1ull << enl) - 1;
    auto issue = [&](int s) {
        const char* xs = xb + s * 128; const char* ws = wb + (size_t)s * 64 * ldw2; const char* es = eb + s * estride;
        const unsigned d0 = lds0 + (s & 3) * P_SLOT + w * 1024, de = lds0 + (s & 3) * P_SLOT + 36864 + w * enl * 16;
        unsigned sv;
        asm volatile(
            "s_mov_b32 %[sv], m0\n\ts_mov_b32 m0, %[d0]\n\ts_nop 0\n\t"
            "global_load_lds_dwordx4 %[x0], %[xs]\n\ts_add_u32 m0, m0, 4096\n\ts_nop 0\n\t"
            "global_load_lds_dwordx4 %[x1], %[xs]\n\ts_add_u32 m0, m0, 4096\n\ts_nop 0\n\t"
            "global_load_lds_dwordx4 %[x2], %[xs]\n\ts_add_u32 m0, m0, 4096\n\ts_nop 0\n\t"
            "global_load_lds_dwordx4 %[x3], %[xs]\n\ts_add_u32 m0, m0, 4096\n\ts_nop 0\n\t"
            "global_load_lds_dwordx4 %[w0], %[ws]\n\ts_add_u32 m0, m0, 4096\n\ts_nop 0\n\t"
            "global_load_lds_dwordx4 %[w1], %[ws]\n\ts_add_u32 m0, m0, 4096\n\ts_nop 0\n\t"
            "global_load_lds_dwordx4 %[w2], %[ws]\n\ts_add_u32 m0, m0, 4096\n\ts_nop 0\n\t"
            "global_load_lds_dwordx4 %[w3], %[ws]\n\ts_add_u32 m0, m0, 4096\n\ts_nop 0\n\t"
            "global_load_lds_dwordx4 %[w4], %[ws]\n\ts_mov_b32 m0, %[de]\n\ts_mov_b64 exec, %[em]\n\t"
            "global_load_lds_dwordx4 %[eo], %[es]\n\ts_mov_b64 exec, -1\n\ts_mov_b32 m0, %[sv]"
            : [sv] "=&s"(sv)
            : [d0] "s"(d0), [de] "s"(de), [em] "s"(emask), [xs] "s"(xs), [ws] "s"(ws), [es] "s"(es),
              [x0] "v"(xo[0]), [x1] "v"(xo[1]), [x2] "v"(xo[2]), [x3] "v"(xo[3]),
              [w0] "v"(wo[0]), [w1] "v"(wo[1]), [w2] "v"(wo[2]), [w3] "v"(wo[3]), [w4] "v"(wo[4]), [eo] "v"(eo)
            : "memory", "scc");
    };
    issue(0); issue(1); issue(2); issue(3);
    int xr[4];
#pragma unroll
    for (int i = 0; i < 4; ++i) { const int row = 64 * wm + 16 * i + r; xr[i] = row * 128 + ((g ^ ((row >> 1) & 7)) << 4); }
    const int exr = (16 * ets + r) * 128 + ((g ^ (((16 * ets + r) >> 1) & 7)) << 4);
    int wr[5];
    const int q = r >> 2, p = r & 3;
#pragma unroll
    for (int j = 0; j < 5; ++j) {
        int colb;
        if (VT) colb = (80 * wn + 16 * j + 4 * p) * 2;
        else colb = (j < 4) ? ((4 * wn + j) * 20 + 4 * p) * 2 : ((4 * wn + p) * 20 + 16) * 2;
        colb ^= (g & 1) << 5;
        wr[j] = 16384 + (8 * g + q) * 320 + colb;
    }
    const int ewr = 36864 + (8 * g + q) * 40 + ((ekind & 1) ? 32 : 8 * p);
    const int egr = 36864 + g * 16;
    for (int s = 0; s < 10; ++s) {
        if (s == 0) asm volatile("s_waitcnt vmcnt(30)\n\ts_barrier" ::: "memory");
        else if (s <= 7) asm volatile("s_waitcnt vmcnt(20)\n\ts_barrier" ::: "memory");
        else if (s == 8) asm volatile("s_waitcnt vmcnt(10)\n\ts_barrier" ::: "memory");
        else asm volatile("s_waitcnt vmcnt(0)\n\ts_barrier" ::: "memory");
        if (s >= 1 && s <= 6) issue(s + 3);
        const LDSP char* sb = (const LDSP char*)lds + (s & 3) * P_SLOT;
#pragma unroll
        for (int ks = 0; ks < 2; ++ks) {
            bf16x8 xf[4], wf[5];
#pragma unroll
            for (int i = 0; i < 4; ++i) xf[i] = *(const LDSP bf16x8*)(sb + (xr[i] ^ (ks << 6)));
#pragma unroll
            for (int j = 0; j < 5; ++j) wf[j] = cat8(tr_read(sb + wr[j] + ks * 32 * 320), tr_read(sb + wr[j] + ks * 32 * 320 + 4 * 320));
            const bf16x8 ex = *(const LDSP bf16x8*)(sb + (exr ^ (ks << 6)));
            bf16x8 ew;
            if (ekind < 4) ew = cat8(tr_read(sb + ewr + ks * 32 * 40), tr_read(sb + ewr + ks * 32 * 40 + 4 * 40));
            else ew = *(const LDSP bf16x8*)(sb + egr + ks * 64);
#pragma unroll
            for (int i = 0; i < 4; ++i)
#pragma unroll
                for (int j = 0; j < 5; ++j) acc[i][j] = VT ? MFMA16(xf[i], wf[j], acc[i][j]) : MFMA16(wf[j], xf[i], acc[i][j]);
            eacc = VT ? MFMA16(ex, ew, eacc) : MFMA16(ew, ex, eacc);
        }
        asm volatile("s_waitcnt lgkmcnt(0)" ::: "memory");
    }
}

__global__ __launch_bounds__(256, 1) void k_proj(const char* __restrict__ x, const char* __restrict__ wq, const char* __restrict__ wk,
                                                 const char* __restrict__ wv, const char* __restrict__ wqkv, const char* __restrict__ gw,
                                                 char* __restrict__ ws) {
    __shared__ __attribute__((aligned(16))) char lds[4 * P_SLOT];
    const int b = blockIdx.x, xcd = b & 7, ii = b >> 3;
    const int mb = (xcd & 1) * 8 + (ii & 7), type = xcd >> 1, nq = ii >> 3, nb = type * 4 + nq;
    const int tid = threadIdx.x, l = tid & 63, w = __builtin_amdgcn_readfirstlane(tid >> 6);
    const int wm = w >> 1, wn = w & 1, r = l & 15, g = l >> 4;
    const char* xb = x + (size_t)mb * 128 * DM * 2;
    const char* wb = type == 0 ? wq + nq * 320 : wqkv + (type - 1) * 1280 + nq * 320;
    const int ldw2 = type == 0 ? 1280 : 3840;
    const int ekind = nb >= 12 ? 2 + ((nb - 12) >> 1) : (nb < 4 ? (nb >> 1) : (nb < 6 ? 4 : 5));
    const int ets = 4 * (nb & 1) + w;
    const char* eb = ekind < 2 ? wk : (ekind < 4 ? wv : gw);
    const int estride = ekind < 4 ? 2560 : 128, enl = ekind < 4 ? 40 : 2;
    f32x4 acc[4][5], eacc = {0.f, 0.f, 0.f, 0.f};
#pragma unroll
    for (int i = 0; i < 4; ++i)
#pragma unroll
        for (int j = 0; j < 5; ++j) acc[i][j] = eacc;
    const int chunk = mb * 2 + wm;
    char* kva = ws + WS_KV + 32 * KV_HEAD;
    const int etok = mb * 128 + 16 * ets;
    if (type == 3) {
        proj_body<true>(lds, xb, wb, ldw2, eb, estride, enl, ekind, ets, acc, eacc);
        char* base = ws + WS_KV + (size_t)nq * 8 * KV_HEAD + chunk * 5120 + 2560 + (g & 1) * 320 + (g >> 1) * 8;
#pragma unroll
        for (int j = 0; j < 5; ++j) {
            const int n = 80 * wn + 16 * j + r, hl = n / 20, d = n - 20 * hl;
#pragma unroll
            for (int i = 0; i < 4; ++i) {
                u32x2 v = {pk_bf16(acc[i][j][0], acc[i][j][1]), pk_bf16(acc[i][j][2], acc[i][j][3])};
                *(u32x2*)(base + (size_t)hl * KV_HEAD + i * 640 + d * 16) = v;
            }
        }
        const int d = (ekind & 1) * 16 + r;
        if (d < 20) {
            u32x2 v = {pk_bf16(eacc[0], eacc[1]), pk_bf16(eacc[2], eacc[3])};
            *(u32x2*)(kva + (etok >> 6) * 5120 + 2560 + ((etok >> 4) & 3) * 640 + (g & 1) * 320 + (g >> 1) * 8 + d * 16) = v;
        }
    } else {
        proj_body<false>(lds, xb, wb, ldw2, eb, estride, enl, ekind, ets, acc, eacc);
        const float sc = type == 2 ? 1.0f : 0.32259784f;
        const size_t hstride = type == 2 ? KV_HEAD : Q_HEAD;
        char* base = (type == 2 ? ws + WS_KV + chunk * 5120 : ws + WS_Q + (size_t)type * 32 * Q_HEAD + chunk * 2560) + (size_t)(nq * 8 + 4 * wn) * hstride;
#pragma unroll
        for (int i = 0; i < 4; ++i) {
#pragma unroll
            for (int j = 0; j < 5; ++j) {
                u32x2 v = {pk_bf16(acc[i][j][0] * sc, acc[i][j][1] * sc), pk_bf16(acc[i][j][2] * sc, acc[i][j][3] * sc)};
                if (j < 4) *(u32x2*)(base + j * hstride + (i >> 1) * 1024 + (g >> 1) * 512 + ((i & 1) * 16 + r) * 16 + (g & 1) * 8) = v;
                else *(u32x2*)(base + g * hstride + 2048 + (16 * i + r) * 8) = v;
            }
        }
        u32x2 v = {pk_bf16(eacc[0], eacc[1]), pk_bf16(eacc[2], eacc[3])};
        const int tok = etok + r;
        if (ekind == 0) *(u32x2*)(kva + (tok >> 6) * 5120 + ((tok >> 5) & 1) * 1024 + (g >> 1) * 512 + (tok & 31) * 16 + (g & 1) * 8) = v;
        else if (ekind == 1) { if (g == 0) *(u32x2*)(kva + (tok >> 6) * 5120 + 2048 + (tok & 63) * 8) = v; }
        else if (ekind == 4) { if (g == 0) ((float*)(ws + WS_G))[tok] = 1.0f / (1.0f + __expf(-eacc[0])); }
    }
}

#define STR_(x) #x
#define STR(x) STR_(x)
#define S0R 128
#define S1R 160
#define VAR 192
#define VBR 200
#define KAR 208
#define KBR 212
#define E_(X, i) "v_exp_f32 v[" STR(X) "+" #i "], v[" STR(X) "+" #i "]\n\t"
#define C_(X, d, a, b) "v_cvt_pk_bf16_f32 v[" STR(X) "+" #d "], v[" STR(X) "+" #a "], v[" STR(X) "+" #b "]\n\t"
#define PV_(O, V, vo, X, po) "v_mfma_f32_32x32x16_bf16 %[" #O "], v[" STR(V) "+" #vo ":" STR(V) "+" #vo "+3], v[" STR(X) "+" #po ":" STR(X) "+" #po "+3], %[" #O "]\n\t"
#define M1_(Y, KB) "v_mfma_f32_32x32x4_2b_bf16 v[" STR(Y) ":" STR(Y) "+31], " KB ", %[q2], %[negm]\n\t"
#define M2_(Y, o, KA, Q) "v_mfma_f32_32x32x16_bf16 v[" STR(Y) "+" #o ":" STR(Y) "+" #o "+15], " KA ", %[" #Q "], v[" STR(Y) "+" #o ":" STR(Y) "+" #o "+15]\n\t"
#define KA_ "v[" STR(KAR) ":" STR(KAR) "+3]"
#define KB_ "v[" STR(KBR) ":" STR(KBR) "+1]"
#define RDKA(off) "ds_read_b128 " KA_ ", %[ak1] offset:" STR(off) "\n\t"
#define RDKB(off) "ds_read_b64 " KB_ ", %[ak2] offset:" STR(off) "\n\t"
#define RDV(V, off) "s_mov_b64 exec, %[vmask]\n\tds_read_b128 v[" STR(V) ":" STR(V) "+3], %[av] offset:" STR(off) "\n\t" \
                    "ds_read_b128 v[" STR(V) "+4:" STR(V) "+7], %[av] offset:" STR(off) "+640\n\ts_mov_b64 exec, -1\n\t"
#define TILE(X, Y, VC, VP, KAOFF, KBOFF, VOFF, PRE) \
    PRE E_(X, 0) E_(X, 1) \
    PV_(ob, VP, 4, Y, 24) \
    E_(X, 2) E_(X, 3) C_(X, 0, 0, 1) E_(X, 4) E_(X, 5) C_(X, 1, 2, 3) \
    "s_waitcnt lgkmcnt(0)\n\t" M1_(Y, KB_) RDKB(KBOFF) \
    E_(X, 6) E_(X, 7) C_(X, 2, 4, 5) E_(X, 8) C_(X, 3, 6, 7) E_(X, 9) E_(X, 10) E_(X, 11) C_(X, 8, 8, 9) \
    PV_(oa, VC, 0, X, 0) RDV(VP, VOFF) \
    E_(X, 12) C_(X, 9, 10, 11) E_(X, 13) E_(X, 14) E_(X, 15) C_(X, 10, 12, 13) E_(X, 16) C_(X, 11, 14, 15) E_(X, 17) \
    M2_(Y, 0, KA_, q1a) \
    E_(X, 18) E_(X, 19) C_(X, 16, 16, 17) E_(X, 20) E_(X, 21) C_(X, 17, 18, 19) \
    PV_(oa, VC, 4, X, 8) \
    E_(X, 22) E_(X, 23) C_(X, 18, 20, 21) E_(X, 24) C_(X, 19, 22, 23) E_(X, 25) \
    M2_(Y, 16, KA_, q1b) RDKA(KAOFF) \
    E_(X, 26) E_(X, 27) C_(X, 24, 24, 25) E_(X, 28) E_(X, 29) C_(X, 25, 26, 27) \
    PV_(ob, VC, 0, X, 16) \
    E_(X, 30) E_(X, 31) C_(X, 26, 28, 29) C_(X, 27, 30, 31)
#define CLOB8(a) "v" STR(a##0), "v" STR(a##1), "v" STR(a##2), "v" STR(a##3), "v" STR(a##4), "v" STR(a##5), "v" STR(a##6), "v" STR(a##7), "v" STR(a##8), "v" STR(a##9)

DI void attn_fallback(const char* lds, bf16x8 q1, s16x4 q2, f32x16& o) {
    const int l = threadIdx.x & 63, r32 = l & 31, h = l >> 5;
    const s16x4 z4 = {0, 0, 0, 0};
    const bf16x8 q2f = cat8(h == 0 ? q2 : z4, z4);
    const bf16x8 ones = {0x3f80, 0x3f80, 0x3f80, 0x3f80, 0x3f80, 0x3f80, 0x3f80, 0x3f80};
    float m = -INFINITY;
#pragma unroll
    for (int i = 0; i < 16; ++i) o[i] = 0.f;
    for (int t = 0; t < 64; ++t) {
        const LDSP char* cb = (const LDSP char*)lds + (t >> 1) * 5120;
        const bf16x8 k1 = *(const LDSP bf16x8*)(cb + (t & 1) * 1024 + l * 16);
        const s16x4 k2 = *(const LDSP s16x4*)(cb + 2048 + (t & 1) * 256 + r32 * 8);
        f32x16 s;
#pragma unroll
        for (int i = 0; i < 16; ++i) s[i] = 0.f;
        s = MFMA32(k1, q1, s);
        s = MFMA32(cat8(h == 0 ? k2 : z4, z4), q2f, s);
        float tm = s[0];
#pragma unroll
        for (int i = 1; i < 16; ++i) tm = fmaxf(tm, s[i]);
        tm = fmaxf(tm, __shfl_xor(tm, 32));
        const float mn = fmaxf(m, tm), alpha = __builtin_amdgcn_exp2f(m - mn);
        m = mn;
        u32x4 p0, p1;
#pragma unroll
        for (int i = 0; i < 4; ++i) {
            p0[i] = pk_bf16(__builtin_amdgcn_exp2f(s[2 * i] - m), __builtin_amdgcn_exp2f(s[2 * i + 1] - m));
            p1[i] = pk_bf16(__builtin_amdgcn_exp2f(s[8 + 2 * i] - m), __builtin_amdgcn_exp2f(s[9 + 2 * i] - m));
        }
#pragma unroll
        for (int i = 0; i < 16; ++i) o[i] *= alpha;
        const int dv = r32 < 20 ? r32 : 0;
        bf16x8 v0 = *(const LDSP bf16x8*)(cb + 2560 + (t & 1) * 1280 + h * 320 + dv * 16);
        bf16x8 v1 = *(const LDSP bf16x8*)(cb + 2560 + (t & 1) * 1280 + 640 + h * 320 + dv * 16);
        if (r32 >= 20) { v0 = ones; v1 = ones; }
        o = MFMA32(v0, __builtin_bit_cast(bf16x8, p0), o);
        o = MFMA32(v1, __builtin_bit_cast(bf16x8, p1), o);
    }
}

DI bool attn_ok(const f32x16& o) {
    float a = 0.f;
#pragma unroll
    for (int i = 0; i < 12; ++i) a = fmaxf(a, fabsf(o[i]));
    float s = 0.f;
#pragma unroll
    for (int i = 0; i < 12; ++i) s += o[i] * 0.f;
    return o[12] < 0x1p80f && a < 0x1p126f && s == 0.f;
}

__global__ __launch_bounds__(512, 2) void k_attn(const char* __restrict__ woa, const char* __restrict__ wob, char* __restrict__ ws) {
    __shared__ __attribute__((aligned(16))) char lds[163840];
    const int b = blockIdx.x, xcd = b & 7, ii = b >> 3;
    const int hq = ii >> 2, head = (hq & 1) * 32 + xcd * 4 + (hq >> 1), qb = ii & 3;
    const int tid = threadIdx.x, l = tid & 63, w = __builtin_amdgcn_readfirstlane(tid >> 6), r32 = l & 31, h = l >> 5;
    const int qc = qb * 8 + w;
    const char* qp = ws + WS_Q + (size_t)head * Q_HEAD + qc * 2560;
    const char* kvp = ws + WS_KV + (size_t)(head < 32 ? 32 : head - 32) * KV_HEAD;
    const unsigned lds0 = (unsigned)(uintptr_t)lds;
    bf16x8 q1a, q1b, k1f; s16x4 q2, k2f;
    {
        unsigned sv; unsigned dv = w * 1024 + l * 16; const unsigned d0 = lds0 + w * 1024;
#define DMA1 "global_load_lds_dwordx4 %[dv], %[kvp]\n\ts_add_u32 m0, m0, 8192\n\tv_add_u32 %[dv], 0x2000, %[dv]\n\t"
#define DMA5 DMA1 DMA1 DMA1 DMA1 DMA1
        asm volatile(
            "global_load_dwordx4 %[q1a], %[o16], %[qp]\n\t"
            "global_load_dwordx4 %[q1b], %[o16], %[qp] offset:1024\n\t"
            "global_load_dwordx2 %[q2], %[o8], %[qp] offset:2048\n\t"
            "global_load_dwordx4 %[k1f], %[o16], %[kvp]\n\t"
            "global_load_dwordx2 %[k2f], %[o8r], %[kvp] offset:2048\n\t"
            "s_mov_b32 %[sv], m0\n\ts_mov_b32 m0, %[d0]\n\ts_nop 0\n\t"
            DMA5 DMA5 DMA5 DMA5
            "s_mov_b32 m0, %[sv]\n\ts_waitcnt vmcnt(20)"
            : [q1a] "=&v"(q1a), [q1b] "=&v"(q1b), [q2] "=&v"(q2), [k1f] "=&v"(k1f), [k2f] "=&v"(k2f), [sv] "=&s"(sv), [dv] "+v"(dv)
            : [qp] "s"(qp), [kvp] "s"(kvp), [d0] "s"(d0), [o16] "v"(l * 16), [o8] "v"(l * 8), [o8r] "v"(r32 * 8)
            : "memory", "scc");
    }
    f32x32 negm;
    {
        f32x32 z;
#pragma unroll
        for (int i = 0; i < 32; ++i) z[i] = 0.f;
        const f32x32 s2 = __builtin_amdgcn_mfma_f32_32x32x4bf16_1k(k2f, q2, z, 0, 0, 0);
        f32x16 sa = __builtin_shufflevector(s2, s2, 0, 1, 2, 3, 4, 5, 6, 7, 8, 9, 10, 11, 12, 13, 14, 15);
        f32x16 sb = __builtin_shufflevector(s2, s2, 16, 17, 18, 19, 20, 21, 22, 23, 24, 25, 26, 27, 28, 29, 30, 31);
        sa = MFMA32(k1f, q1a, sa); sb = MFMA32(k1f, q1b, sb);
        float ma = sa[0], mb = sb[0];
#pragma unroll
        for (int i = 1; i < 16; ++i) { ma = fmaxf(ma, sa[i]); mb = fmaxf(mb, sb[i]); }
        ma = fmaxf(ma, __shfl_xor(ma, 32)); mb = fmaxf(mb, __shfl_xor(mb, 32));
#pragma unroll
        for (int i = 0; i < 16; ++i) { negm[i] = -ma; negm[16 + i] = -mb; }
    }
    const int gw = b * 8 + w, fid = gw < 1600 ? gw : 1599, wks = fid / 40, wns = fid - 40 * wks;
    const int r = l & 15, g = l >> 4;
    const int wcol = 80 * (wns / 5) + 20 * (r >> 2) + 4 * (wns % 5) + (r & 3);
    const int wrow = wks < 32 ? ((2 * wks + (g >> 1)) & 31) * 20 + 4 * (g & 1) : ((8 * (wks - 32) + 2 * g) & 31) * 20 + 16;
    const char* wsrc = (wks < 16 || (wks >= 32 && wks < 36)) ? woa : wob;
    const unsigned wv0 = wrow * 1280 + wcol * 2, wv1 = wv0 + (wks < 32 ? 8 : 20) * 1280;
    const char* gp = ws + WS_G + qc * 256;
    f32x16 oa, ob;
#pragma unroll
    for (int i = 0; i < 16; ++i) { oa[i] = 0.f; ob[i] = 0.f; }
    unsigned ak1 = lds0 + l * 16, ak2 = lds0 + 2048 + r32 * 8, av = lds0 + 2560 + h * 320 + r32 * 16;
    unsigned e0, e1, e2, e3, e4, e5, e6, e7, cnt, inc; float ga, gb;
    asm volatile(
        "global_load_ushort %[e0], %[wv0], %[wsrc]\n\t"
        "global_load_ushort %[e1], %[wv0], %[wsrc] offset:1280\n\t"
        "global_load_ushort %[e2], %[wv0], %[wsrc] offset:2560\n\t"
        "global_load_ushort %[e3], %[wv0], %[wsrc] offset:3840\n\t"
        "global_load_ushort %[e4], %[wv1], %[wsrc]\n\t"
        "global_load_ushort %[e5], %[wv1], %[wsrc] offset:1280\n\t"
        "global_load_ushort %[e6], %[wv1], %[wsrc] offset:2560\n\t"
        "global_load_ushort %[e7], %[wv1], %[wsrc] offset:3840\n\t"
        "global_load_dword %[ga], %[go], %[gp]\n\t"
        "global_load_dword %[gb], %[go], %[gp] offset:128\n\t"
        M1_(S0R, "%[k2f]")
        ".irp n,0,1,2,3,4,5,6,7,8,9,10,11,12,13,14,15\n\tv_mov_b32 v[" STR(VAR) "+\\n], 0x3f803f80\n\t.endr\n\t"
        ".irp n,24,25,26,27\n\tv_mov_b32 v[" STR(S1R) "+\\n], 0\n\t.endr\n\t"
        M2_(S0R, 0, "%[k1f]", q1a) M2_(S0R, 16, "%[k1f]", q1b)
        "s_mov_b32 %[cnt], 0\n\ts_mov_b32 %[inc], 20480\n\t"
        "s_waitcnt vmcnt(26)\n\ts_barrier\n\t"
        RDKB(256) RDKA(1024) RDV(VAR, 0)
        "s_nop 7\n\t"
        "1:\n\t"
        TILE(S0R, S1R, VAR, VBR, 5120, 5120, 1280, "")
        TILE(S1R, S0R, VBR, VAR, 5120 + 1024, 5120 + 256, 5120, "")
        TILE(S0R, S1R, VAR, VBR, 10240, 10240, 5120 + 1280, "")
        TILE(S1R, S0R, VBR, VAR, 10240 + 1024, 10240 + 256, 10240, "")
        TILE(S0R, S1R, VAR, VBR, 15360, 15360, 10240 + 1280, "")
        TILE(S1R, S0R, VBR, VAR, 15360 + 1024, 15360 + 256, 15360, "s_cmp_eq_u32 %[cnt], 7\n\ts_cselect_b32 %[inc], -143360, %[inc]\n\t")
        TILE(S0R, S1R, VAR, VBR, 0, 0, 15360 + 1280, "v_add_u32 %[ak1], %[inc], %[ak1]\n\tv_add_u32 %[ak2], %[inc], %[ak2]\n\t")
        TILE(S1R, S0R, VBR, VAR, 1024, 256, 0, "v_add_u32 %[av], %[inc], %[av]\n\t")
        "s_add_u32 %[cnt], %[cnt], 1\n\t"
        "s_cmp_eq_u32 %[cnt], 1\n\ts_cbranch_scc1 2f\n\t"
        "s_cmp_eq_u32 %[cnt], 3\n\ts_cbranch_scc1 3f\n\t"
        "s_cmp_lt_u32 %[cnt], 8\n\ts_cbranch_scc1 1b\n\t"
        "s_branch 4f\n\t"
        "2:\n\ts_waitcnt vmcnt(21)\n\ts_barrier\n\ts_branch 1b\n\t"
        "3:\n\ts_waitcnt vmcnt(0)\n\ts_barrier\n\ts_branch 1b\n\t"
        "4:\n\ts_nop 1\n\t"
        PV_(ob, VBR, 4, S1R, 24)
        "s_waitcnt lgkmcnt(0)\n\ts_nop 15\n\ts_nop 7"
        : [oa] "+v"(oa), [ob] "+v"(ob), [ak1] "+v"(ak1), [ak2] "+v"(ak2), [av] "+v"(av),
          [e0] "=&v"(e0), [e1] "=&v"(e1), [e2] "=&v"(e2), [e3] "=&v"(e3), [e4] "=&v"(e4), [e5] "=&v"(e5), [e6] "=&v"(e6), [e7] "=&v"(e7),
          [ga] "=&v"(ga), [gb] "=&v"(gb), [cnt] "=&s"(cnt), [inc] "=&s"(inc)
        : [negm] "v"(negm), [q1a] "v"(q1a), [q1b] "v"(q1b), [q2] "v"(q2), [k1f] "v"(k1f), [k2f] "v"(k2f),
          [vmask] "s"(0x000fffff000fffffull), [wsrc] "s"(wsrc), [wv0] "v"(wv0), [wv1] "v"(wv1), [gp] "s"(gp), [go] "v"(r32 * 4)
        : "memory", "scc", "v128", "v129", CLOB8(13), CLOB8(14), CLOB8(15), CLOB8(16), CLOB8(17), CLOB8(18), CLOB8(19), CLOB8(20),
          "v210", "v211", "v212", "v213");
    if (gw < 1600) {
        u32x4 v = {e0 | (e1 << 16), e2 | (e3 << 16), e4 | (e5 << 16), e6 | (e7 << 16)};
        *(u32x4*)(ws + WS_WO + (size_t)fid * 1024 + l * 16) = v;
    }
    if (__builtin_expect(!__all(attn_ok(oa) && attn_ok(ob)), 0)) {
        s16x4 q2a, q2b;
#pragma unroll
        for (int i = 0; i < 4; ++i) { q2a[i] = (short)__shfl((int)q2[i], r32); q2b[i] = (short)__shfl((int)q2[i], r32 + 32); }
        attn_fallback(lds, q1a, q2a, oa);
        attn_fallback(lds, q1b, q2b, ob);
    }
    const float fa = (head < 32 ? ga : 1.0f - ga) / oa[12], fb = (head < 32 ? gb : 1.0f - gb) / ob[12];
    char* o1 = ws + WS_O1 + (size_t)head * 65536 + qc * 2048 + l * 16;
    char* o2 = ws + WS_O2 + (size_t)(head >> 1) * 32768 + (qc * 64 + r32) * 16 + (head & 1) * 8;
    u32x4 va = {pk_bf16(oa[0] * fa, oa[1] * fa), pk_bf16(oa[2] * fa, oa[3] * fa), pk_bf16(oa[4] * fa, oa[5] * fa), pk_bf16(oa[6] * fa, oa[7] * fa)};
    u32x4 vb = {pk_bf16(ob[0] * fb, ob[1] * fb), pk_bf16(ob[2] * fb, ob[3] * fb), pk_bf16(ob[4] * fb, ob[5] * fb), pk_bf16(ob[6] * fb, ob[7] * fb)};
    *(u32x4*)o1 = va;
    *(u32x4*)(o1 + 1024) = vb;
    if (h == 0) {
        u32x2 ta = {pk_bf16(oa[8] * fa, oa[9] * fa), pk_bf16(oa[10] * fa, oa[11] * fa)};
        u32x2 tb = {pk_bf16(ob[8] * fb, ob[9] * fb), pk_bf16(ob[10] * fb, ob[11] * fb)};
        *(u32x2*)o2 = ta;
        *(u32x2*)(o2 + 512) = tb;
    }
}

__global__ __launch_bounds__(256, 1) void k_out(const char* __restrict__ ws, char* __restrict__ out) {
    __shared__ __attribute__((aligned(16))) char lds[4 * 3 * 5 * 1024];
    const int b = blockIdx.x, xcd = b & 7, ii = b >> 3;
    const int mb = (xcd >> 1) * 8 + (ii & 7), nb = (xcd & 1) * 4 + (ii >> 3);
    const int tid = threadIdx.x, l = tid & 63, w = __builtin_amdgcn_readfirstlane(tid >> 6), r = l & 15, g = l >> 4;
    f32x4 acc[4][5];
#pragma unroll
    for (int i = 0; i < 4; ++i)
#pragma unroll
        for (int j = 0; j < 5; ++j) acc[i][j] = f32x4{0.f, 0.f, 0.f, 0.f};
    const char* wp = ws + WS_WO + nb * 5 * 1024 + l * 16;
    int ao1[4], ao2[4];
#pragma unroll
    for (int i = 0; i < 4; ++i) {
        const int ts = (i + w) & 3;
        ao1[i] = (g >> 1) * 65536 + mb * 2048 + (ts >> 1) * 1024 + (g & 1) * 512 + ((ts & 1) * 16 + r) * 16;
        ao2[i] = g * 32768 + (mb * 64 + ts * 16 + r) * 16;
    }
#pragma unroll
    for (int s = 0; s < 10; ++s) {
        const int ks = s < 8 ? 8 * w + s : 32 + 2 * w + (s - 8);
        bf16x8 af[4], wf[5];
#pragma unroll
        for (int i = 0; i < 4; ++i)
            af[i] = s < 8 ? *(const bf16x8*)(ws + WS_O1 + (size_t)ks * 131072 + ao1[i]) : *(const bf16x8*)(ws + WS_O2 + (size_t)(ks - 32) * 131072 + ao2[i]);
#pragma unroll
        for (int j = 0; j < 5; ++j) wf[j] = *(const bf16x8*)(wp + (size_t)ks * 40960 + j * 1024);
#pragma unroll
        for (int i = 0; i < 4; ++i)
#pragma unroll
            for (int j = 0; j < 5; ++j) acc[i][j] = MFMA16(wf[j], af[i], acc[i][j]);
    }
#pragma unroll
    for (int i = 1; i < 4; ++i)
#pragma unroll
        for (int j = 0; j < 5; ++j) *(f32x4*)(lds + ((((i + w) & 3) * 3 + i - 1) * 5 + j) * 1024 + l * 16) = acc[i][j];
    __syncthreads();
#pragma unroll
    for (int i = 0; i < 3; ++i)
#pragma unroll
        for (int j = 0; j < 5; ++j) acc[0][j] += *(const f32x4*)(lds + ((w * 3 + i) * 5 + j) * 1024 + l * 16);
    char* op = out + (size_t)(mb * 64 + w * 16 + r) * 1280 + nb * 160 + g * 40;
#pragma unroll
    for (int j = 0; j < 5; ++j) {
        u32x2 v = {pk_bf16(acc[0][j][0], acc[0][j][1]), pk_bf16(acc[0][j][2], acc[0][j][3])};
        *(u32x2*)(op + j * 8) = v;
    }
}

extern "C" void kernel_launch(void* const* d_in, const int* in_sizes, int n_in, void* d_out, int out_size, void* d_ws, size_t ws_size,
                              hipStream_t stream) {
    const char* x = (const char*)d_in[0];
    const char* wq = (const char*)d_in[1];
    const char* wk = (const char*)d_in[2];
    const char* wv = (const char*)d_in[3];
    const char* woa = (const char*)d_in[4];
    const char* wqkv = (const char*)d_in[6];
    const char* wob = (const char*)d_in[7];
    const char* gw = (const char*)d_in[8];
    char* ws = (char*)d_ws;
    k_proj<<<256, 256, 0, stream>>>(x, wq, wk, wv, wqkv, gw, ws);
    k_attn<<<256, 512, 0, stream>>>(woa, wob, ws);
    k_out<<<256, 256, 0, stream>>>(ws, (char*)d_out);
}
```

```cpp
#include <hip/hip_runtime.h>
#include <hip/hip_bf16.h>
#include <stdint.h>

typedef __attribute__((ext_vector_type(8))) short bf16x8;
typedef __attribute__((ext_vector_type(4))) short s16x4;
typedef __attribute__((ext_vector_type(4))) float f32x4;
typedef __attribute__((ext_vector_type(16))) float f32x16;
typedef __attribute__((ext_vector_type(32))) float f32x32;
typedef __attribute__((ext_vector_type(2))) unsigned u32x2;
typedef __attribute__((ext_vector_type(4))) unsigned u32x4;
typedef __attribute__((ext_vector_type(2))) __bf16 bf16x2_t;
typedef __attribute__((ext_vector_type(2))) float f32x2;
#define LDSP __attribute__((address_space(3)))
#define DI __device__ __forceinline__

#define STAMP_PARAMS
#define STAMP_ARGS
#define STAMP_DECL
#define STAMP(slot)
#define STAMP_FLUSH(wsp, kid)
constexpr int SEQ = 2048, DM = 640;
constexpr size_t Q_HEAD = 32 * 2560;
constexpr size_t KV_HEAD = 32 * 5120;
constexpr size_t WS_Q = 0;
constexpr size_t WS_KV = WS_Q + 64 * Q_HEAD;
constexpr size_t WS_G = WS_KV + 33 * KV_HEAD;
constexpr size_t WS_O1 = WS_G + 2048 * 4;
constexpr size_t WS_O2 = WS_O1 + 64 * 65536;
constexpr size_t WS_WO = WS_O2 + 32 * 2048 * 16;

DI unsigned pk_bf16(float a, float b) {
    f32x2 v = {a, b};
    return __builtin_bit_cast(unsigned, __builtin_convertvector(v, bf16x2_t));
}
DI s16x4 tr_read(const LDSP char* p) {
    typedef short v4i16_t __attribute__((ext_vector_type(4)));
    return __builtin_amdgcn_ds_read_tr16_b64_v4i16((LDSP v4i16_t*)p);
}
DI bf16x8 cat8(s16x4 lo, s16x4 hi) { return __builtin_shufflevector(lo, hi, 0, 1, 2, 3, 4, 5, 6, 7); }
#define MFMA16(a, b, c) __builtin_amdgcn_mfma_f32_16x16x32_bf16((a), (b), (c), 0, 0, 0)
#define MFMA32(a, b, c) __builtin_amdgcn_mfma_f32_32x32x16_bf16((a), (b), (c), 0, 0, 0)

constexpr int P_SLOT = 40960;

DI void proj_loader(unsigned lds0, const char* xb, const char* wb, int ldw2, const char* eb, int ebytes, int rot) {
    const int l = threadIdx.x & 63, w = __builtin_amdgcn_readfirstlane(threadIdx.x >> 6) - 4;
    unsigned xo[2], wo[3];
#pragma unroll
    for (int n = 0; n < 2; ++n) { const int row = 8 * (w + 8 * n) + (l >> 3); xo[n] = row * (DM * 2) + (((l & 7) ^ ((row >> 1) & 7)) << 4); }
#pragma unroll
    for (int n = 0; n < 3; ++n) { const int cid = 64 * (w + 8 * n) + l, k = (cid * 3277) >> 16, c = cid - 20 * k; wo[n] = k * ldw2 + ((c ^ (((k >> 3) & 1) << 1)) << 4); }
    const char* p5 = wb; int st5 = 64 * ldw2; unsigned long long m5 = ~0ull;
    if (w >= 4) {
        const int nbytes = ebytes - (w - 4) * 1024;
        const int nl = nbytes >= 1024 ? 64 : (nbytes > 0 ? nbytes >> 4 : 1);
        m5 = nl == 64 ? ~0ull : (1ull << nl) - 1;
        p5 = nbytes > 0 ? eb + (w - 4) * 1024 : eb; st5 = ebytes; wo[2] = l * 16;
    }
    const unsigned dw = __builtin_amdgcn_readfirstlane(lds0 + w * 1024);
    int st = rot;
#pragma unroll 1
    for (int k = -2; k < 10; ++k) {
        if (k >= 0) {
            if (k < 9) asm volatile("s_waitcnt vmcnt(5)\n\ts_barrier" ::: "memory");
            else asm volatile("s_waitcnt vmcnt(0)\n\ts_barrier" ::: "memory");
        }
        if (k < 8) {
            const unsigned d0 = dw + ((k + 2) & 3) * P_SLOT;
            const char* xs = xb + st * 128; const char* ws = wb + st * 64 * ldw2; const char* es = p5 + st * st5;
            unsigned sv;
            asm volatile(
                "s_mov_b32 %[sv], m0\n\ts_mov_b32 m0, %[d0]\n\ts_nop 0\n\t"
                "global_load_lds_dwordx4 %[x0], %[xs]\n\ts_add_u32 m0, m0, 8192\n\ts_nop 0\n\t"
                "global_load_lds_dwordx4 %[x1], %[xs]\n\ts_add_u32 m0, m0, 8192\n\ts_nop 0\n\t"
                "global_load_lds_dwordx4 %[w0], %[ws]\n\ts_add_u32 m0, m0, 8192\n\ts_nop 0\n\t"
                "global_load_lds_dwordx4 %[w1], %[ws]\n\ts_add_u32 m0, m0, 8192\n\ts_mov_b64 exec, %[m5]\n\t"
                "global_load_lds_dwordx4 %[w2], %[es]\n\ts_mov_b64 exec, -1\n\ts_mov_b32 m0, %[sv]"
                : [sv] "=&s"(sv)
                : [d0] "s"(d0), [m5] "s"(m5), [xs] "s"(xs), [ws] "s"(ws), [es] "s"(es),
                  [x0] "v"(xo[0]), [x1] "v"(xo[1]), [w0] "v"(wo[0]), [w1] "v"(wo[1]), [w2] "v"(wo[2])
                : "memory", "scc");
            st = st == 9 ? 0 : st + 1;
        }
    }
}

#define STR_(x) #x
#define STR(x) STR_(x)
#define PR4(b) "v[" STR(b) ":" STR(b) "+3]"
#define PM_F(j, i, X) "v_mfma_f32_16x16x32_bf16 " PR4(20 + 16 * j + 4 * i) ", " PR4(136 + 4 * j) ", " PR4(X + 4 * i) ", " PR4(20 + 16 * j + 4 * i) "\n\t"
#define PM_T(j, i, X) "v_mfma_f32_16x16x32_bf16 " PR4(20 + 16 * j + 4 * i) ", " PR4(X + 4 * i) ", " PR4(136 + 4 * j) ", " PR4(20 + 16 * j + 4 * i) "\n\t"
#define PE_F "s_waitcnt lgkmcnt(2)\n\tv_mfma_f32_16x16x32_bf16 v[100:103], v[160:163], v[156:159], v[100:103]\n\t"
#define PE_T "s_waitcnt lgkmcnt(2)\n\tv_mfma_f32_16x16x32_bf16 v[100:103], v[156:159], v[160:163], v[100:103]\n\t"
#define PTL(j, sl, ks) "ds_read_b64_tr_b16 v[136+4*" #j ":136+4*" #j "+1], %[aw" #j "] offset:" #sl "*40960+" #ks "*10240\n\t"
#define PTH(j, sl, ks) "ds_read_b64_tr_b16 v[136+4*" #j "+2:136+4*" #j "+3], %[aw" #j "] offset:" #sl "*40960+" #ks "*10240+1280\n\t"
#define PBX(X, i, sl, ks) "ds_read_b128 " PR4(X + 4 * i) ", %[ax" #ks "] offset:" #sl "*40960+" #i "*2048\n\t"
#define PX1_0(sl, ks) ""
#define PX2_0(sl, ks) ""
#define PX3_0(sl, ks) ""
#define PX1_1(sl, ks) "ds_read_b128 v[156:159], %[ae" #ks "] offset:" #sl "*40960\n\t"
#define PX2_1(sl, ks) "ds_read_b64_tr_b16 v[160:161], %[aew] offset:" #sl "*40960+" #ks "*1280\n\t"
#define PX3_1(sl, ks) "ds_read_b64_tr_b16 v[162:163], %[aew] offset:" #sl "*40960+" #ks "*1280+160\n\t"
#define PX1_2(sl, ks) PX1_1(sl, ks)
#define PX2_2(sl, ks) "ds_read_b128 v[160:163], %[aew] offset:" #sl "*40960+" #ks "*64\n\t"
#define PX3_2(sl, ks) ""
#define PTG(a) "v_add_u32 %[" #a "], %[tg], %[" #a "]\n\t"
#define PSTEP(M, EK, PE, WB, X, Y, cs, ck, ns, nk, BAR, F1, F2, F3) \
    "s_waitcnt lgkmcnt(4)\n\t" \
    M(0, 0, X) PTL(4, cs, ck) M(0, 1, X) PTH(4, cs, ck) M(0, 2, X) BAR PBX(Y, 0, ns, nk) M(0, 3, X) PBX(Y, 1, ns, nk) \
    M(1, 0, X) PTL(0, ns, nk) M(1, 1, X) PTH(0, ns, nk) M(1, 2, X) PBX(Y, 2, ns, nk) M(1, 3, X) PX1_##EK(cs, ck) \
    "s_waitcnt lgkmcnt(" #WB ")\n\t" \
    M(2, 0, X) PTL(1, ns, nk) M(2, 1, X) PTH(1, ns, nk) M(2, 2, X) PBX(Y, 3, ns, nk) M(2, 3, X) PX2_##EK(cs, ck) \
    M(3, 0, X) PTL(2, ns, nk) M(3, 1, X) PTH(2, ns, nk) M(3, 2, X) PX3_##EK(cs, ck) M(3, 3, X) F1 \
    M(4, 0, X) PTL(3, ns, nk) M(4, 1, X) PTH(3, ns, nk) M(4, 2, X) F2 M(4, 3, X) F3 PE
#define CL10(a) "v" #a "0", "v" #a "1", "v" #a "2", "v" #a "3", "v" #a "4", "v" #a "5", "v" #a "6", "v" #a "7", "v" #a "8", "v" #a "9"
#define PLOOP(M, EK, PE, WB) \
    asm volatile( \
        "s_mov_b32 %[cnt], 0\n\ts_mov_b32 %[tg], 81920\n\t" \
        "s_barrier\n\t" \
        PBX(104, 0, 0, 0) PBX(104, 1, 0, 0) PTL(0, 0, 0) PTH(0, 0, 0) PBX(104, 2, 0, 0) PTL(1, 0, 0) PTH(1, 0, 0) PBX(104, 3, 0, 0) \
        PTL(2, 0, 0) PTH(2, 0, 0) PTL(3, 0, 0) PTH(3, 0, 0) \
        "1:\n\t" \
        PSTEP(M, EK, PE, WB, 104, 120, 0, 0, 0, 1, "", "", "", "") \
        PSTEP(M, EK, PE, WB, 120, 104, 0, 1, 1, 0, "s_barrier\n\t", PTG(ax0), "s_add_u32 %[cnt], %[cnt], 1\n\t", "") \
        PSTEP(M, EK, PE, WB, 104, 120, 1, 0, 1, 1, "", PTG(aw0) PTG(ae0), PTG(aw1) PTG(ax1), PTG(aw2)) \
        PSTEP(M, EK, PE, WB, 120, 104, 1, 1, 0, 0, "s_cmp_eq_u32 %[cnt], 5\n\ts_cbranch_scc1 2f\n\ts_barrier\n\t2:\n\t", PTG(aw3) PTG(ae1), PTG(aw4), PTG(aew)) \
        "s_sub_u32 %[tg], 0, %[tg]\n\ts_cmp_lg_u32 %[cnt], 5\n\ts_cbranch_scc1 1b\n\t" \
        "s_waitcnt lgkmcnt(0)\n\ts_nop 7" \
        : "+{v[20:35]}"(acc[0]), "+{v[36:51]}"(acc[1]), "+{v[52:67]}"(acc[2]), "+{v[68:83]}"(acc[3]), "+{v[84:99]}"(acc[4]), "+{v[100:103]}"(eacc), \
          [ax0] "+v"(ax0), [ax1] "+v"(ax1), [aw0] "+v"(aw[0]), [aw1] "+v"(aw[1]), [aw2] "+v"(aw[2]), [aw3] "+v"(aw[3]), [aw4] "+v"(aw[4]), \
          [ae0] "+v"(ae0), [ae1] "+v"(ae1), [aew] "+v"(aew), [cnt] "=&s"(cnt), [tg] "=&s"(tg) \
        : \
        : "memory", "scc", "v104", "v105", "v106", "v107", "v108", "v109", CL10(11), CL10(12), CL10(13), CL10(14), CL10(15), "v160", "v161", "v162", "v163")

template <bool VT, int EK>
DI void proj_mma(unsigned lds0, int ehi, int ets, f32x16 (&acc)[5], f32x4& eacc) {
    const int tid = threadIdx.x, l = tid & 63, w = __builtin_amdgcn_readfirstlane(tid >> 6);
    const int wm = w >> 1, wn = w & 1, r = l & 15, g = l >> 4;
    const int xsw = (g ^ ((r >> 1) & 7)) << 4;
    unsigned ax0 = lds0 + (64 * wm + r) * 128 + xsw, ax1 = lds0 + (64 * wm + r) * 128 + (xsw ^ 64);
    unsigned ae0 = lds0 + (16 * ets + r) * 128 + xsw, ae1 = lds0 + (16 * ets + r) * 128 + (xsw ^ 64);
    unsigned aw[5];
    const int q = r >> 2, p = r & 3;
#pragma unroll
    for (int j = 0; j < 5; ++j) {
        int col;
        if (j == 4) col = (4 * wn + p) * 20 + 16;
        else if (VT) col = (4 * wn + j) * 20 + 4 * p;
        else col = (4 * wn + (j & 2) + (p >> 1)) * 20 + 8 * (p & 1) + 4 * (j & 1);
        aw[j] = lds0 + 16384 + (8 * g + q) * 320 + ((col * 2) ^ ((g & 1) << 5));
    }
    unsigned aew = lds0 + (EK == 1 ? 36864 + (8 * g + q) * 40 + (ehi ? 32 : 8 * p) : 36864 + g * 16);
    unsigned cnt, tg;
    if constexpr (VT) PLOOP(PM_T, 1, PE_T, 6);
    else if constexpr (EK == 1) PLOOP(PM_F, 1, PE_F, 6);
    else if constexpr (EK == 2) PLOOP(PM_F, 2, PE_F, 6);
    else PLOOP(PM_F, 0, "", 5);
}
#define ACC(i, j) f32x4{acc[j][4 * (i)], acc[j][4 * (i) + 1], acc[j][4 * (i) + 2], acc[j][4 * (i) + 3]}

DI u32x4 pk8(const f32x4& a, const f32x4& b, float sc) {
    return u32x4{pk_bf16(a[0] * sc, a[1] * sc), pk_bf16(a[2] * sc, a[3] * sc), pk_bf16(b[0] * sc, b[1] * sc), pk_bf16(b[2] * sc, b[3] * sc)};
}

__global__ __launch_bounds__(768, 3) void k_proj(const char* __restrict__ x, const char* __restrict__ wq, const char* __restrict__ wk,
                                                 const char* __restrict__ wv, const char* __restrict__ wqkv, const char* __restrict__ gw,
                                                 char* __restrict__ ws) {
    __shared__ __attribute__((aligned(16))) char lds[4 * P_SLOT];
    STAMP_DECL; STAMP(0);
    const int b = blockIdx.x, xcd = b & 7, ii = b >> 3;
    const int mb = (xcd & 1) * 8 + (ii & 7), type = xcd >> 1, nq = ii >> 3;
    const int tid = threadIdx.x, l = tid & 63, w = __builtin_amdgcn_readfirstlane(tid >> 6);
    const int ekind = type == 3 ? 2 + (nq >> 1) : (nq < 2 ? (type == 2 ? 4 : type) : 5);
    if (w >= 4) {
        int rot = (ii & 7) + ((nq * 5) >> 1); rot = rot >= 10 ? rot - 10 : rot;
        proj_loader((unsigned)(uintptr_t)lds, x + (size_t)mb * 128 * DM * 2, type == 0 ? wq + nq * 320 : wqkv + (type - 1) * 1280 + nq * 320,
                    type == 0 ? 1280 : 3840, ekind < 2 ? wk : (ekind < 4 ? wv : gw), ekind < 4 ? 2560 : 128, rot);
        return;
    }
    const int wm = w >> 1, wn = w & 1, r = l & 15, g = l >> 4;
    const int ets = 4 * (nq & 1) + w;
    f32x16 acc[5]; f32x4 eacc = {0.f, 0.f, 0.f, 0.f};
#pragma unroll
    for (int j = 0; j < 5; ++j)
#pragma unroll
        for (int i = 0; i < 16; ++i) acc[j][i] = 0.f;
    const unsigned lds0 = (unsigned)(uintptr_t)lds;
    const int chunk = mb * 2 + wm;
    char* kva = ws + WS_KV + 32 * KV_HEAD + (mb * 2 + (nq & 1)) * 5120;
    if (type == 3) {
        proj_mma<true, 1>(lds0, ekind & 1, ets, acc, eacc);
        STAMP(3);
        char* base = ws + WS_KV + (size_t)(nq * 8 + 4 * wn) * KV_HEAD + chunk * 5120 + 2560 + g * 320;
#pragma unroll
        for (int t = 0; t < 2; ++t) {
#pragma unroll
            for (int j = 0; j < 4; ++j) *(u32x4*)(base + (size_t)j * KV_HEAD + t * 1280 + r * 16) = pk8(ACC(2 * t, j), ACC(2 * t + 1, j), 1.0f);
            *(u32x4*)(base + (size_t)(r >> 2) * KV_HEAD + t * 1280 + (16 + (r & 3)) * 16) = pk8(ACC(2 * t, 4), ACC(2 * t + 1, 4), 1.0f);
        }
        const int d = (ekind & 1) * 16 + r;
        if (d < 20) {
            u32x2 v = {pk_bf16(eacc[0], eacc[1]), pk_bf16(eacc[2], eacc[3])};
            *(u32x2*)(kva + 2560 + (w >> 1) * 1280 + g * 320 + d * 16 + (w & 1) * 8) = v;
        }
    } else {
        if (ekind < 4) proj_mma<false, 1>(lds0, ekind & 1, ets, acc, eacc);
        else if (ekind == 4) proj_mma<false, 2>(lds0, 0, ets, acc, eacc);
        else proj_mma<false, 0>(lds0, 0, ets, acc, eacc);
        STAMP(3);
        const float sc = type == 2 ? 1.0f : 0.32259784f;
        const size_t hstride = type == 2 ? KV_HEAD : Q_HEAD;
        const int row0 = type == 2 ? 16 * (r >> 3) + (r & 7) : r, ustep = type == 2 ? 128 : 256;
        char* base = (type == 2 ? ws + WS_KV + chunk * 5120 : ws + WS_Q + (size_t)type * 32 * Q_HEAD + chunk * 2560) + (size_t)(nq * 8 + 4 * wn) * hstride + row0 * 16;
#pragma unroll
        for (int i = 0; i < 4; ++i)
#pragma unroll
            for (int jj = 0; jj < 2; ++jj)
                *(u32x4*)(base + (2 * jj + (g >> 1)) * hstride + (i >> 1) * 1024 + (g & 1) * 512 + (i & 1) * ustep) = pk8(ACC(i, 2 * jj), ACC(i, 2 * jj + 1), sc);
#pragma unroll
        for (int u = 0; u < 2; ++u) *(u32x4*)(base + g * hstride + 2048 + u * ustep) = pk8(ACC(u, 4), ACC(u + 2, 4), sc);
        u32x2 v = {pk_bf16(eacc[0], eacc[1]), pk_bf16(eacc[2], eacc[3])};
        const int erow = 16 * (r >> 3) + 8 * (w & 1) + (r & 7);
        if (ekind == 0) *(u32x2*)(kva + (w >> 1) * 1024 + (g >> 1) * 512 + erow * 16 + (g & 1) * 8) = v;
        else if (ekind == 1) { if (g == 0) *(u32x2*)(kva + 2048 + erow * 16 + (w >> 1) * 8) = v; }
        else if (ekind == 4) { if (g == 0) ((float*)(ws + WS_G))[mb * 128 + 16 * ets + r] = 1.0f / (1.0f + __expf(-eacc[0])); }
    }
    STAMP(4); STAMP_FLUSH(ws, 0);
}

#define S0R 128
#define S1R 160
#define VAR 192
#define VBR 200
#define KAR 208
#define KBR 212
#define E_(X, i) "v_exp_f32 v[" STR(X) "+" #i "], v[" STR(X) "+" #i "]\n\t"
#define C_(X, d, a, b) "v_cvt_pk_bf16_f32 v[" STR(X) "+" #d "], v[" STR(X) "+" #a "], v[" STR(X) "+" #b "]\n\t"
#define PV_(O, V, vo, X, po) "v_mfma_f32_32x32x16_bf16 %[" #O "], v[" STR(V) "+" #vo ":" STR(V) "+" #vo "+3], v[" STR(X) "+" #po ":" STR(X) "+" #po "+3], %[" #O "]\n\t"
#define M1_(Y, KB) "v_mfma_f32_32x32x4_2b_bf16 v[" STR(Y) ":" STR(Y) "+31], " KB ", %[q2], %[negm]\n\t"
#define M2_(Y, o, KA, Q) "v_mfma_f32_32x32x16_bf16 v[" STR(Y) "+" #o ":" STR(Y) "+" #o "+15], " KA ", %[" #Q "], v[" STR(Y) "+" #o ":" STR(Y) "+" #o "+15]\n\t"
#define KA_ "v[" STR(KAR) ":" STR(KAR) "+3]"
#define KB0_ "v[" STR(KBR) ":" STR(KBR) "+1]"
#define KB1_ "v[" STR(KBR) "+2:" STR(KBR) "+3]"
#define RDKA(off) "ds_read_b128 " KA_ ", %[ak1] offset:" STR(off) "\n\t"
#define RDKB(off) "ds_read_b128 v[" STR(KBR) ":" STR(KBR) "+3], %[ak2] offset:" STR(off) "\n\t"
#define RDV(V, off) "s_mov_b64 exec, %[vmask]\n\tds_read_b128 v[" STR(V) ":" STR(V) "+3], %[av] offset:" STR(off) "\n\t" \
                    "ds_read_b128 v[" STR(V) "+4:" STR(V) "+7], %[av] offset:" STR(off) "+640\n\ts_mov_b64 exec, -1\n\t"
#define PRIO(n) "s_setprio " #n "\n\t"
#define TILE(X, Y, VC, VP, KAOFF, KB, KBRD, VOFF, PRE) \
    PRE "s_cmp_lt_u32 %[cnt], 2\n\ts_cbranch_scc0 7f\n\t" DMA1 "7:\n\t" E_(X, 0) E_(X, 1) \
    PV_(ob, VP, 4, Y, 24) \
    E_(X, 2) E_(X, 3) C_(X, 0, 0, 1) E_(X, 4) E_(X, 5) C_(X, 1, 2, 3) \
    "s_waitcnt lgkmcnt(0)\n\t" M1_(Y, KB) KBRD \
    E_(X, 6) E_(X, 7) C_(X, 2, 4, 5) E_(X, 8) C_(X, 3, 6, 7) E_(X, 9) E_(X, 10) E_(X, 11) C_(X, 8, 8, 9) \
    PV_(oa, VC, 0, X, 0) RDV(VP, VOFF) \
    E_(X, 12) C_(X, 9, 10, 11) E_(X, 13) E_(X, 14) E_(X, 15) C_(X, 10, 12, 13) E_(X, 16) C_(X, 11, 14, 15) E_(X, 17) \
    M2_(Y, 0, KA_, q1a) \
    E_(X, 18) E_(X, 19) C_(X, 16, 16, 17) E_(X, 20) E_(X, 21) C_(X, 17, 18, 19) \
    PV_(oa, VC, 4, X, 8) \
    E_(X, 22) E_(X, 23) C_(X, 18, 20, 21) E_(X, 24) C_(X, 19, 22, 23) E_(X, 25) \
    M2_(Y, 16, KA_, q1b) RDKA(KAOFF) \
    E_(X, 26) E_(X, 27) C_(X, 24, 24, 25) E_(X, 28) E_(X, 29) C_(X, 25, 26, 27) \
    PV_(ob, VC, 0, X, 16) \
    E_(X, 30) E_(X, 31) C_(X, 26, 28, 29) C_(X, 27, 30, 31)
#define CLOB8(a) "v" STR(a##0), "v" STR(a##1), "v" STR(a##2), "v" STR(a##3), "v" STR(a##4), "v" STR(a##5), "v" STR(a##6), "v" STR(a##7), "v" STR(a##8), "v" STR(a##9)

DI void attn_fallback(const char* lds, bf16x8 q1, s16x4 q2, f32x16& o) {
    const int l = threadIdx.x & 63, r32 = l & 31, h = l >> 5;
    const s16x4 z4 = {0, 0, 0, 0};
    const bf16x8 q2f = cat8(h == 0 ? q2 : z4, z4);
    const bf16x8 ones = {0x3f80, 0x3f80, 0x3f80, 0x3f80, 0x3f80, 0x3f80, 0x3f80, 0x3f80};
    float m = -INFINITY;
#pragma unroll
    for (int i = 0; i < 16; ++i) o[i] = 0.f;
    for (int t = 0; t < 64; ++t) {
        const LDSP char* cb = (const LDSP char*)lds + (t >> 1) * 5120;
        const bf16x8 k1 = *(const LDSP bf16x8*)(cb + (t & 1) * 1024 + l * 16);
        const s16x4 k2 = *(const LDSP s16x4*)(cb + 2048 + r32 * 16 + (t & 1) * 8);
        f32x16 s;
#pragma unroll
        for (int i = 0; i < 16; ++i) s[i] = 0.f;
        s = MFMA32(k1, q1, s);
        s = MFMA32(cat8(h == 0 ? k2 : z4, z4), q2f, s);
        float tm = s[0];
#pragma unroll
        for (int i = 1; i < 16; ++i) tm = fmaxf(tm, s[i]);
        tm = fmaxf(tm, __shfl_xor(tm, 32));
        const float mn = fmaxf(m, tm), alpha = __builtin_amdgcn_exp2f(m - mn);
        m = mn;
        u32x4 p0, p1;
#pragma unroll
        for (int i = 0; i < 4; ++i) {
            p0[i] = pk_bf16(__builtin_amdgcn_exp2f(s[2 * i] - m), __builtin_amdgcn_exp2f(s[2 * i + 1] - m));
            p1[i] = pk_bf16(__builtin_amdgcn_exp2f(s[8 + 2 * i] - m), __builtin_amdgcn_exp2f(s[9 + 2 * i] - m));
        }
#pragma unroll
        for (int i = 0; i < 16; ++i) o[i] *= alpha;
        const int dv = r32 < 20 ? r32 : 0;
        bf16x8 v0 = *(const LDSP bf16x8*)(cb + 2560 + (t & 1) * 1280 + h * 320 + dv * 16);
        bf16x8 v1 = *(const LDSP bf16x8*)(cb + 2560 + (t & 1) * 1280 + 640 + h * 320 + dv * 16);
        if (r32 >= 20) { v0 = ones; v1 = ones; }
        o = MFMA32(v0, __builtin_bit_cast(bf16x8, p0), o);
        o = MFMA32(v1, __builtin_bit_cast(bf16x8, p1), o);
    }
}

DI bool attn_ok(const f32x16& o) {
    float a = 0.f;
#pragma unroll
    for (int i = 0; i < 12; ++i) a = fmaxf(a, fabsf(o[i]));
    float s = 0.f;
#pragma unroll
    for (int i = 0; i < 12; ++i) s += o[i] * 0.f;
    return o[12] < 0x1p80f && a < 0x1p126f && s == 0.f;
}

__global__ __launch_bounds__(512, 2) void k_attn(const char* __restrict__ woa, const char* __restrict__ wob, char* __restrict__ ws) {
    __shared__ __attribute__((aligned(16))) char lds[163840];
    STAMP_DECL; STAMP(0);
    const int b = blockIdx.x, xcd = b & 7, ii = b >> 3;
    const int hq = ii >> 2, head = (hq & 1) * 32 + xcd * 4 + (hq >> 1), qb = ii & 3;
    const int tid = threadIdx.x, l = tid & 63, w = __builtin_amdgcn_readfirstlane(tid >> 6), r32 = l & 31, h = l >> 5;
    const int qc = qb * 8 + w;
    const char* qp = ws + WS_Q + (size_t)head * Q_HEAD + qc * 2560;
    const char* kvp = ws + WS_KV + (size_t)(head < 32 ? 32 : head - 32) * KV_HEAD;
    const unsigned lds0 = (unsigned)(uintptr_t)lds;
    bf16x8 q1a, q1b, k1f; s16x4 q2, k2f;
    unsigned dv = w * 1024 + l * 16, dm = __builtin_amdgcn_readfirstlane(lds0 + w * 1024);
    {
        unsigned sv;
#define DMA1 "s_mov_b32 m0, %[dm]\n\ts_add_u32 %[dm], %[dm], 8192\n\tglobal_load_lds_dwordx4 %[dv], %[kvp]\n\tv_add_u32 %[dv], 0x2000, %[dv]\n\t"
        asm volatile(
            "global_load_dwordx4 %[q1a], %[o16], %[qp]\n\t"
            "global_load_dwordx4 %[q1b], %[o16], %[qp] offset:1024\n\t"
            "global_load_dwordx2 %[q2], %[o8], %[qp] offset:2048\n\t"
            "global_load_dwordx4 %[k1f], %[o16], %[kvp]\n\t"
            "global_load_dwordx2 %[k2f], %[o8r], %[kvp] offset:2048\n\t"
            "s_mov_b32 %[sv], m0\n\t"
            DMA1 DMA1 DMA1 DMA1
            "s_mov_b32 m0, %[sv]\n\ts_waitcnt vmcnt(4)"
            : [q1a] "=&v"(q1a), [q1b] "=&v"(q1b), [q2] "=&v"(q2), [k1f] "=&v"(k1f), [k2f] "=&v"(k2f), [sv] "=&s"(sv), [dv] "+v"(dv), [dm] "+s"(dm)
            : [qp] "s"(qp), [kvp] "s"(kvp), [o16] "v"(l * 16), [o8] "v"(r32 * 16 + h * 8), [o8r] "v"(r32 * 16)
            : "memory", "scc");
    }
    STAMP(1);
    f32x32 negm;
    {
        f32x32 z;
#pragma unroll
        for (int i = 0; i < 32; ++i) z[i] = 0.f;
        const f32x32 s2 = __builtin_amdgcn_mfma_f32_32x32x4bf16_1k(k2f, q2, z, 0, 0, 0);
        f32x16 sa = __builtin_shufflevector(s2, s2, 0, 1, 2, 3, 4, 5, 6, 7, 8, 9, 10, 11, 12, 13, 14, 15);
        f32x16 sb = __builtin_shufflevector(s2, s2, 16, 17, 18, 19, 20, 21, 22, 23, 24, 25, 26, 27, 28, 29, 30, 31);
        sa = MFMA32(k1f, q1a, sa); sb = MFMA32(k1f, q1b, sb);
        float ma = sa[0], mb = sb[0];
#pragma unroll
        for (int i = 1; i < 16; ++i) { ma = fmaxf(ma, sa[i]); mb = fmaxf(mb, sb[i]); }
        ma = fmaxf(ma, __shfl_xor(ma, 32)); mb = fmaxf(mb, __shfl_xor(mb, 32));
#pragma unroll
        for (int i = 0; i < 16; ++i) { negm[i] = -ma; negm[16 + i] = -mb; }
    }
    const int gw = b * 8 + w, fid = gw < 1600 ? gw : 1599, wks = fid / 40, wns = fid - 40 * wks;
    const int r = l & 15, g = l >> 4;
    const int wcol = 16 * wns + r;
    const int wrow = wks < 32 ? ((2 * wks + (g >> 1)) & 31) * 20 + 4 * (g & 1) : ((8 * (wks - 32) + 2 * g) & 31) * 20 + 16;
    const char* wsrc = (wks < 16 || (wks >= 32 && wks < 36)) ? woa : wob;
    const unsigned wv0 = wrow * 1280 + wcol * 2, wv1 = wv0 + (wks < 32 ? 8 : 20) * 1280;
    const char* gp = ws + WS_G + qc * 256;
    f32x16 oa, ob;
#pragma unroll
    for (int i = 0; i < 16; ++i) { oa[i] = 0.f; ob[i] = 0.f; }
    unsigned ak1 = lds0 + l * 16, ak2 = lds0 + 2048 + r32 * 16, av = lds0 + 2560 + h * 320 + r32 * 16;
    unsigned e0, e1, e2, e3, e4, e5, e6, e7, cnt, inc, svm, dm2 = __builtin_amdgcn_readfirstlane(lds0 + w * 1024 + 32768); float ga, gb;
    const unsigned vini = (r32 & 27) == 24 ? 0x3f803f80u : 0u;
    STAMP(2);
    asm volatile(
        "global_load_ushort %[e0], %[wv0], %[wsrc]\n\t"
        "global_load_ushort %[e1], %[wv0], %[wsrc] offset:1280\n\t"
        "global_load_ushort %[e2], %[wv0], %[wsrc] offset:2560\n\t"
        "global_load_ushort %[e3], %[wv0], %[wsrc] offset:3840\n\t"
        "global_load_ushort %[e4], %[wv1], %[wsrc]\n\t"
        "global_load_ushort %[e5], %[wv1], %[wsrc] offset:1280\n\t"
        "global_load_ushort %[e6], %[wv1], %[wsrc] offset:2560\n\t"
        "global_load_ushort %[e7], %[wv1], %[wsrc] offset:3840\n\t"
        "global_load_dword %[ga], %[go], %[gp]\n\t"
        "global_load_dword %[gb], %[go], %[gp] offset:128\n\t"
        M1_(S0R, "%[k2f]")
        ".irp n,0,1,2,3,4,5,6,7,8,9,10,11,12,13,14,15\n\tv_mov_b32 v[" STR(VAR) "+\\n], %[vini]\n\t.endr\n\t"
        ".irp n,24,25,26,27\n\tv_mov_b32 v[" STR(S1R) "+\\n], 0\n\t.endr\n\t"
        M2_(S0R, 0, "%[k1f]", q1a) M2_(S0R, 16, "%[k1f]", q1b)
        "s_mov_b32 %[cnt], 0\n\ts_mov_b32 %[inc], 20480\n\t"
        "s_mov_b32 %[sv], m0\n\ts_waitcnt vmcnt(10)\n\ts_barrier\n\t"
        RDKB(0) RDKA(1024) RDV(VAR, 0)
        "s_nop 7\n\t"
        "1:\n\t"
        TILE(S0R, S1R, VAR, VBR, 5120, KB1_, RDKB(5120), 1280, PRIO(3))
        TILE(S1R, S0R, VBR, VAR, 5120 + 1024, KB0_, "", 5120, "")
        TILE(S0R, S1R, VAR, VBR, 10240, KB1_, RDKB(10240), 5120 + 1280, "")
        TILE(S1R, S0R, VBR, VAR, 10240 + 1024, KB0_, "", 10240, "")
        TILE(S0R, S1R, VAR, VBR, 15360, KB1_, RDKB(15360), 10240 + 1280, PRIO(2))
        TILE(S1R, S0R, VBR, VAR, 15360 + 1024, KB0_, "", 15360, "s_cmp_eq_u32 %[cnt], 7\n\ts_cselect_b32 %[inc], -143360, %[inc]\n\t")
        TILE(S0R, S1R, VAR, VBR, 0, KB1_, RDKB(0), 15360 + 1280, PRIO(1) "v_add_u32 %[ak1], %[inc], %[ak1]\n\tv_add_u32 %[ak2], %[inc], %[ak2]\n\t")
        TILE(S1R, S0R, VBR, VAR, 1024, KB0_, "", 0, PRIO(0) "v_add_u32 %[av], %[inc], %[av]\n\t")
        "5:\n\ts_add_u32 %[cnt], %[cnt], 1\n\t"
        "s_cmp_eq_u32 %[cnt], 1\n\ts_cbranch_scc1 2f\n\t"
        "s_cmp_eq_u32 %[cnt], 3\n\ts_cbranch_scc1 3f\n\t"
        "s_cmp_lt_u32 %[cnt], 8\n\ts_cbranch_scc1 1b\n\t"
        "s_branch 4f\n\t"
        "2:\n\ts_waitcnt vmcnt(3)\n\ts_barrier\n\ts_branch 1b\n\t"
        "3:\n\ts_waitcnt vmcnt(0)\n\ts_barrier\n\ts_branch 1b\n\t"
        "4:\n\ts_nop 1\n\t"
        PV_(ob, VBR, 4, S1R, 24)
        "s_mov_b32 m0, %[sv]\n\ts_waitcnt lgkmcnt(0)\n\ts_nop 15\n\ts_nop 7"
        : [oa] "+v"(oa), [ob] "+v"(ob), [ak1] "+v"(ak1), [ak2] "+v"(ak2), [av] "+v"(av),
          [e0] "=&v"(e0), [e1] "=&v"(e1), [e2] "=&v"(e2), [e3] "=&v"(e3), [e4] "=&v"(e4), [e5] "=&v"(e5), [e6] "=&v"(e6), [e7] "=&v"(e7),
          [ga] "=&v"(ga), [gb] "=&v"(gb), [cnt] "=&s"(cnt), [inc] "=&s"(inc), [sv] "=&s"(svm), [dv] "+v"(dv), [dm] "+s"(dm2)
        : [negm] "v"(negm), [q1a] "v"(q1a), [q1b] "v"(q1b), [q2] "v"(q2), [k1f] "v"(k1f), [k2f] "v"(k2f),
          [vmask] "s"(0x000fffff000fffffull), [wsrc] "s"(wsrc), [wv0] "v"(wv0), [wv1] "v"(wv1), [gp] "s"(gp), [go] "v"(r32 * 4), [vini] "v"(vini), [kvp] "s"(kvp)
        : "memory", "scc", "v128", "v129", CLOB8(13), CLOB8(14), CLOB8(15), CLOB8(16), CLOB8(17), CLOB8(18), CLOB8(19), CLOB8(20),
          "v210", "v211", "v212", "v213", "v214", "v215"
          );
    STAMP(3);
    if (gw < 1600) {
        u32x4 v = {e0 | (e1 << 16), e2 | (e3 << 16), e4 | (e5 << 16), e6 | (e7 << 16)};
        *(u32x4*)(ws + WS_WO + (size_t)fid * 1024 + l * 16) = v;
    }
    if (__builtin_expect(!__all(attn_ok(oa) && attn_ok(ob)), 0)) {
        s16x4 q2a, q2b;
#pragma unroll
        for (int i = 0; i < 4; ++i) { q2a[i] = (short)__shfl((int)q2[i], r32); q2b[i] = (short)__shfl((int)q2[i], r32 + 32); }
        attn_fallback(lds, q1a, q2a, oa);
        attn_fallback(lds, q1b, q2b, ob);
    }
    const float fa = (head < 32 ? ga : 1.0f - ga) / oa[12], fb = (head < 32 ? gb : 1.0f - gb) / ob[12];
    char* o1 = ws + WS_O1 + (size_t)head * 65536 + qc * 2048 + l * 16;
    char* o2 = ws + WS_O2 + (size_t)(head >> 1) * 32768 + (qc * 64 + r32) * 16 + (head & 1) * 8;
    u32x4 va = {pk_bf16(oa[0] * fa, oa[1] * fa), pk_bf16(oa[2] * fa, oa[3] * fa), pk_bf16(oa[4] * fa, oa[5] * fa), pk_bf16(oa[6] * fa, oa[7] * fa)};
    u32x4 vb = {pk_bf16(ob[0] * fb, ob[1] * fb), pk_bf16(ob[2] * fb, ob[3] * fb), pk_bf16(ob[4] * fb, ob[5] * fb), pk_bf16(ob[6] * fb, ob[7] * fb)};
    *(u32x4*)o1 = va;
    *(u32x4*)(o1 + 1024) = vb;
    if (h == 0) {
        u32x2 ta = {pk_bf16(oa[8] * fa, oa[9] * fa), pk_bf16(oa[10] * fa, oa[11] * fa)};
        u32x2 tb = {pk_bf16(ob[8] * fb, ob[9] * fb), pk_bf16(ob[10] * fb, ob[11] * fb)};
        *(u32x2*)o2 = ta;
        *(u32x2*)(o2 + 512) = tb;
    }
    STAMP(4); STAMP_FLUSH(ws, 1);
}

__global__ __launch_bounds__(320, 2) void k_out(const char* __restrict__ ws, char* __restrict__ out) {
    __shared__ __attribute__((aligned(16))) char lds[5 * 4 * 4 * 1024];
    STAMP_DECL; STAMP(0);
    const int b = blockIdx.x, xcd = b & 7, ii = b >> 3;
    const int mb = (xcd >> 1) * 8 + (ii & 7), nb = (xcd & 1) * 4 + (ii >> 3);
    const int tid = threadIdx.x, l = tid & 63, w = __builtin_amdgcn_readfirstlane(tid >> 6), r = l & 15, g = l >> 4;
    const char* ap = w < 4 ? ws + WS_O1 + (size_t)w * 8 * 131072 + mb * 2048 : ws + WS_O2 + mb * 1024;
    const char* wp = ws + WS_WO + (size_t)w * 8 * 40960 + nb * 5 * 1024;
    unsigned ao[4], wo[5];
#pragma unroll
    for (int i = 0; i < 4; ++i) ao[i] = w < 4 ? (g >> 1) * 65536 + (i >> 1) * 1024 + (g & 1) * 512 + ((i & 1) * 16 + r) * 16 : g * 32768 + (i * 16 + r) * 16;
#pragma unroll
    for (int j = 0; j < 5; ++j) wo[j] = ((j + w) % 5) * 1024 + l * 16;
    bf16x8 af[4][4], wf[4][5];
    auto issue = [&](int slot) {
        asm volatile(
            "global_load_dwordx4 %0, %9, %18\n\tglobal_load_dwordx4 %4, %13, %19\n\tglobal_load_dwordx4 %1, %10, %18\n\tglobal_load_dwordx4 %5, %14, %19\n\t"
            "global_load_dwordx4 %2, %11, %18\n\tglobal_load_dwordx4 %6, %15, %19\n\tglobal_load_dwordx4 %3, %12, %18\n\tglobal_load_dwordx4 %7, %16, %19\n\t"
            "global_load_dwordx4 %8, %17, %19"
            : "=&v"(af[slot][0]), "=&v"(af[slot][1]), "=&v"(af[slot][2]), "=&v"(af[slot][3]),
              "=&v"(wf[slot][0]), "=&v"(wf[slot][1]), "=&v"(wf[slot][2]), "=&v"(wf[slot][3]), "=&v"(wf[slot][4])
            : "v"(ao[0]), "v"(ao[1]), "v"(ao[2]), "v"(ao[3]), "v"(wo[0]), "v"(wo[1]), "v"(wo[2]), "v"(wo[3]), "v"(wo[4]), "s"(ap), "s"(wp)
            : "memory");
        ap += 131072; wp += 40960;
    };
#define OUT_WAIT(slot, N) asm volatile("s_waitcnt vmcnt(" #N ")" : "+v"(af[slot][0]), "+v"(af[slot][1]), "+v"(af[slot][2]), "+v"(af[slot][3]), \
        "+v"(wf[slot][0]), "+v"(wf[slot][1]), "+v"(wf[slot][2]), "+v"(wf[slot][3]), "+v"(wf[slot][4]) :: "memory")
    issue(0); issue(1); issue(2); issue(3);
    f32x4 acc[4][5];
#pragma unroll
    for (int i = 0; i < 4; ++i)
#pragma unroll
        for (int j = 0; j < 5; ++j) acc[i][j] = f32x4{0.f, 0.f, 0.f, 0.f};
    auto mm = [&](int slot) {
#pragma unroll
        for (int j = 0; j < 5; ++j)
#pragma unroll
            for (int i = 0; i < 4; ++i) acc[i][j] = MFMA16(wf[slot][j], af[slot][i], acc[i][j]);
        __builtin_amdgcn_sched_barrier(0);
    };
    OUT_WAIT(0, 27); mm(0); issue(0);
    OUT_WAIT(1, 27); mm(1); issue(1);
    OUT_WAIT(2, 27); mm(2); issue(2);
    OUT_WAIT(3, 27); mm(3); issue(3);
    OUT_WAIT(0, 27); mm(0);
    OUT_WAIT(1, 18); mm(1);
    OUT_WAIT(2, 9); mm(2);
    OUT_WAIT(3, 0); mm(3);
    STAMP(3);
#pragma unroll
    for (int j = 1; j < 5; ++j)
#pragma unroll
        for (int i = 0; i < 4; ++i) *(f32x4*)(lds + ((((j + w) % 5) * 4 + j - 1) * 4 + i) * 1024 + l * 16) = acc[i][j];
    __syncthreads();
#pragma unroll
    for (int j = 0; j < 4; ++j)
#pragma unroll
        for (int i = 0; i < 4; ++i) acc[i][0] += *(const f32x4*)(lds + ((w * 4 + j) * 4 + i) * 1024 + l * 16);
    char* op = out + (size_t)(mb * 64 + r) * 1280 + nb * 160 + w * 32 + g * 8;
#pragma unroll
    for (int i = 0; i < 4; ++i) {
        u32x2 v = {pk_bf16(acc[i][0][0], acc[i][0][1]), pk_bf16(acc[i][0][2], acc[i][0][3])};
        *(u32x2*)(op + i * 16 * 1280) = v;
    }
    STAMP(4); STAMP_FLUSH(ws, 2);
}

extern "C" void kernel_launch(void* const* d_in, const int* in_sizes, int n_in, void* d_out, int out_size, void* d_ws, size_t ws_size,
                              hipStream_t stream) {
    const char* x = (const char*)d_in[0];
    const char* wq = (const char*)d_in[1];
    const char* wk = (const char*)d_in[2];
    const char* wv = (const char*)d_in[3];
    const char* woa = (const char*)d_in[4];
    const char* wqkv = (const char*)d_in[6];
    const char* wob = (const char*)d_in[7];
    const char* gw = (const char*)d_in[8];
    char* ws = (char*)d_ws;
    k_proj<<<256, 768, 0, stream>>>(x, wq, wk, wv, wqkv, gw, ws);
    k_attn<<<256, 512, 0, stream>>>(woa, wob, ws);
    k_out<<<256, 320, 0, stream>>>(ws, (char*)d_out);
}
```

```cpp
#include <hip/hip_runtime.h>
#include <hip/hip_bf16.h>
#include <stdint.h>

typedef __attribute__((ext_vector_type(8))) short bf16x8;
typedef __attribute__((ext_vector_type(4))) short s16x4;
typedef __attribute__((ext_vector_type(4))) float f32x4;
typedef __attribute__((ext_vector_type(16))) float f32x16;
typedef __attribute__((ext_vector_type(32))) float f32x32;
typedef __attribute__((ext_vector_type(2))) unsigned u32x2;
typedef __attribute__((ext_vector_type(4))) unsigned u32x4;
typedef __attribute__((ext_vector_type(2))) __bf16 bf16x2_t;
typedef __attribute__((ext_vector_type(2))) float f32x2;
#define LDSP __attribute__((address_space(3)))
#define DI __device__ __forceinline__

#define STAMP_PARAMS
#define STAMP_ARGS
#define STAMP_DECL
#define STAMP(slot)
#define STAMP_FLUSH(wsp, kid)
constexpr int SEQ = 2048, DM = 640;
constexpr size_t Q_HEAD = 32 * 2560;
constexpr size_t KV_HEAD = 32 * 5120;
constexpr size_t WS_Q = 0;
constexpr size_t WS_KV = WS_Q + 64 * Q_HEAD;
constexpr size_t WS_G = WS_KV + 33 * KV_HEAD;
constexpr size_t WS_O1 = WS_G + 2048 * 4;
constexpr size_t WS_O2 = WS_O1 + 64 * 65536;
constexpr size_t WS_WO = WS_O2 + 32 * 2048 * 16;
constexpr size_t WS_CNT = WS_WO + 1600 * 1024;

DI unsigned pk_bf16(float a, float b) {
    f32x2 v = {a, b};
    return __builtin_bit_cast(unsigned, __builtin_convertvector(v, bf16x2_t));
}
#define ST_FLAGS "sc1"
DI void st16(char* p, u32x4 v) { asm volatile("global_store_dwordx4 %0, %1, off " ST_FLAGS "\n\ts_nop 1" :: "v"(p), "v"(v) : "memory"); }
DI void st8(char* p, u32x2 v) { asm volatile("global_store_dwordx2 %0, %1, off " ST_FLAGS :: "v"(p), "v"(v) : "memory"); }
DI s16x4 tr_read(const LDSP char* p) {
    typedef short v4i16_t __attribute__((ext_vector_type(4)));
    return __builtin_amdgcn_ds_read_tr16_b64_v4i16((LDSP v4i16_t*)p);
}
DI bf16x8 cat8(s16x4 lo, s16x4 hi) { return __builtin_shufflevector(lo, hi, 0, 1, 2, 3, 4, 5, 6, 7); }
#define MFMA16(a, b, c) __builtin_amdgcn_mfma_f32_16x16x32_bf16((a), (b), (c), 0, 0, 0)
#define MFMA32(a, b, c) __builtin_amdgcn_mfma_f32_32x32x16_bf16((a), (b), (c), 0, 0, 0)

constexpr int P_SLOT = 40960;

DI void proj_loader(unsigned lds0, const char* xb, const char* wb, int ldw2, const char* eb, int ebytes, int rot,
                    const char* woa, const char* wob, char* wsp) {
    const int l = threadIdx.x & 63, w = __builtin_amdgcn_readfirstlane(threadIdx.x >> 6) - 4;
    unsigned xo[2], wo[3];
#pragma unroll
    for (int n = 0; n < 2; ++n) { const int row = 8 * (w + 8 * n) + (l >> 3); xo[n] = row * (DM * 2) + (((l & 7) ^ ((row >> 1) & 7)) << 4); }
#pragma unroll
    for (int n = 0; n < 3; ++n) { const int cid = 64 * (w + 8 * n) + l, k = (cid * 3277) >> 16, c = cid - 20 * k; wo[n] = k * ldw2 + ((c ^ (((k >> 3) & 1) << 1)) << 4); }
    const char* p5 = wb; int st5 = 64 * ldw2; unsigned long long m5 = ~0ull;
    if (w >= 4) {
        const int nbytes = ebytes - (w - 4) * 1024;
        const int nl = nbytes >= 1024 ? 64 : (nbytes > 0 ? nbytes >> 4 : 1);
        m5 = nl == 64 ? ~0ull : (1ull << nl) - 1;
        p5 = nbytes > 0 ? eb + (w - 4) * 1024 : eb; st5 = ebytes; wo[2] = l * 16;
    }
    const unsigned dw = __builtin_amdgcn_readfirstlane(lds0 + w * 1024);
    int st = rot;
#pragma unroll 1
    for (int k = -2; k < 8; ++k) {
        if (k >= 0) asm volatile("s_waitcnt vmcnt(5)\n\ts_barrier" ::: "memory");
        {
            const unsigned d0 = dw + ((k + 2) & 3) * P_SLOT;
            const char* xs = xb + st * 128; const char* ws = wb + st * 64 * ldw2; const char* es = p5 + st * st5;
            unsigned sv;
            asm volatile(
                "s_mov_b32 %[sv], m0\n\ts_mov_b32 m0, %[d0]\n\ts_nop 0\n\t"
                "global_load_lds_dwordx4 %[x0], %[xs]\n\ts_add_u32 m0, m0, 8192\n\ts_nop 0\n\t"
                "global_load_lds_dwordx4 %[x1], %[xs]\n\ts_add_u32 m0, m0, 8192\n\ts_nop 0\n\t"
                "global_load_lds_dwordx4 %[w0], %[ws]\n\ts_add_u32 m0, m0, 8192\n\ts_nop 0\n\t"
                "global_load_lds_dwordx4 %[w1], %[ws]\n\ts_add_u32 m0, m0, 8192\n\ts_mov_b64 exec, %[m5]\n\t"
                "global_load_lds_dwordx4 %[w2], %[es]\n\ts_mov_b64 exec, -1\n\ts_mov_b32 m0, %[sv]"
                : [sv] "=&s"(sv)
                : [d0] "s"(d0), [m5] "s"(m5), [xs] "s"(xs), [ws] "s"(ws), [es] "s"(es),
                  [x0] "v"(xo[0]), [x1] "v"(xo[1]), [w0] "v"(wo[0]), [w1] "v"(wo[1]), [w2] "v"(wo[2])
                : "memory", "scc");
            st = st == 9 ? 0 : st + 1;
        }
    }
    const int gw = blockIdx.x * 8 + w, fid = gw < 1600 ? gw : 1599;
    const int fj = fid % 5, fs = (fid / 5) % 5, fg = (fid / 25) & 7, fn = fid / 200, r = l & 15, g = l >> 4;
    const int br = fs < 4 ? fs >> 1 : g >> 1;
    const int row = fs < 4 ? (4 * fg + 2 * (fs & 1) + (g >> 1)) * 20 + 4 * (g & 1) : (4 * fg + 2 * (g & 1)) * 20 + 16;
    const char* p0 = (br ? wob : woa) + row * 1280 + (80 * fn + 16 * fj + r) * 2;
    const char* p1 = p0 + (fs < 4 ? 8 : 20) * 1280;
    unsigned e0, e1, e2, e3, e4, e5, e6, e7;
    asm volatile(
        "global_load_ushort %0, %8, off\n\tglobal_load_ushort %1, %8, off offset:1280\n\tglobal_load_ushort %2, %8, off offset:2560\n\tglobal_load_ushort %3, %8, off offset:3840\n\t"
        "global_load_ushort %4, %9, off\n\tglobal_load_ushort %5, %9, off offset:1280\n\tglobal_load_ushort %6, %9, off offset:2560\n\tglobal_load_ushort %7, %9, off offset:3840\n\t"
        "s_waitcnt vmcnt(13)\n\ts_barrier\n\ts_waitcnt vmcnt(8)\n\ts_barrier\n\ts_waitcnt vmcnt(0)"
        : "=&v"(e0), "=&v"(e1), "=&v"(e2), "=&v"(e3), "=&v"(e4), "=&v"(e5), "=&v"(e6), "=&v"(e7) : "v"(p0), "v"(p1) : "memory");
    if (gw < 1600) st16(wsp + WS_WO + (size_t)fid * 1024 + l * 16, u32x4{e0 | (e1 << 16), e2 | (e3 << 16), e4 | (e5 << 16), e6 | (e7 << 16)});
    if (w == 0 && l == 0) *(unsigned*)(wsp + WS_CNT + blockIdx.x * 128) = 0;
}

#define STR_(x) #x
#define STR(x) STR_(x)
#define PR4(b) "v[" STR(b) ":" STR(b) "+3]"
#define PM_F(j, i, X) "v_mfma_f32_16x16x32_bf16 " PR4(20 + 16 * j + 4 * i) ", " PR4(136 + 4 * j) ", " PR4(X + 4 * i) ", " PR4(20 + 16 * j + 4 * i) "\n\t"
#define PM_T(j, i, X) "v_mfma_f32_16x16x32_bf16 " PR4(20 + 16 * j + 4 * i) ", " PR4(X + 4 * i) ", " PR4(136 + 4 * j) ", " PR4(20 + 16 * j + 4 * i) "\n\t"
#define PE_F "s_waitcnt lgkmcnt(2)\n\tv_mfma_f32_16x16x32_bf16 v[100:103], v[160:163], v[156:159], v[100:103]\n\t"
#define PE_T "s_waitcnt lgkmcnt(2)\n\tv_mfma_f32_16x16x32_bf16 v[100:103], v[156:159], v[160:163], v[100:103]\n\t"
#define PTL(j, sl, ks) "ds_read_b64_tr_b16 v[136+4*" #j ":136+4*" #j "+1], %[aw" #j "] offset:" #sl "*40960+" #ks "*10240\n\t"
#define PTH(j, sl, ks) "ds_read_b64_tr_b16 v[136+4*" #j "+2:136+4*" #j "+3], %[aw" #j "] offset:" #sl "*40960+" #ks "*10240+1280\n\t"
#define PBX(X, i, sl, ks) "ds_read_b128 " PR4(X + 4 * i) ", %[ax" #ks "] offset:" #sl "*40960+" #i "*2048\n\t"
#define PX1_0(sl, ks) ""
#define PX2_0(sl, ks) ""
#define PX3_0(sl, ks) ""
#define PX1_1(sl, ks) "ds_read_b128 v[156:159], %[ae" #ks "] offset:" #sl "*40960\n\t"
#define PX2_1(sl, ks) "ds_read_b64_tr_b16 v[160:161], %[aew] offset:" #sl "*40960+" #ks "*1280\n\t"
#define PX3_1(sl, ks) "ds_read_b64_tr_b16 v[162:163], %[aew] offset:" #sl "*40960+" #ks "*1280+160\n\t"
#define PX1_2(sl, ks) PX1_1(sl, ks)
#define PX2_2(sl, ks) "ds_read_b128 v[160:163], %[aew] offset:" #sl "*40960+" #ks "*64\n\t"
#define PX3_2(sl, ks) ""
#define PTG(a) "v_add_u32 %[" #a "], %[tg], %[" #a "]\n\t"
#define PSTEP(M, EK, PE, WB, X, Y, cs, ck, ns, nk, BAR, F1, F2, F3) \
    "s_waitcnt lgkmcnt(4)\n\t" \
    M(0, 0, X) PTL(4, cs, ck) M(0, 1, X) PTH(4, cs, ck) M(0, 2, X) BAR PBX(Y, 0, ns, nk) M(0, 3, X) PBX(Y, 1, ns, nk) \
    M(1, 0, X) PTL(0, ns, nk) M(1, 1, X) PTH(0, ns, nk) M(1, 2, X) PBX(Y, 2, ns, nk) M(1, 3, X) PX1_##EK(cs, ck) \
    "s_waitcnt lgkmcnt(" #WB ")\n\t" \
    M(2, 0, X) PTL(1, ns, nk) M(2, 1, X) PTH(1, ns, nk) M(2, 2, X) PBX(Y, 3, ns, nk) M(2, 3, X) PX2_##EK(cs, ck) \
    M(3, 0, X) PTL(2, ns, nk) M(3, 1, X) PTH(2, ns, nk) M(3, 2, X) PX3_##EK(cs, ck) M(3, 3, X) F1 \
    M(4, 0, X) PTL(3, ns, nk) M(4, 1, X) PTH(3, ns, nk) M(4, 2, X) F2 M(4, 3, X) F3 PE
#define CL10(a) "v" #a "0", "v" #a "1", "v" #a "2", "v" #a "3", "v" #a "4", "v" #a "5", "v" #a "6", "v" #a "7", "v" #a "8", "v" #a "9"
#define PLOOP(M, EK, PE, WB) \
    asm volatile( \
        "s_mov_b32 %[cnt], 0\n\ts_mov_b32 %[tg], 81920\n\t" \
        "s_barrier\n\t" \
        PBX(104, 0, 0, 0) PBX(104, 1, 0, 0) PTL(0, 0, 0) PTH(0, 0, 0) PBX(104, 2, 0, 0) PTL(1, 0, 0) PTH(1, 0, 0) PBX(104, 3, 0, 0) \
        PTL(2, 0, 0) PTH(2, 0, 0) PTL(3, 0, 0) PTH(3, 0, 0) \
        "1:\n\t" \
        PSTEP(M, EK, PE, WB, 104, 120, 0, 0, 0, 1, "", "", "", "") \
        PSTEP(M, EK, PE, WB, 120, 104, 0, 1, 1, 0, "s_barrier\n\t", PTG(ax0), "s_add_u32 %[cnt], %[cnt], 1\n\t", "") \
        PSTEP(M, EK, PE, WB, 104, 120, 1, 0, 1, 1, "", PTG(aw0) PTG(ae0), PTG(aw1) PTG(ax1), PTG(aw2)) \
        PSTEP(M, EK, PE, WB, 120, 104, 1, 1, 0, 0, "s_cmp_eq_u32 %[cnt], 5\n\ts_cbranch_scc1 2f\n\ts_barrier\n\t2:\n\t", PTG(aw3) PTG(ae1), PTG(aw4), PTG(aew)) \
        "s_sub_u32 %[tg], 0, %[tg]\n\ts_cmp_lg_u32 %[cnt], 5\n\ts_cbranch_scc1 1b\n\t" \
        "s_waitcnt lgkmcnt(0)\n\ts_nop 7" \
        : "+{v[20:35]}"(acc[0]), "+{v[36:51]}"(acc[1]), "+{v[52:67]}"(acc[2]), "+{v[68:83]}"(acc[3]), "+{v[84:99]}"(acc[4]), "+{v[100:103]}"(eacc), \
          [ax0] "+v"(ax0), [ax1] "+v"(ax1), [aw0] "+v"(aw[0]), [aw1] "+v"(aw[1]), [aw2] "+v"(aw[2]), [aw3] "+v"(aw[3]), [aw4] "+v"(aw[4]), \
          [ae0] "+v"(ae0), [ae1] "+v"(ae1), [aew] "+v"(aew), [cnt] "=&s"(cnt), [tg] "=&s"(tg) \
        : \
        : "memory", "scc", "v104", "v105", "v106", "v107", "v108", "v109", CL10(11), CL10(12), CL10(13), CL10(14), CL10(15), "v160", "v161", "v162", "v163")

template <bool VT, int EK>
DI void proj_mma(unsigned lds0, int ehi, int ets, f32x16 (&acc)[5], f32x4& eacc) {
    const int tid = threadIdx.x, l = tid & 63, w = __builtin_amdgcn_readfirstlane(tid >> 6);
    const int wm = w >> 1, wn = w & 1, r = l & 15, g = l >> 4;
    const int xsw = (g ^ ((r >> 1) & 7)) << 4;
    unsigned ax0 = lds0 + (64 * wm + r) * 128 + xsw, ax1 = lds0 + (64 * wm + r) * 128 + (xsw ^ 64);
    unsigned ae0 = lds0 + (16 * ets + r) * 128 + xsw, ae1 = lds0 + (16 * ets + r) * 128 + (xsw ^ 64);
    unsigned aw[5];
    const int q = r >> 2, p = r & 3;
#pragma unroll
    for (int j = 0; j < 5; ++j) {
        int col;
        if (j == 4) col = (4 * wn + p) * 20 + 16;
        else if (VT) col = (4 * wn + j) * 20 + 4 * p;
        else col = (4 * wn + (j & 2) + (p >> 1)) * 20 + 8 * (p & 1) + 4 * (j & 1);
        aw[j] = lds0 + 16384 + (8 * g + q) * 320 + ((col * 2) ^ ((g & 1) << 5));
    }
    unsigned aew = lds0 + (EK == 1 ? 36864 + (8 * g + q) * 40 + (ehi ? 32 : 8 * p) : 36864 + g * 16);
    unsigned cnt, tg;
    if constexpr (VT) PLOOP(PM_T, 1, PE_T, 6);
    else if constexpr (EK == 1) PLOOP(PM_F, 1, PE_F, 6);
    else if constexpr (EK == 2) PLOOP(PM_F, 2, PE_F, 6);
    else PLOOP(PM_F, 0, "", 5);
}
#define ACC(i, j) f32x4{acc[j][4 * (i)], acc[j][4 * (i) + 1], acc[j][4 * (i) + 2], acc[j][4 * (i) + 3]}

DI u32x4 pk8(const f32x4& a, const f32x4& b, float sc) {
    return u32x4{pk_bf16(a[0] * sc, a[1] * sc), pk_bf16(a[2] * sc, a[3] * sc), pk_bf16(b[0] * sc, b[1] * sc), pk_bf16(b[2] * sc, b[3] * sc)};
}

__global__ __launch_bounds__(768, 3) void k_proj(const char* __restrict__ x, const char* __restrict__ wq, const char* __restrict__ wk,
                                                 const char* __restrict__ wv, const char* __restrict__ wqkv, const char* __restrict__ gw,
                                                 const char* __restrict__ woa, const char* __restrict__ wob, char* __restrict__ ws) {
    __shared__ __attribute__((aligned(16))) char lds[4 * P_SLOT];
    STAMP_DECL; STAMP(0);
    const int b = blockIdx.x, xcd = b & 7, ii = b >> 3;
    const int mb = (xcd & 1) * 8 + (ii & 7), type = xcd >> 1, nq = ii >> 3;
    const int tid = threadIdx.x, l = tid & 63, w = __builtin_amdgcn_readfirstlane(tid >> 6);
    const int ekind = type == 3 ? 2 + (nq >> 1) : (nq < 2 ? (type == 2 ? 4 : type) : 5);
    if (w >= 4) {
        int rot = (ii & 7) + ((nq * 5) >> 1); rot = rot >= 10 ? rot - 10 : rot;
        proj_loader((unsigned)(uintptr_t)lds, x + (size_t)mb * 128 * DM * 2, type == 0 ? wq + nq * 320 : wqkv + (type - 1) * 1280 + nq * 320,
                    type == 0 ? 1280 : 3840, ekind < 2 ? wk : (ekind < 4 ? wv : gw), ekind < 4 ? 2560 : 128, rot, woa, wob, ws);
        return;
    }
    const int wm = w >> 1, wn = w & 1, r = l & 15, g = l >> 4;
    const int ets = 4 * (nq & 1) + w;
    f32x16 acc[5]; f32x4 eacc = {0.f, 0.f, 0.f, 0.f};
#pragma unroll
    for (int j = 0; j < 5; ++j)
#pragma unroll
        for (int i = 0; i < 16; ++i) acc[j][i] = 0.f;
    const unsigned lds0 = (unsigned)(uintptr_t)lds;
    const int chunk = mb * 2 + wm;
    char* kva = ws + WS_KV + 32 * KV_HEAD + (mb * 2 + (nq & 1)) * 5120;
    if (type == 3) {
        proj_mma<true, 1>(lds0, ekind & 1, ets, acc, eacc);
        STAMP(3);
        char* base = ws + WS_KV + (size_t)(nq * 8 + 4 * wn) * KV_HEAD + chunk * 5120 + 2560 + g * 320;
#pragma unroll
        for (int t = 0; t < 2; ++t) {
#pragma unroll
            for (int j = 0; j < 4; ++j) st16(base + (size_t)j * KV_HEAD + t * 1280 + r * 16, pk8(ACC(2 * t, j), ACC(2 * t + 1, j), 1.0f));
            st16(base + (size_t)(r >> 2) * KV_HEAD + t * 1280 + (16 + (r & 3)) * 16, pk8(ACC(2 * t, 4), ACC(2 * t + 1, 4), 1.0f));
        }
        const int d = (ekind & 1) * 16 + r;
        if (d < 20) {
            u32x2 v = {pk_bf16(eacc[0], eacc[1]), pk_bf16(eacc[2], eacc[3])};
            st8(kva + 2560 + (w >> 1) * 1280 + g * 320 + d * 16 + (w & 1) * 8, v);
        }
    } else {
        if (ekind < 4) proj_mma<false, 1>(lds0, ekind & 1, ets, acc, eacc);
        else if (ekind == 4) proj_mma<false, 2>(lds0, 0, ets, acc, eacc);
        else proj_mma<false, 0>(lds0, 0, ets, acc, eacc);
        STAMP(3);
        const float sc = type == 2 ? 1.0f : 0.32259784f;
        const size_t hstride = type == 2 ? KV_HEAD : Q_HEAD;
        const int row0 = type == 2 ? 16 * (r >> 3) + (r & 7) : r, ustep = type == 2 ? 128 : 256;
        char* base = (type == 2 ? ws + WS_KV + chunk * 5120 : ws + WS_Q + (size_t)type * 32 * Q_HEAD + chunk * 2560) + (size_t)(nq * 8 + 4 * wn) * hstride + row0 * 16;
#pragma unroll
        for (int i = 0; i < 4; ++i)
#pragma unroll
            for (int jj = 0; jj < 2; ++jj)
                st16(base + (2 * jj + (g >> 1)) * hstride + (i >> 1) * 1024 + (g & 1) * 512 + (i & 1) * ustep, pk8(ACC(i, 2 * jj), ACC(i, 2 * jj + 1), sc));
#pragma unroll
        for (int u = 0; u < 2; ++u) st16(base + g * hstride + 2048 + u * ustep, pk8(ACC(u, 4), ACC(u + 2, 4), sc));
        u32x2 v = {pk_bf16(eacc[0], eacc[1]), pk_bf16(eacc[2], eacc[3])};
        const int erow = 16 * (r >> 3) + 8 * (w & 1) + (r & 7);
        if (ekind == 0) st8(kva + (w >> 1) * 1024 + (g >> 1) * 512 + erow * 16 + (g & 1) * 8, v);
        else if (ekind == 1) { if (g == 0) st8(kva + 2048 + erow * 16 + (w >> 1) * 8, v); }
        else if (ekind == 4) { if (g == 0) ((float*)(ws + WS_G))[mb * 128 + 16 * ets + r] = 1.0f / (1.0f + __expf(-eacc[0])); }
    }
    STAMP(4); STAMP_FLUSH(ws, 0);
}

#define S0R 56
#define S1R 88
#define NMR 120
#define VAR 152
#define VBR 160
#define KAR 168
#define KBR 172
#define WFR 176
#define E_(X, i) "v_exp_f32 v[" STR(X) "+" #i "], v[" STR(X) "+" #i "]\n\t"
#define C_(X, d, a, b) "v_cvt_pk_bf16_f32 v[" STR(X) "+" #d "], v[" STR(X) "+" #a "], v[" STR(X) "+" #b "]\n\t"
#define PV_(O, V, vo, X, po) "v_mfma_f32_32x32x16_bf16 %[" #O "], v[" STR(V) "+" #vo ":" STR(V) "+" #vo "+3], v[" STR(X) "+" #po ":" STR(X) "+" #po "+3], %[" #O "]\n\t"
#define M1_(Y, KB) "v_mfma_f32_32x32x4_2b_bf16 v[" STR(Y) ":" STR(Y) "+31], " KB ", %[q2], v[" STR(NMR) ":" STR(NMR) "+31]\n\t"
#define M2_(Y, o, Q) "v_mfma_f32_32x32x16_bf16 v[" STR(Y) "+" #o ":" STR(Y) "+" #o "+15], " KA_ ", %[" #Q "], v[" STR(Y) "+" #o ":" STR(Y) "+" #o "+15]\n\t"
#define KA_ "v[" STR(KAR) ":" STR(KAR) "+3]"
#define KB0_ "v[" STR(KBR) ":" STR(KBR) "+1]"
#define KB1_ "v[" STR(KBR) "+2:" STR(KBR) "+3]"
#define RDKA(off) "ds_read_b128 " KA_ ", %[ak1] offset:" STR(off) "\n\t"
#define RDKB(off) "ds_read_b128 v[" STR(KBR) ":" STR(KBR) "+3], %[ak2] offset:" STR(off) "\n\t"
#define RDV(V, off) "s_mov_b64 exec, %[vmask]\n\tds_read_b128 v[" STR(V) ":" STR(V) "+3], %[av] offset:" STR(off) "\n\t" \
                    "ds_read_b128 v[" STR(V) "+4:" STR(V) "+7], %[av] offset:" STR(off) "+640\n\ts_mov_b64 exec, -1\n\t"
#define MX3(d, a, i) "v_max3_f32 v[" STR(NMR) "+" #d "], v[" STR(a) "], v[" STR(S0R) "+" #i "], v[" STR(S0R) "+" #i "+1]\n\t"
#define PRIO(n) "s_setprio " #n "\n\t"
#define DMA1 "s_mov_b32 m0, %[dm]\n\ts_add_u32 %[dm], %[dm], 8192\n\tglobal_load_lds_dwordx4 %[dv], %[kvp]\n\tv_add_u32 %[dv], 0x2000, %[dv]\n\t"
#define WLD1(s, j) "global_load_dwordx4 v[" STR(WFR) "+20*" #s "+4*" #j ":" STR(WFR) "+20*" #s "+4*" #j "+3], %[wv], %[wp] offset:" #j "*1024-2048\n\t"
#define WLD(s) WLD1(s, 0) WLD1(s, 1) WLD1(s, 2) WLD1(s, 3) WLD1(s, 4) "v_add_u32 %[wv], 0x1400, %[wv]\n\ts_branch 1b\n\t"
#define TILE(X, Y, VC, VP, KAOFF, KB, KBRD, VOFF, PRE) \
    PRE "s_cmp_lt_u32 %[cnt], 2\n\ts_cbranch_scc0 7f\n\t" DMA1 "7:\n\t" E_(X, 0) E_(X, 1) \
    PV_(ob, VP, 4, Y, 24) \
    E_(X, 2) E_(X, 3) C_(X, 0, 0, 1) E_(X, 4) E_(X, 5) C_(X, 1, 2, 3) \
    "s_waitcnt lgkmcnt(0)\n\t" M1_(Y, KB) KBRD \
    E_(X, 6) E_(X, 7) C_(X, 2, 4, 5) E_(X, 8) C_(X, 3, 6, 7) E_(X, 9) E_(X, 10) E_(X, 11) C_(X, 8, 8, 9) \
    PV_(oa, VC, 0, X, 0) RDV(VP, VOFF) \
    E_(X, 12) C_(X, 9, 10, 11) E_(X, 13) E_(X, 14) E_(X, 15) C_(X, 10, 12, 13) E_(X, 16) C_(X, 11, 14, 15) E_(X, 17) \
    M2_(Y, 0, q1a) \
    E_(X, 18) E_(X, 19) C_(X, 16, 16, 17) E_(X, 20) E_(X, 21) C_(X, 17, 18, 19) \
    PV_(oa, VC, 4, X, 8) \
    E_(X, 22) E_(X, 23) C_(X, 18, 20, 21) E_(X, 24) C_(X, 19, 22, 23) E_(X, 25) \
    M2_(Y, 16, q1b) RDKA(KAOFF) \
    E_(X, 26) E_(X, 27) C_(X, 24, 24, 25) E_(X, 28) E_(X, 29) C_(X, 25, 26, 27) \
    PV_(ob, VC, 0, X, 16) \
    E_(X, 30) E_(X, 31) C_(X, 26, 28, 29) C_(X, 27, 30, 31)

DI float max_halves(float m) {
    auto rr = __builtin_amdgcn_permlane32_swap(__float_as_uint(m), __float_as_uint(m), false, false);
    return fmaxf(__uint_as_float(rr[0]), __uint_as_float(rr[1]));
}
DI void attn_fallback(const char* lds, int l, bf16x8 q1, s16x4 q2, int hsel, f32x16& o) {
    const int r32 = l & 31, h = l >> 5;
    const s16x4 z4 = {0, 0, 0, 0};
    const bf16x8 q2f = cat8(h == hsel ? q2 : z4, z4);
    const bf16x8 ones = {0x3f80, 0x3f80, 0x3f80, 0x3f80, 0x3f80, 0x3f80, 0x3f80, 0x3f80};
    float m = -INFINITY;
#pragma unroll
    for (int i = 0; i < 16; ++i) o[i] = 0.f;
    for (int t = 0; t < 64; ++t) {
        const LDSP char* cb = (const LDSP char*)lds + (t >> 1) * 5120;
        const bf16x8 k1 = *(const LDSP bf16x8*)(cb + (t & 1) * 1024 + l * 16);
        const s16x4 k2 = *(const LDSP s16x4*)(cb + 2048 + r32 * 16 + (t & 1) * 8);
        f32x16 s;
#pragma unroll
        for (int i = 0; i < 16; ++i) s[i] = 0.f;
        s = MFMA32(k1, q1, s);
        s = MFMA32(cat8(k2, z4), q2f, s);
        float tm = s[0];
#pragma unroll
        for (int i = 1; i < 16; ++i) tm = fmaxf(tm, s[i]);
        tm = max_halves(tm);
        const float mn = fmaxf(m, tm), alpha = __builtin_amdgcn_exp2f(m - mn);
        m = mn;
        u32x4 p0, p1;
#pragma unroll
        for (int i = 0; i < 4; ++i) {
            p0[i] = pk_bf16(__builtin_amdgcn_exp2f(s[2 * i] - m), __builtin_amdgcn_exp2f(s[2 * i + 1] - m));
            p1[i] = pk_bf16(__builtin_amdgcn_exp2f(s[8 + 2 * i] - m), __builtin_amdgcn_exp2f(s[9 + 2 * i] - m));
        }
#pragma unroll
        for (int i = 0; i < 16; ++i) o[i] *= alpha;
        const int dv = r32 < 20 ? r32 : 0;
        bf16x8 v0 = *(const LDSP bf16x8*)(cb + 2560 + (t & 1) * 1280 + h * 320 + dv * 16);
        bf16x8 v1 = *(const LDSP bf16x8*)(cb + 2560 + (t & 1) * 1280 + 640 + h * 320 + dv * 16);
        if (r32 >= 20) { v0 = ones; v1 = ones; }
        o = MFMA32(v0, __builtin_bit_cast(bf16x8, p0), o);
        o = MFMA32(v1, __builtin_bit_cast(bf16x8, p1), o);
    }
}

DI bool attn_ok(const f32x16& o) {
    float a = fabsf(o[0]);
#pragma unroll
    for (int i = 1; i < 12; ++i) a += fabsf(o[i]);
    return (o[12] < 0x1p80f) & (a < 0x1p126f);
}

typedef __attribute__((ext_vector_type(16))) unsigned u32x16;
#define WSUB(v, i) __builtin_bit_cast(bf16x8, __builtin_shufflevector(v, v, 4 * (i), 4 * (i) + 1, 4 * (i) + 2, 4 * (i) + 3))

__global__ __launch_bounds__(512, 2) void k_attn(char* __restrict__ ws, char* __restrict__ out) {
    __shared__ __attribute__((aligned(16))) char lds[163840];
    STAMP_DECL; STAMP(0);
    const int b = blockIdx.x, xcd = b & 7, ii = b >> 3;
    const int pos = ii >> 2, head = (pos >> 2) * 32 + xcd * 4 + (pos & 3), slot = xcd * 8 + pos, qb = ii & 3;
    const int w = __builtin_amdgcn_readfirstlane(threadIdx.x >> 6);
    int l = threadIdx.x & 63, r32 = l & 31, h = l >> 5;
    const int qc = qb * 8 + w;
    const char* qp = ws + WS_Q + (size_t)head * Q_HEAD + qc * 2560;
    const char* kvp = ws + WS_KV + (size_t)(head < 32 ? 32 : head - 32) * KV_HEAD;
    const char* gp = ws + WS_G + qc * 256;
    const unsigned lds0 = (unsigned)(uintptr_t)lds;
    bf16x8 q1a, q1b; s16x4 q2; float ga, gb;
    unsigned dv = w * 1024 + l * 16, dm = __builtin_amdgcn_readfirstlane(lds0 + w * 1024);
    unsigned t0 = r32 * 16 + h * 8, t1 = r32 * 16, t2 = r32 * 4;
    const int mb = (xcd >> 1) * 8 + (ii & 7), nb = (xcd & 1) * 4 + (ii >> 3);
    const char* wp = ws + WS_WO + (size_t)(nb * 8 + w) * 25600 + 2048;
    f32x16 oa, ob; u32x16 vini;
#pragma unroll
    for (int i = 0; i < 16; ++i) { oa[i] = 0.f; ob[i] = 0.f; vini[i] = (r32 & 27) == 24 ? 0x3f803f80u : 0u; }
    unsigned ak1 = lds0 + l * 16, ak2 = lds0 + 2048 + r32 * 16, av = lds0 + 2560 + h * 320 + r32 * 16, wv = l * 16;
    unsigned cnt, inc, svm;
    u32x16 wt0, wt1, wt2, wt3, wt4;
    STAMP(2);
#define LSTAMP ""
    asm volatile(
        "global_load_dwordx4 %[q1a], %[wv], %[qp]\n\t"
        "global_load_dwordx4 %[q1b], %[wv], %[qp] offset:1024\n\t"
        "global_load_dwordx2 %[q2], v88, %[qp] offset:2048\n\t"
        "global_load_dwordx4 " KA_ ", %[wv], %[kvp]\n\t"
        "global_load_dwordx4 v[" STR(KBR) ":" STR(KBR) "+3], v89, %[kvp] offset:2048\n\t"
        "global_load_dword %[ga], v90, %[gp]\n\t"
        "global_load_dword %[gb], v90, %[gp] offset:128\n\t"
        "s_mov_b32 %[sv], m0\n\t"
        DMA1 DMA1 DMA1 DMA1
        ".irp n,24,25,26,27\n\tv_mov_b32 v[" STR(S1R) "+\\n], 0\n\t.endr\n\t"
        "s_mov_b32 %[cnt], 0\n\ts_mov_b32 %[inc], 20480\n\t"
        "s_waitcnt vmcnt(6)\n\t"
        "v_mfma_f32_32x32x4_2b_bf16 v[" STR(S0R) ":" STR(S0R) "+31], " KB0_ ", %[q2], 0\n\ts_nop 15\n\ts_nop 1\n\t"
        M2_(S0R, 0, q1a) M2_(S0R, 16, q1b)
        "s_nop 10\n\t"
        MX3(0, S0R + 0, 1) MX3(16, S0R + 16, 17) MX3(0, NMR, 3) MX3(16, NMR + 16, 19) MX3(0, NMR, 5) MX3(16, NMR + 16, 21) MX3(0, NMR, 7) MX3(16, NMR + 16, 23)
        MX3(0, NMR, 9) MX3(16, NMR + 16, 25) MX3(0, NMR, 11) MX3(16, NMR + 16, 27) MX3(0, NMR, 13) MX3(16, NMR + 16, 29) MX3(0, NMR, 14) MX3(16, NMR + 16, 30)
        "v_mov_b32 v[" STR(NMR) "+1], v[" STR(NMR) "]\n\tv_mov_b32 v[" STR(NMR) "+17], v[" STR(NMR) "+16]\n\ts_nop 1\n\t"
        "v_permlane32_swap_b32 v[" STR(NMR) "], v[" STR(NMR) "+1]\n\tv_permlane32_swap_b32 v[" STR(NMR) "+16], v[" STR(NMR) "+17]\n\ts_nop 1\n\t"
        "v_min_f32 v[" STR(NMR) "], -v[" STR(NMR) "], -v[" STR(NMR) "+1]\n\tv_min_f32 v[" STR(NMR) "+16], -v[" STR(NMR) "+16], -v[" STR(NMR) "+17]\n\t"
        ".irp n,1,2,3,4,5,6,7,8,9,10,11,12,13,14,15\n\tv_mov_b32 v[" STR(NMR) "+\\n], v[" STR(NMR) "]\n\tv_mov_b32 v[" STR(NMR) "+16+\\n], v[" STR(NMR) "+16]\n\t.endr\n\t"
        ".irp n,0,2,4,6,8,10,12,14,16,18,20,22,24,26,28,30\n\tv_pk_add_f32 v[" STR(S0R) "+\\n:" STR(S0R) "+\\n+1], v[" STR(S0R) "+\\n:" STR(S0R) "+\\n+1], v[" STR(NMR) "+\\n:" STR(NMR) "+\\n+1]\n\t.endr\n\t"
        "s_waitcnt vmcnt(0)\n\ts_barrier\n\t"
        RDKA(1024) RDV(VAR, 0)
        "1:\n\t"
        LSTAMP
        TILE(S0R, S1R, VAR, VBR, 5120, KB1_, RDKB(5120), 1280, PRIO(3))
        TILE(S1R, S0R, VBR, VAR, 5120 + 1024, KB0_, "", 5120, "")
        TILE(S0R, S1R, VAR, VBR, 10240, KB1_, RDKB(10240), 5120 + 1280, "")
        TILE(S1R, S0R, VBR, VAR, 10240 + 1024, KB0_, "", 10240, "")
        TILE(S0R, S1R, VAR, VBR, 15360, KB1_, RDKB(15360), 10240 + 1280, PRIO(2))
        TILE(S1R, S0R, VBR, VAR, 15360 + 1024, KB0_, "", 15360, "s_cmp_eq_u32 %[cnt], 7\n\ts_cselect_b32 %[inc], -143360, %[inc]\n\t")
        TILE(S0R, S1R, VAR, VBR, 0, KB1_, RDKB(0), 15360 + 1280, PRIO(1) "v_add_u32 %[ak1], %[inc], %[ak1]\n\tv_add_u32 %[ak2], %[inc], %[ak2]\n\t")
        TILE(S1R, S0R, VBR, VAR, 1024, KB0_, "", 0, PRIO(0) "v_add_u32 %[av], %[inc], %[av]\n\t")
        "s_add_u32 %[cnt], %[cnt], 1\n\t"
        "s_cmp_eq_u32 %[cnt], 1\n\ts_cbranch_scc1 2f\n\t"
        "s_cmp_eq_u32 %[cnt], 3\n\ts_cbranch_scc1 3f\n\t"
        "s_cmp_eq_u32 %[cnt], 4\n\ts_cbranch_scc1 5f\n\t"
        "s_cmp_eq_u32 %[cnt], 5\n\ts_cbranch_scc1 6f\n\t"
        "s_cmp_eq_u32 %[cnt], 6\n\ts_cbranch_scc1 8f\n\t"
        "s_cmp_lt_u32 %[cnt], 8\n\ts_cbranch_scc1 1b\n\t"
        "s_branch 4f\n\t"
        "2:\n\ts_waitcnt vmcnt(3)\n\ts_barrier\n\ts_branch 1b\n\t"
        "3:\n\ts_waitcnt vmcnt(0)\n\ts_barrier\n\t" WLD(0)
        "5:\n\t" WLD(1)
        "6:\n\t" WLD(2)
        "8:\n\t" WLD(3)
        "4:\n\ts_nop 1\n\t"
        LSTAMP
        PV_(ob, VBR, 4, S1R, 24)
        "s_mov_b32 m0, %[sv]\n\ts_waitcnt vmcnt(0) lgkmcnt(0)\n\ts_nop 15\n\ts_nop 7"
        : [oa] "+{v[24:39]}"(oa), [ob] "+{v[40:55]}"(ob), [ak1] "+v"(ak1), [ak2] "+v"(ak2), [av] "+v"(av), [dv] "+v"(dv), [wv] "+v"(wv),
          "+{v[152:167]}"(vini), "+{v88}"(t0), "+{v89}"(t1), "+{v90}"(t2),
          [q1a] "=&v"(q1a), [q1b] "=&v"(q1b), [q2] "=&v"(q2), [ga] "=&v"(ga), [gb] "=&v"(gb),
          "=&{v[176:191]}"(wt0), "=&{v[192:207]}"(wt1), "=&{v[208:223]}"(wt2), "=&{v[224:239]}"(wt3), "=&{v[240:255]}"(wt4),
          [cnt] "=&s"(cnt), [inc] "=&s"(inc), [sv] "=&s"(svm), [dm] "+s"(dm)
        : [vmask] "s"(0x000fffff000fffffull), [kvp] "s"(kvp), [wp] "s"(wp), [qp] "s"(qp), [gp] "s"(gp)
        : "memory", "scc", "v56", "v57", "v58", "v59", CL10(6), CL10(7), "v80", "v81", "v82", "v83", "v84", "v85", "v86", "v87",
          "v91", "v92", "v93", "v94", "v95", "v96", "v97", "v98", "v99", CL10(10), CL10(11), CL10(12), CL10(13), CL10(14), "v150", "v151",
          "v168", "v169", "v170", "v171", "v172", "v173", "v174", "v175"
          );
    asm volatile("v_mbcnt_lo_u32_b32 %0, -1, 0\n\tv_mbcnt_hi_u32_b32 %0, -1, %0" : "=v"(l));
    r32 = l & 31; h = l >> 5;
    STAMP(3);
    if (__builtin_expect(!__all(attn_ok(oa) & attn_ok(ob)), 0)) {
        attn_fallback(lds, l, q1a, q2, 0, oa);
        attn_fallback(lds, l, q1b, q2, 1, ob);
    }
    {
        const float fa = (head < 32 ? ga : 1.0f - ga) / oa[12], fb = (head < 32 ? gb : 1.0f - gb) / ob[12];
        char* o1 = ws + WS_O1 + (size_t)slot * 65536 + qc * 2048 + l * 16;
        char* o2 = ws + WS_O2 + (size_t)(slot >> 1) * 32768 + (qc * 64 + r32) * 16 + (slot & 1) * 8;
        u32x4 va = {pk_bf16(oa[0] * fa, oa[1] * fa), pk_bf16(oa[2] * fa, oa[3] * fa), pk_bf16(oa[4] * fa, oa[5] * fa), pk_bf16(oa[6] * fa, oa[7] * fa)};
        u32x4 vb = {pk_bf16(ob[0] * fb, ob[1] * fb), pk_bf16(ob[2] * fb, ob[3] * fb), pk_bf16(ob[4] * fb, ob[5] * fb), pk_bf16(ob[6] * fb, ob[7] * fb)};
        st16(o1, va);
        st16(o1 + 1024, vb);
        if (h == 0) {
            u32x2 ta = {pk_bf16(oa[8] * fa, oa[9] * fa), pk_bf16(oa[10] * fa, oa[11] * fa)};
            u32x2 tb = {pk_bf16(ob[8] * fb, ob[9] * fb), pk_bf16(ob[10] * fb, ob[11] * fb)};
            st8(o2, ta);
            st8(o2 + 512, tb);
        }
    }
    asm volatile("s_waitcnt vmcnt(0)\n\ts_barrier" ::: "memory");
    if (w == 0 && l < 8) __hip_atomic_fetch_add((__attribute__((address_space(1))) unsigned*)(ws + WS_CNT + ((qb * 8 + xcd) * 8 + l) * 128), 1u, __ATOMIC_RELAXED, __HIP_MEMORY_SCOPE_AGENT);
    STAMP(4);
    const int r = l & 15, g = l >> 4;
    bf16x8 wl[5], af[5][4];
    asm volatile(
        "global_load_dwordx4 %0, %5, %6 offset:-2048\n\tglobal_load_dwordx4 %1, %5, %6 offset:-1024\n\tglobal_load_dwordx4 %2, %5, %6\n\t"
        "global_load_dwordx4 %3, %5, %6 offset:1024\n\tglobal_load_dwordx4 %4, %5, %6 offset:2048\n\ts_waitcnt vmcnt(0)"
        : "=&v"(wl[0]), "=&v"(wl[1]), "=&v"(wl[2]), "=&v"(wl[3]), "=&v"(wl[4]) : "v"(wv), "s"(wp) : "memory");
    {
        const char* cp = ws + WS_CNT + (((mb >> 3) * 8 + w) * 8 + nb) * 128;
#pragma unroll 1
        for (int spin = 0; spin < 400000; ++spin) {
            unsigned v;
            asm volatile("global_load_dword %0, %1, %2 sc1\n\ts_waitcnt vmcnt(0)" : "=v"(v) : "v"(0), "s"(cp) : "memory");
            if (__builtin_amdgcn_readfirstlane(v) >= 8) break;
            __builtin_amdgcn_s_sleep(4);
        }
    }
    STAMP(5);
    {
        const char* a1 = ws + WS_O1 + (size_t)w * 8 * 65536 + mb * 2048;
        const char* a2 = ws + WS_O2 + (size_t)w * 4 * 32768 + mb * 1024;
        const unsigned o1 = (g >> 1) * 65536 + (g & 1) * 512 + r * 16, o2 = g * 32768 + r * 16;
#define ALD(s, base, off, d1, d2) asm volatile( \
            "global_load_dwordx4 %0, %4, %5 sc1\n\tglobal_load_dwordx4 %1, %4, %5 offset:" #d1 " sc1\n\t" \
            "global_load_dwordx4 %2, %4, %5 offset:" #d2 " sc1\n\tglobal_load_dwordx4 %3, %4, %5 offset:" #d1 "+" #d2 " sc1" \
            : "=&v"(af[s][0]), "=&v"(af[s][1]), "=&v"(af[s][2]), "=&v"(af[s][3]) : "v"(off), "s"(base) : "memory")
        ALD(0, a1, o1, 256, 1024); ALD(1, a1 + 131072, o1, 256, 1024); ALD(2, a1 + 2 * 131072, o1, 256, 1024); ALD(3, a1 + 3 * 131072, o1, 256, 1024);
        ALD(4, a2, o2, 256, 512);
    }
    f32x4 acc[4][5];
#pragma unroll
    for (int i = 0; i < 4; ++i)
#pragma unroll
        for (int j = 0; j < 5; ++j) acc[i][j] = f32x4{0.f, 0.f, 0.f, 0.f};
#define AWAIT(s, N) asm volatile("s_waitcnt vmcnt(" #N ")" : "+v"(af[s][0]), "+v"(af[s][1]), "+v"(af[s][2]), "+v"(af[s][3]) :: "memory")
#define OMM(s, w0, w1, w2, w3, w4) do { const bf16x8 wf_[5] = {w0, w1, w2, w3, w4}; \
        _Pragma("unroll") for (int j = 0; j < 5; ++j) _Pragma("unroll") for (int i = 0; i < 4; ++i) acc[i][j] = MFMA16(wf_[j], af[s][i], acc[i][j]); \
        __builtin_amdgcn_sched_barrier(0); } while (0)
    AWAIT(0, 16); OMM(0, WSUB(wt0, 0), WSUB(wt0, 1), WSUB(wt0, 2), WSUB(wt0, 3), WSUB(wt1, 0));
    AWAIT(1, 12); OMM(1, WSUB(wt1, 1), WSUB(wt1, 2), WSUB(wt1, 3), WSUB(wt2, 0), WSUB(wt2, 1));
    AWAIT(2, 8); OMM(2, WSUB(wt2, 2), WSUB(wt2, 3), WSUB(wt3, 0), WSUB(wt3, 1), WSUB(wt3, 2));
    AWAIT(3, 4); OMM(3, WSUB(wt3, 3), WSUB(wt4, 0), WSUB(wt4, 1), WSUB(wt4, 2), WSUB(wt4, 3));
    AWAIT(4, 0); OMM(4, wl[0], wl[1], wl[2], wl[3], wl[4]);
    STAMP(6);
    {
        char* wb = lds + w * 20480 + (g * 16 + (r ^ ((g >> 1) << 3))) * 16;
#pragma unroll
        for (int i = 0; i < 4; ++i)
#pragma unroll
            for (int j = 0; j < 5; ++j) *(f32x4*)(wb + (i * 5 + j) * 1024) = acc[i][j];
        __syncthreads();
        const int r8 = l & 7, g2 = (l >> 3) & 3;
        const char* rb = lds + ((w >> 1) * 5 + h) * 1024 + (g2 * 16 + ((8 * (w & 1) + r8) ^ ((g2 >> 1) << 3))) * 16;
        char* op = out + (size_t)(mb * 64 + 8 * w + r8) * 1280 + nb * 160 + h * 32 + g2 * 8;
#pragma unroll
        for (int p = 0; p < 3; ++p) {
            if (p < 2 || h == 0) {
                f32x4 t = *(const f32x4*)(rb + p * 2048);
#pragma unroll
                for (int u = 1; u < 8; ++u) t += *(const f32x4*)(rb + p * 2048 + u * 20480);
                u32x2 v = {pk_bf16(t[0], t[1]), pk_bf16(t[2], t[3])};
                *(u32x2*)(op + p * 64) = v;
            }
        }
    }
    STAMP(7); STAMP_FLUSH(ws, 1);
}

extern "C" void kernel_launch(void* const* d_in, const int* in_sizes, int n_in, void* d_out, int out_size, void* d_ws, size_t ws_size,
                              hipStream_t stream) {
    const char* x = (const char*)d_in[0];
    const char* wq = (const char*)d_in[1];
    const char* wk = (const char*)d_in[2];
    const char* wv = (const char*)d_in[3];
    const char* woa = (const char*)d_in[4];
    const char* wqkv = (const char*)d_in[6];
    const char* wob = (const char*)d_in[7];
    const char* gw = (const char*)d_in[8];
    char* ws = (char*)d_ws;
    k_proj<<<256, 768, 0, stream>>>(x, wq, wk, wv, wqkv, gw, woa, wob, ws);
    k_attn<<<256, 512, 0, stream>>>(ws, (char*)d_out);
}
```

```cpp
#include <hip/hip_runtime.h>
#include <hip/hip_bf16.h>
#include <stdint.h>

typedef __attribute__((ext_vector_type(8))) short bf16x8;
typedef __attribute__((ext_vector_type(4))) short s16x4;
typedef __attribute__((ext_vector_type(4))) float f32x4;
typedef __attribute__((ext_vector_type(16))) float f32x16;
typedef __attribute__((ext_vector_type(32))) float f32x32;
typedef __attribute__((ext_vector_type(2))) unsigned u32x2;
typedef __attribute__((ext_vector_type(4))) unsigned u32x4;
typedef __attribute__((ext_vector_type(2))) __bf16 bf16x2_t;
typedef __attribute__((ext_vector_type(2))) float f32x2;
#define LDSP __attribute__((address_space(3)))
#define DI __device__ __forceinline__

#define STAMP_PARAMS
#define STAMP_ARGS
#define STAMP_DECL
#define STAMP(slot)
#define STAMP_FLUSH(wsp, kid)
constexpr int SEQ = 2048, DM = 640;
constexpr size_t Q_HEAD = 32 * 2560;
constexpr size_t KV_HEAD = 32 * 5120;
constexpr size_t WS_Q = 0;
constexpr size_t WS_KV = WS_Q + 64 * Q_HEAD;
constexpr size_t WS_G = WS_KV + 33 * KV_HEAD;
constexpr size_t WS_O1 = WS_G + 2048 * 4;
constexpr size_t WS_O2 = WS_O1 + 64 * 65536;
constexpr size_t WS_WO = WS_O2 + 32 * 2048 * 16;
#define CNT_STRIDE 128
#define POLL_SLEEP 4
constexpr size_t WS_CNT = WS_WO + 1600 * 1024;

DI unsigned pk_bf16(float a, float b) {
    f32x2 v = {a, b};
    return __builtin_bit_cast(unsigned, __builtin_convertvector(v, bf16x2_t));
}
#define ST_FLAGS "sc1"
DI void st16(char* p, u32x4 v) { asm volatile("global_store_dwordx4 %0, %1, off " ST_FLAGS "\n\ts_nop 1" :: "v"(p), "v"(v) : "memory"); }
DI void st8(char* p, u32x2 v) { asm volatile("global_store_dwordx2 %0, %1, off " ST_FLAGS :: "v"(p), "v"(v) : "memory"); }
DI s16x4 tr_read(const LDSP char* p) {
    typedef short v4i16_t __attribute__((ext_vector_type(4)));
    return __builtin_amdgcn_ds_read_tr16_b64_v4i16((LDSP v4i16_t*)p);
}
DI bf16x8 cat8(s16x4 lo, s16x4 hi) { return __builtin_shufflevector(lo, hi, 0, 1, 2, 3, 4, 5, 6, 7); }
#define MFMA16(a, b, c) __builtin_amdgcn_mfma_f32_16x16x32_bf16((a), (b), (c), 0, 0, 0)
#define MFMA32(a, b, c) __builtin_amdgcn_mfma_f32_32x32x16_bf16((a), (b), (c), 0, 0, 0)

constexpr int P_SLOT = 40960;

DI void proj_loader(unsigned lds0, const char* xb, const char* wb, int ldw2, const char* eb, int ebytes, int rot,
                    const char* woa, const char* wob, char* wsp) {
    const int l = threadIdx.x & 63, w = __builtin_amdgcn_readfirstlane(threadIdx.x >> 6) - 4;
    unsigned xo[2], wo[3];
#pragma unroll
    for (int n = 0; n < 2; ++n) { const int row = 8 * (w + 8 * n) + (l >> 3); xo[n] = row * (DM * 2) + (((l & 7) ^ ((row >> 1) & 7)) << 4); }
#pragma unroll
    for (int n = 0; n < 3; ++n) { const int cid = 64 * (w + 8 * n) + l, k = (cid * 3277) >> 16, c = cid - 20 * k; wo[n] = k * ldw2 + ((c ^ (((k >> 3) & 1) << 1)) << 4); }
    const char* p5 = wb; int st5 = 64 * ldw2; unsigned long long m5 = ~0ull;
    if (w >= 4) {
        const int nbytes = ebytes - (w - 4) * 1024;
        const int nl = nbytes >= 1024 ? 64 : (nbytes > 0 ? nbytes >> 4 : 1);
        m5 = nl == 64 ? ~0ull : (1ull << nl) - 1;
        p5 = nbytes > 0 ? eb + (w - 4) * 1024 : eb; st5 = ebytes; wo[2] = l * 16;
    }
    const unsigned dw = __builtin_amdgcn_readfirstlane(lds0 + w * 1024);
    int st = rot;
#pragma unroll 1
    for (int k = -2; k < 8; ++k) {
        if (k >= 0) asm volatile("s_waitcnt vmcnt(5)\n\ts_barrier" ::: "memory");
        {
            const unsigned d0 = dw + ((k + 2) & 3) * P_SLOT;
            const char* xs = xb + st * 128; const char* ws = wb + st * 64 * ldw2; const char* es = p5 + st * st5;
            unsigned sv;
            asm volatile(
                "s_mov_b32 %[sv], m0\n\ts_mov_b32 m0, %[d0]\n\ts_nop 0\n\t"
                "global_load_lds_dwordx4 %[x0], %[xs]\n\ts_add_u32 m0, m0, 8192\n\ts_nop 0\n\t"
                "global_load_lds_dwordx4 %[x1], %[xs]\n\ts_add_u32 m0, m0, 8192\n\ts_nop 0\n\t"
                "global_load_lds_dwordx4 %[w0], %[ws]\n\ts_add_u32 m0, m0, 8192\n\ts_nop 0\n\t"
                "global_load_lds_dwordx4 %[w1], %[ws]\n\ts_add_u32 m0, m0, 8192\n\ts_mov_b64 exec, %[m5]\n\t"
                "global_load_lds_dwordx4 %[w2], %[es]\n\ts_mov_b64 exec, -1\n\ts_mov_b32 m0, %[sv]"
                : [sv] "=&s"(sv)
                : [d0] "s"(d0), [m5] "s"(m5), [xs] "s"(xs), [ws] "s"(ws), [es] "s"(es),
                  [x0] "v"(xo[0]), [x1] "v"(xo[1]), [w0] "v"(wo[0]), [w1] "v"(wo[1]), [w2] "v"(wo[2])
                : "memory", "scc");
            st = st == 9 ? 0 : st + 1;
        }
    }
    const int gw = blockIdx.x * 8 + w, fid = gw < 1600 ? gw : 1599;
    const int fj = fid % 5, fs = (fid / 5) % 5, fg = (fid / 25) & 7, fn = fid / 200, r = l & 15, g = l >> 4;
    const int br = fs < 4 ? fs >> 1 : g >> 1;
    const int row = fs < 4 ? (4 * fg + 2 * (fs & 1) + (g >> 1)) * 20 + 4 * (g & 1) : (4 * fg + 2 * (g & 1)) * 20 + 16;
    const char* p0 = (br ? wob : woa) + row * 1280 + (80 * fn + 16 * fj + r) * 2;
    const char* p1 = p0 + (fs < 4 ? 8 : 20) * 1280;
    unsigned e0, e1, e2, e3, e4, e5, e6, e7;
    asm volatile(
        "global_load_ushort %0, %8, off\n\tglobal_load_ushort %1, %8, off offset:1280\n\tglobal_load_ushort %2, %8, off offset:2560\n\tglobal_load_ushort %3, %8, off offset:3840\n\t"
        "global_load_ushort %4, %9, off\n\tglobal_load_ushort %5, %9, off offset:1280\n\tglobal_load_ushort %6, %9, off offset:2560\n\tglobal_load_ushort %7, %9, off offset:3840\n\t"
        "s_waitcnt vmcnt(13)\n\ts_barrier\n\ts_waitcnt vmcnt(8)\n\ts_barrier\n\ts_waitcnt vmcnt(0)"
        : "=&v"(e0), "=&v"(e1), "=&v"(e2), "=&v"(e3), "=&v"(e4), "=&v"(e5), "=&v"(e6), "=&v"(e7) : "v"(p0), "v"(p1) : "memory");
    if (gw < 1600) st16(wsp + WS_WO + (size_t)fid * 1024 + l * 16, u32x4{e0 | (e1 << 16), e2 | (e3 << 16), e4 | (e5 << 16), e6 | (e7 << 16)});
    if (w == 0 && l == 0) *(unsigned*)(wsp + WS_CNT + blockIdx.x * CNT_STRIDE) = 0;
}

#define STR_(x) #x
#define STR(x) STR_(x)
#define PR4(b) "v[" STR(b) ":" STR(b) "+3]"
#define PM_F(j, i, X) "v_mfma_f32_16x16x32_bf16 " PR4(20 + 16 * j + 4 * i) ", " PR4(136 + 4 * j) ", " PR4(X + 4 * i) ", " PR4(20 + 16 * j + 4 * i) "\n\t"
#define PM_T(j, i, X) "v_mfma_f32_16x16x32_bf16 " PR4(20 + 16 * j + 4 * i) ", " PR4(X + 4 * i) ", " PR4(136 + 4 * j) ", " PR4(20 + 16 * j + 4 * i) "\n\t"
#define PE_F "s_waitcnt lgkmcnt(2)\n\tv_mfma_f32_16x16x32_bf16 v[100:103], v[160:163], v[156:159], v[100:103]\n\t"
#define PE_T "s_waitcnt lgkmcnt(2)\n\tv_mfma_f32_16x16x32_bf16 v[100:103], v[156:159], v[160:163], v[100:103]\n\t"
#define PTL(j, sl, ks) "ds_read_b64_tr_b16 v[136+4*" #j ":136+4*" #j "+1], %[aw" #j "] offset:" #sl "*40960+" #ks "*10240\n\t"
#define PTH(j, sl, ks) "ds_read_b64_tr_b16 v[136+4*" #j "+2:136+4*" #j "+3], %[aw" #j "] offset:" #sl "*40960+" #ks "*10240+1280\n\t"
#define PBX(X, i, sl, ks) "ds_read_b128 " PR4(X + 4 * i) ", %[ax" #ks "] offset:" #sl "*40960+" #i "*2048\n\t"
#define PX1_0(sl, ks) ""
#define PX2_0(sl, ks) ""
#define PX3_0(sl, ks) ""
#define PX1_1(sl, ks) "ds_read_b128 v[156:159], %[ae" #ks "] offset:" #sl "*40960\n\t"
#define PX2_1(sl, ks) "ds_read_b64_tr_b16 v[160:161], %[aew] offset:" #sl "*40960+" #ks "*1280\n\t"
#define PX3_1(sl, ks) "ds_read_b64_tr_b16 v[162:163], %[aew] offset:" #sl "*40960+" #ks "*1280+160\n\t"
#define PX1_2(sl, ks) PX1_1(sl, ks)
#define PX2_2(sl, ks) "ds_read_b128 v[160:163], %[aew] offset:" #sl "*40960+" #ks "*64\n\t"
#define PX3_2(sl, ks) ""
#define PTG(a) "v_add_u32 %[" #a "], %[tg], %[" #a "]\n\t"
#define PSTEP(M, EK, PE, WB, X, Y, cs, ck, ns, nk, BAR, F1, F2, F3) \
    "s_waitcnt lgkmcnt(4)\n\t" \
    M(0, 0, X) PTL(4, cs, ck) M(0, 1, X) PTH(4, cs, ck) M(0, 2, X) BAR PBX(Y, 0, ns, nk) M(0, 3, X) PBX(Y, 1, ns, nk) \
    M(1, 0, X) PTL(0, ns, nk) M(1, 1, X) PTH(0, ns, nk) M(1, 2, X) PBX(Y, 2, ns, nk) M(1, 3, X) PX1_##EK(cs, ck) \
    "s_waitcnt lgkmcnt(" #WB ")\n\t" \
    M(2, 0, X) PTL(1, ns, nk) M(2, 1, X) PTH(1, ns, nk) M(2, 2, X) PBX(Y, 3, ns, nk) M(2, 3, X) PX2_##EK(cs, ck) \
    M(3, 0, X) PTL(2, ns, nk) M(3, 1, X) PTH(2, ns, nk) M(3, 2, X) PX3_##EK(cs, ck) M(3, 3, X) F1 \
    M(4, 0, X) PTL(3, ns, nk) M(4, 1, X) PTH(3, ns, nk) M(4, 2, X) F2 M(4, 3, X) F3 PE
#define CL10(a) "v" #a "0", "v" #a "1", "v" #a "2", "v" #a "3", "v" #a "4", "v" #a "5", "v" #a "6", "v" #a "7", "v" #a "8", "v" #a "9"
#define PLOOP(M, EK, PE, WB) \
    asm volatile( \
        "s_mov_b32 %[cnt], 0\n\ts_mov_b32 %[tg], 81920\n\t" \
        "s_barrier\n\t" \
        PBX(104, 0, 0, 0) PBX(104, 1, 0, 0) PTL(0, 0, 0) PTH(0, 0, 0) PBX(104, 2, 0, 0) PTL(1, 0, 0) PTH(1, 0, 0) PBX(104, 3, 0, 0) \
        PTL(2, 0, 0) PTH(2, 0, 0) PTL(3, 0, 0) PTH(3, 0, 0) \
        "1:\n\t" \
        PSTEP(M, EK, PE, WB, 104, 120, 0, 0, 0, 1, "", "", "", "") \
        PSTEP(M, EK, PE, WB, 120, 104, 0, 1, 1, 0, "s_barrier\n\t", PTG(ax0), "s_add_u32 %[cnt], %[cnt], 1\n\t", "") \
        PSTEP(M, EK, PE, WB, 104, 120, 1, 0, 1, 1, "", PTG(aw0) PTG(ae0), PTG(aw1) PTG(ax1), PTG(aw2)) \
        PSTEP(M, EK, PE, WB, 120, 104, 1, 1, 0, 0, "s_cmp_eq_u32 %[cnt], 5\n\ts_cbranch_scc1 2f\n\ts_barrier\n\t2:\n\t", PTG(aw3) PTG(ae1), PTG(aw4), PTG(aew)) \
        "s_sub_u32 %[tg], 0, %[tg]\n\ts_cmp_lg_u32 %[cnt], 5\n\ts_cbranch_scc1 1b\n\t" \
        "s_waitcnt lgkmcnt(0)\n\ts_nop 7" \
        : "+{v[20:35]}"(acc[0]), "+{v[36:51]}"(acc[1]), "+{v[52:67]}"(acc[2]), "+{v[68:83]}"(acc[3]), "+{v[84:99]}"(acc[4]), "+{v[100:103]}"(eacc), \
          [ax0] "+v"(ax0), [ax1] "+v"(ax1), [aw0] "+v"(aw[0]), [aw1] "+v"(aw[1]), [aw2] "+v"(aw[2]), [aw3] "+v"(aw[3]), [aw4] "+v"(aw[4]), \
          [ae0] "+v"(ae0), [ae1] "+v"(ae1), [aew] "+v"(aew), [cnt] "=&s"(cnt), [tg] "=&s"(tg) \
        : \
        : "memory", "scc", "v104", "v105", "v106", "v107", "v108", "v109", CL10(11), CL10(12), CL10(13), CL10(14), CL10(15), "v160", "v161", "v162", "v163")

template <bool VT, int EK>
DI void proj_mma(unsigned lds0, int ehi, int ets, f32x16 (&acc)[5], f32x4& eacc) {
    const int tid = threadIdx.x, l = tid & 63, w = __builtin_amdgcn_readfirstlane(tid >> 6);
    const int wm = w >> 1, wn = w & 1, r = l & 15, g = l >> 4;
    const int xsw = (g ^ ((r >> 1) & 7)) << 4;
    unsigned ax0 = lds0 + (64 * wm + r) * 128 + xsw, ax1 = lds0 + (64 * wm + r) * 128 + (xsw ^ 64);
    unsigned ae0 = lds0 + (16 * ets + r) * 128 + xsw, ae1 = lds0 + (16 * ets + r) * 128 + (xsw ^ 64);
    unsigned aw[5];
    const int q = r >> 2, p = r & 3;
#pragma unroll
    for (int j = 0; j < 5; ++j) {
        int col;
        if (j == 4) col = (4 * wn + p) * 20 + 16;
        else if (VT) col = (4 * wn + j) * 20 + 4 * p;
        else col = (4 * wn + (j & 2) + (p >> 1)) * 20 + 8 * (p & 1) + 4 * (j & 1);
        aw[j] = lds0 + 16384 + (8 * g + q) * 320 + ((col * 2) ^ ((g & 1) << 5));
    }
    unsigned aew = lds0 + (EK == 1 ? 36864 + (8 * g + q) * 40 + (ehi ? 32 : 8 * p) : 36864 + g * 16);
    unsigned cnt, tg;
    if constexpr (VT) PLOOP(PM_T, 1, PE_T, 6);
    else if constexpr (EK == 1) PLOOP(PM_F, 1, PE_F, 6);
    else if constexpr (EK == 2) PLOOP(PM_F, 2, PE_F, 6);
    else PLOOP(PM_F, 0, "", 5);
}
#define ACC(i, j) f32x4{acc[j][4 * (i)], acc[j][4 * (i) + 1], acc[j][4 * (i) + 2], acc[j][4 * (i) + 3]}

DI u32x4 pk8(const f32x4& a, const f32x4& b, float sc) {
    return u32x4{pk_bf16(a[0] * sc, a[1] * sc), pk_bf16(a[2] * sc, a[3] * sc), pk_bf16(b[0] * sc, b[1] * sc), pk_bf16(b[2] * sc, b[3] * sc)};
}

#define CODE_PROJ "_Z6k_projPKcS0_S0_S0_S0_S0_S0_S0_Pc"
#define CODE_ATTN "_Z6k_attnPcS_"
#define CODE_BYTES 9600
__global__ __launch_bounds__(768, 3) void k_proj(const char* __restrict__ x, const char* __restrict__ wq, const char* __restrict__ wk,
                                                 const char* __restrict__ wv, const char* __restrict__ wqkv, const char* __restrict__ gw,
                                                 const char* __restrict__ woa, const char* __restrict__ wob, char* __restrict__ ws) {
    __shared__ __attribute__((aligned(16))) char lds[4 * P_SLOT];
    STAMP_DECL; STAMP(0);
    const int b = blockIdx.x, xcd = b & 7, ii = b >> 3;
    const int mb = (xcd & 1) * 8 + (ii & 7), type = xcd >> 1, nq = ii >> 3;
    const int tid = threadIdx.x, l = tid & 63, w = __builtin_amdgcn_readfirstlane(tid >> 6);
    const int ekind = type == 3 ? 2 + (nq >> 1) : (nq < 2 ? (type == 2 ? 4 : type) : 5);
    if (w >= 4) {
        int rot = (ii & 7) + ((nq * 5) >> 1); rot = rot >= 10 ? rot - 10 : rot;
        proj_loader((unsigned)(uintptr_t)lds, x + (size_t)mb * 128 * DM * 2, type == 0 ? wq + nq * 320 : wqkv + (type - 1) * 1280 + nq * 320,
                    type == 0 ? 1280 : 3840, ekind < 2 ? wk : (ekind < 4 ? wv : gw), ekind < 4 ? 2560 : 128, rot, woa, wob, ws);
        return;
    }
    if (w < 2) {
        const unsigned dp = (unsigned)(uintptr_t)lds + P_SLOT - 512, off = (ii * 3 + l) * 128; unsigned sv; unsigned long long pc;
        if (w == 0) asm volatile("s_getpc_b64 s[40:41]\n\ts_add_u32 s40, s40, " CODE_PROJ "@rel32@lo+4\n\ts_addc_u32 s41, s41, " CODE_PROJ "@rel32@hi+12" : "={s[40:41]}"(pc) :: "scc");
        else asm volatile("s_getpc_b64 s[40:41]\n\ts_add_u32 s40, s40, " CODE_ATTN "@rel32@lo+4\n\ts_addc_u32 s41, s41, " CODE_ATTN "@rel32@hi+12" : "={s[40:41]}"(pc) :: "scc");
        if (l < 3 && (w == 0 || off < CODE_BYTES))
            asm volatile("s_mov_b32 %0, m0\n\ts_mov_b32 m0, %1\n\ts_nop 0\n\tglobal_load_lds_dword %2, %3\n\ts_mov_b32 m0, %0" : "=&s"(sv) : "s"(dp), "v"(off), "s"(pc) : "memory");
    }
    const int wm = w >> 1, wn = w & 1, r = l & 15, g = l >> 4;
    const int ets = 4 * (nq & 1) + w;
    f32x16 acc[5]; f32x4 eacc = {0.f, 0.f, 0.f, 0.f};
#pragma unroll
    for (int j = 0; j < 5; ++j)
#pragma unroll
        for (int i = 0; i < 16; ++i) acc[j][i] = 0.f;
    const unsigned lds0 = (unsigned)(uintptr_t)lds;
    const int chunk = mb * 2 + wm;
    char* kva = ws + WS_KV + 32 * KV_HEAD + (mb * 2 + (nq & 1)) * 5120;
    if (type == 3) {
        proj_mma<true, 1>(lds0, ekind & 1, ets, acc, eacc);
        STAMP(3);
        char* base = ws + WS_KV + (size_t)(nq * 8 + 4 * wn) * KV_HEAD + chunk * 5120 + 2560 + g * 320;
#pragma unroll
        for (int t = 0; t < 2; ++t) {
#pragma unroll
            for (int j = 0; j < 4; ++j) st16(base + (size_t)j * KV_HEAD + t * 1280 + r * 16, pk8(ACC(2 * t, j), ACC(2 * t + 1, j), 1.0f));
            st16(base + (size_t)(r >> 2) * KV_HEAD + t * 1280 + (16 + (r & 3)) * 16, pk8(ACC(2 * t, 4), ACC(2 * t + 1, 4), 1.0f));
        }
        const int d = (ekind & 1) * 16 + r;
        if (d < 20) {
            u32x2 v = {pk_bf16(eacc[0], eacc[1]), pk_bf16(eacc[2], eacc[3])};
            st8(kva + 2560 + (w >> 1) * 1280 + g * 320 + d * 16 + (w & 1) * 8, v);
        }
    } else {
        if (ekind < 4) proj_mma<false, 1>(lds0, ekind & 1, ets, acc, eacc);
        else if (ekind == 4) proj_mma<false, 2>(lds0, 0, ets, acc, eacc);
        else proj_mma<false, 0>(lds0, 0, ets, acc, eacc);
        STAMP(3);
        const float sc = type == 2 ? 1.0f : 0.32259784f;
        const size_t hstride = type == 2 ? KV_HEAD : Q_HEAD;
        const int row0 = type == 2 ? 16 * (r >> 3) + (r & 7) : r, ustep = type == 2 ? 128 : 256;
        char* base = (type == 2 ? ws + WS_KV + chunk * 5120 : ws + WS_Q + (size_t)type * 32 * Q_HEAD + chunk * 2560) + (size_t)(nq * 8 + 4 * wn) * hstride + row0 * 16;
#pragma unroll
        for (int i = 0; i < 4; ++i)
#pragma unroll
            for (int jj = 0; jj < 2; ++jj)
                st16(base + (2 * jj + (g >> 1)) * hstride + (i >> 1) * 1024 + (g & 1) * 512 + (i & 1) * ustep, pk8(ACC(i, 2 * jj), ACC(i, 2 * jj + 1), sc));
#pragma unroll
        for (int u = 0; u < 2; ++u) st16(base + g * hstride + 2048 + u * ustep, pk8(ACC(u, 4), ACC(u + 2, 4), sc));
        u32x2 v = {pk_bf16(eacc[0], eacc[1]), pk_bf16(eacc[2], eacc[3])};
        const int erow = 16 * (r >> 3) + 8 * (w & 1) + (r & 7);
        if (ekind == 0) st8(kva + (w >> 1) * 1024 + (g >> 1) * 512 + erow * 16 + (g & 1) * 8, v);
        else if (ekind == 1) { if (g == 0) st8(kva + 2048 + erow * 16 + (w >> 1) * 8, v); }
        else if (ekind == 4) { if (g == 0) ((float*)(ws + WS_G))[mb * 128 + 16 * ets + r] = 1.0f / (1.0f + __expf(-eacc[0])); }
    }
    STAMP(4); STAMP_FLUSH(ws, 0);
}

#define S0R 56
#define S1R 88
#define NMR 120
#define VAR 152
#define VBR 160
#define KAR 168
#define KBR 172
#define WFR 176
#define E_(X, i) "v_exp_f32 v[" STR(X) "+" #i "], v[" STR(X) "+" #i "]\n\t"
#define C_(X, d, a, b) "v_cvt_pk_bf16_f32 v[" STR(X) "+" #d "], v[" STR(X) "+" #a "], v[" STR(X) "+" #b "]\n\t"
#define PV_(O, V, vo, X, po) "v_mfma_f32_32x32x16_bf16 %[" #O "], v[" STR(V) "+" #vo ":" STR(V) "+" #vo "+3], v[" STR(X) "+" #po ":" STR(X) "+" #po "+3], %[" #O "]\n\t"
#define M1_(Y, KB) "v_mfma_f32_32x32x4_2b_bf16 v[" STR(Y) ":" STR(Y) "+31], " KB ", %[q2], v[" STR(NMR) ":" STR(NMR) "+31]\n\t"
#define M2_(Y, o, Q) "v_mfma_f32_32x32x16_bf16 v[" STR(Y) "+" #o ":" STR(Y) "+" #o "+15], " KA_ ", %[" #Q "], v[" STR(Y) "+" #o ":" STR(Y) "+" #o "+15]\n\t"
#define KA_ "v[" STR(KAR) ":" STR(KAR) "+3]"
#define KB0_ "v[" STR(KBR) ":" STR(KBR) "+1]"
#define KB1_ "v[" STR(KBR) "+2:" STR(KBR) "+3]"
#define RDKA(off) "ds_read_b128 " KA_ ", %[ak1] offset:" STR(off) "\n\t"
#define RDKB(off) "ds_read_b128 v[" STR(KBR) ":" STR(KBR) "+3], %[ak2] offset:" STR(off) "\n\t"
#define RDV(V, off) "s_mov_b64 exec, %[vmask]\n\tds_read_b128 v[" STR(V) ":" STR(V) "+3], %[av] offset:" STR(off) "\n\t" \
                    "ds_read_b128 v[" STR(V) "+4:" STR(V) "+7], %[av] offset:" STR(off) "+640\n\ts_mov_b64 exec, -1\n\t"
#define MX3(d, a, i) "v_max3_f32 v[" STR(NMR) "+" #d "], v[" STR(a) "], v[" STR(S0R) "+" #i "], v[" STR(S0R) "+" #i "+1]\n\t"
#define PRIO(n) "s_setprio " #n "\n\t"
#define DMA1 "s_mov_b32 m0, %[dm]\n\ts_add_u32 %[dm], %[dm], 8192\n\tglobal_load_lds_dwordx4 %[dv], %[kvp]\n\tv_add_u32 %[dv], 0x2000, %[dv]\n\t"
#define WLD1(s, j) "global_load_dwordx4 v[" STR(WFR) "+20*" #s "+4*" #j ":" STR(WFR) "+20*" #s "+4*" #j "+3], %[wv], %[wp] offset:" #j "*1024-2048\n\t"
#define WLD(s) WLD1(s, 0) WLD1(s, 1) WLD1(s, 2) WLD1(s, 3) WLD1(s, 4) "v_add_u32 %[wv], 0x1400, %[wv]\n\ts_branch 1b\n\t"
#define TILE(X, Y, VC, VP, KAOFF, KB, KBRD, VOFF, PRE) \
    PRE E_(X, 0) E_(X, 1) \
    PV_(ob, VP, 4, Y, 24) \
    E_(X, 2) E_(X, 3) C_(X, 0, 0, 1) E_(X, 4) E_(X, 5) C_(X, 1, 2, 3) \
    "s_waitcnt lgkmcnt(0)\n\t" M1_(Y, KB) KBRD \
    E_(X, 6) E_(X, 7) C_(X, 2, 4, 5) E_(X, 8) C_(X, 3, 6, 7) E_(X, 9) E_(X, 10) E_(X, 11) C_(X, 8, 8, 9) \
    PV_(oa, VC, 0, X, 0) RDV(VP, VOFF) \
    E_(X, 12) C_(X, 9, 10, 11) E_(X, 13) E_(X, 14) E_(X, 15) C_(X, 10, 12, 13) E_(X, 16) C_(X, 11, 14, 15) E_(X, 17) \
    M2_(Y, 0, q1a) \
    E_(X, 18) E_(X, 19) C_(X, 16, 16, 17) E_(X, 20) E_(X, 21) C_(X, 17, 18, 19) \
    PV_(oa, VC, 4, X, 8) \
    E_(X, 22) E_(X, 23) C_(X, 18, 20, 21) E_(X, 24) C_(X, 19, 22, 23) E_(X, 25) \
    M2_(Y, 16, q1b) RDKA(KAOFF) \
    E_(X, 26) E_(X, 27) C_(X, 24, 24, 25) E_(X, 28) E_(X, 29) C_(X, 25, 26, 27) \
    PV_(ob, VC, 0, X, 16) \
    E_(X, 30) E_(X, 31) C_(X, 26, 28, 29) C_(X, 27, 30, 31)

DI float max_halves(float m) {
    auto rr = __builtin_amdgcn_permlane32_swap(__float_as_uint(m), __float_as_uint(m), false, false);
    return fmaxf(__uint_as_float(rr[0]), __uint_as_float(rr[1]));
}
DI void attn_fallback(const char* lds, int l, bf16x8 q1, s16x4 q2, int hsel, f32x16& o) {
    const int r32 = l & 31, h = l >> 5;
    const s16x4 z4 = {0, 0, 0, 0};
    const bf16x8 q2f = cat8(h == hsel ? q2 : z4, z4);
    const bf16x8 ones = {0x3f80, 0x3f80, 0x3f80, 0x3f80, 0x3f80, 0x3f80, 0x3f80, 0x3f80};
    float m = -INFINITY;
#pragma unroll
    for (int i = 0; i < 16; ++i) o[i] = 0.f;
    for (int t = 0; t < 64; ++t) {
        const LDSP char* cb = (const LDSP char*)lds + (t >> 1) * 5120;
        const bf16x8 k1 = *(const LDSP bf16x8*)(cb + (t & 1) * 1024 + l * 16);
        const s16x4 k2 = *(const LDSP s16x4*)(cb + 2048 + r32 * 16 + (t & 1) * 8);
        f32x16 s;
#pragma unroll
        for (int i = 0; i < 16; ++i) s[i] = 0.f;
        s = MFMA32(k1, q1, s);
        s = MFMA32(cat8(k2, z4), q2f, s);
        float tm = s[0];
#pragma unroll
        for (int i = 1; i < 16; ++i) tm = fmaxf(tm, s[i]);
        tm = max_halves(tm);
        const float mn = fmaxf(m, tm), alpha = __builtin_amdgcn_exp2f(m - mn);
        m = mn;
        u32x4 p0, p1;
#pragma unroll
        for (int i = 0; i < 4; ++i) {
            p0[i] = pk_bf16(__builtin_amdgcn_exp2f(s[2 * i] - m), __builtin_amdgcn_exp2f(s[2 * i + 1] - m));
            p1[i] = pk_bf16(__builtin_amdgcn_exp2f(s[8 + 2 * i] - m), __builtin_amdgcn_exp2f(s[9 + 2 * i] - m));
        }
#pragma unroll
        for (int i = 0; i < 16; ++i) o[i] *= alpha;
        const int dv = r32 < 20 ? r32 : 0;
        bf16x8 v0 = *(const LDSP bf16x8*)(cb + 2560 + (t & 1) * 1280 + h * 320 + dv * 16);
        bf16x8 v1 = *(const LDSP bf16x8*)(cb + 2560 + (t & 1) * 1280 + 640 + h * 320 + dv * 16);
        if (r32 >= 20) { v0 = ones; v1 = ones; }
        o = MFMA32(v0, __builtin_bit_cast(bf16x8, p0), o);
        o = MFMA32(v1, __builtin_bit_cast(bf16x8, p1), o);
    }
}

DI bool attn_ok(const f32x16& o) {
    float a = fabsf(o[0]);
#pragma unroll
    for (int i = 1; i < 12; ++i) a += fabsf(o[i]);
    return (o[12] < 0x1p80f) & (a < 0x1p126f);
}

typedef __attribute__((ext_vector_type(16))) unsigned u32x16;
#define WSUB(v, i) __builtin_bit_cast(bf16x8, __builtin_shufflevector(v, v, 4 * (i), 4 * (i) + 1, 4 * (i) + 2, 4 * (i) + 3))

__global__ __launch_bounds__(512, 2) void k_attn(char* __restrict__ ws, char* __restrict__ out) {
    __shared__ __attribute__((aligned(16))) char lds[163840];
    STAMP_DECL; STAMP(0);
    const int b = blockIdx.x, xcd = b & 7, ii = b >> 3;
    const int pos = ii >> 2, head = (pos >> 2) * 32 + xcd * 4 + (pos & 3), slot = xcd * 8 + pos, qb = ii & 3;
    const int w = __builtin_amdgcn_readfirstlane(threadIdx.x >> 6);
    int l = threadIdx.x & 63, r32 = l & 31, h = l >> 5;
    const int qc = qb * 8 + w;
    const char* qp = ws + WS_Q + (size_t)head * Q_HEAD + qc * 2560;
    const char* kvp = ws + WS_KV + (size_t)(head < 32 ? 32 : head - 32) * KV_HEAD;
    const char* gp = ws + WS_G + qc * 256;
    const unsigned lds0 = (unsigned)(uintptr_t)lds;
    bf16x8 q1a, q1b; s16x4 q2; float ga, gb;
    unsigned dv = w * 1024 + l * 16, dm = __builtin_amdgcn_readfirstlane(lds0 + w * 1024);
    unsigned t0 = r32 * 16 + h * 8, t1 = r32 * 16, t2 = r32 * 4;
    const int mb = (xcd >> 1) * 8 + (ii & 7), nb = (xcd & 1) * 4 + (ii >> 3);
    const char* wp = ws + WS_WO + (size_t)(nb * 8 + w) * 25600 + 2048;
    f32x16 oa, ob; u32x16 vini;
#pragma unroll
    for (int i = 0; i < 16; ++i) { oa[i] = 0.f; ob[i] = 0.f; vini[i] = (r32 & 27) == 24 ? 0x3f803f80u : 0u; }
    unsigned ak1 = lds0 + l * 16, ak2 = lds0 + 2048 + r32 * 16, av = lds0 + 2560 + h * 320 + r32 * 16, wv = l * 16;
    unsigned cnt, inc, svm;
    u32x16 wt0, wt1, wt2, wt3, wt4;
    STAMP(2);
#define LSTAMP ""
    asm volatile(
        "global_load_dwordx4 %[q1a], %[wv], %[qp]\n\t"
        "global_load_dwordx4 %[q1b], %[wv], %[qp] offset:1024\n\t"
        "global_load_dwordx2 %[q2], v88, %[qp] offset:2048\n\t"
        "global_load_dwordx4 " KA_ ", %[wv], %[kvp]\n\t"
        "global_load_dwordx4 v[" STR(KBR) ":" STR(KBR) "+3], v89, %[kvp] offset:2048\n\t"
        "global_load_dword %[ga], v90, %[gp]\n\t"
        "global_load_dword %[gb], v90, %[gp] offset:128\n\t"
        "s_mov_b32 %[sv], m0\n\t"
        DMA1 DMA1 DMA1 DMA1
        ".irp n,24,25,26,27\n\tv_mov_b32 v[" STR(S1R) "+\\n], 0\n\t.endr\n\t"
        "s_mov_b32 %[cnt], 0\n\ts_mov_b32 %[inc], 20480\n\ts_mov_b64 vcc, -1\n\t"
        "s_waitcnt vmcnt(6)\n\t"
        "v_mfma_f32_32x32x4_2b_bf16 v[" STR(S0R) ":" STR(S0R) "+31], " KB0_ ", %[q2], 0\n\ts_nop 15\n\ts_nop 1\n\t"
        M2_(S0R, 0, q1a) M2_(S0R, 16, q1b)
        "s_nop 10\n\t"
        MX3(0, S0R + 0, 1) MX3(16, S0R + 16, 17) MX3(0, NMR, 3) MX3(16, NMR + 16, 19) MX3(0, NMR, 5) MX3(16, NMR + 16, 21) MX3(0, NMR, 7) MX3(16, NMR + 16, 23)
        MX3(0, NMR, 9) MX3(16, NMR + 16, 25) MX3(0, NMR, 11) MX3(16, NMR + 16, 27) MX3(0, NMR, 13) MX3(16, NMR + 16, 29) MX3(0, NMR, 14) MX3(16, NMR + 16, 30)
        "v_mov_b32 v[" STR(NMR) "+1], v[" STR(NMR) "]\n\tv_mov_b32 v[" STR(NMR) "+17], v[" STR(NMR) "+16]\n\ts_nop 1\n\t"
        "v_permlane32_swap_b32 v[" STR(NMR) "], v[" STR(NMR) "+1]\n\tv_permlane32_swap_b32 v[" STR(NMR) "+16], v[" STR(NMR) "+17]\n\ts_nop 1\n\t"
        "v_min_f32 v[" STR(NMR) "], -v[" STR(NMR) "], -v[" STR(NMR) "+1]\n\tv_min_f32 v[" STR(NMR) "+16], -v[" STR(NMR) "+16], -v[" STR(NMR) "+17]\n\t"
        ".irp n,1,2,3,4,5,6,7,8,9,10,11,12,13,14,15\n\tv_mov_b32 v[" STR(NMR) "+\\n], v[" STR(NMR) "]\n\tv_mov_b32 v[" STR(NMR) "+16+\\n], v[" STR(NMR) "+16]\n\t.endr\n\t"
        ".irp n,0,2,4,6,8,10,12,14,16,18,20,22,24,26,28,30\n\tv_pk_add_f32 v[" STR(S0R) "+\\n:" STR(S0R) "+\\n+1], v[" STR(S0R) "+\\n:" STR(S0R) "+\\n+1], v[" STR(NMR) "+\\n:" STR(NMR) "+\\n+1]\n\t.endr\n\t"
        "s_waitcnt vmcnt(0)\n\ts_barrier\n\t"
        RDKA(1024) RDV(VAR, 0)
        "1:\n\t"
        LSTAMP
        TILE(S0R, S1R, VAR, VBR, 5120, KB1_, RDKB(5120), 1280, PRIO(3) "s_cbranch_vccnz 11f\n\t21:\n\t")
        TILE(S1R, S0R, VBR, VAR, 5120 + 1024, KB0_, "", 5120, "s_cbranch_vccnz 12f\n\t22:\n\t")
        TILE(S0R, S1R, VAR, VBR, 10240, KB1_, RDKB(10240), 5120 + 1280, "s_cbranch_vccnz 13f\n\t23:\n\t")
        TILE(S1R, S0R, VBR, VAR, 10240 + 1024, KB0_, "", 10240, "s_cbranch_vccnz 14f\n\t24:\n\t")
        TILE(S0R, S1R, VAR, VBR, 15360, KB1_, RDKB(15360), 10240 + 1280, PRIO(2))
        TILE(S1R, S0R, VBR, VAR, 15360 + 1024, KB0_, "", 15360, "s_cmp_eq_u32 %[cnt], 7\n\ts_cselect_b32 %[inc], -143360, %[inc]\n\t")
        TILE(S0R, S1R, VAR, VBR, 0, KB1_, RDKB(0), 15360 + 1280, PRIO(1) "v_add_u32 %[ak1], %[inc], %[ak1]\n\tv_add_u32 %[ak2], %[inc], %[ak2]\n\t")
        TILE(S1R, S0R, VBR, VAR, 1024, KB0_, "", 0, PRIO(0) "v_add_u32 %[av], %[inc], %[av]\n\t")
        "s_add_u32 %[cnt], %[cnt], 1\n\t"
        "s_cmp_eq_u32 %[cnt], 1\n\ts_cbranch_scc1 2f\n\t"
        "s_cmp_eq_u32 %[cnt], 3\n\ts_cbranch_scc1 3f\n\t"
        "s_cmp_eq_u32 %[cnt], 4\n\ts_cbranch_scc1 5f\n\t"
        "s_cmp_eq_u32 %[cnt], 5\n\ts_cbranch_scc1 6f\n\t"
        "s_cmp_eq_u32 %[cnt], 6\n\ts_cbranch_scc1 8f\n\t"
        "s_cmp_lt_u32 %[cnt], 8\n\ts_cbranch_scc1 1b\n\t"
        "s_branch 4f\n\t"
        "11:\n\t" DMA1 DMA1 DMA1 DMA1 "s_branch 21b\n\t"
        "12:\n\t" DMA1 DMA1 DMA1 DMA1 "s_branch 22b\n\t"
        "13:\n\t" DMA1 DMA1 DMA1 DMA1 "s_branch 23b\n\t"
        "14:\n\t" DMA1 DMA1 DMA1 DMA1 "s_branch 24b\n\t"
        "2:\n\ts_mov_b64 vcc, 0\n\ts_waitcnt vmcnt(0)\n\ts_barrier\n\ts_branch 1b\n\t"
        "3:\n\t" WLD(0)
        "5:\n\t" WLD(1)
        "6:\n\t" WLD(2)
        "8:\n\tv_and_b32 %[dv], 0x3ff, %[wv]\n\tv_lshlrev_b32 %[dv], 3, %[dv]\n\tv_min_u32 %[dv], 0x1380, %[dv]\n\tv_add_u32 %[dv], 0x5000, %[dv]\n\t"
        "global_load_dword %[dv], %[dv], %[wp] offset:-2048\n\t" WLD(3)
        "4:\n\ts_nop 1\n\t"
        LSTAMP
        PV_(ob, VBR, 4, S1R, 24)
        "s_mov_b32 m0, %[sv]\n\ts_waitcnt vmcnt(0) lgkmcnt(0)\n\ts_nop 15\n\ts_nop 7"
        : [oa] "+{v[24:39]}"(oa), [ob] "+{v[40:55]}"(ob), [ak1] "+v"(ak1), [ak2] "+v"(ak2), [av] "+v"(av), [dv] "+v"(dv), [wv] "+v"(wv),
          "+{v[152:167]}"(vini), "+{v88}"(t0), "+{v89}"(t1), "+{v90}"(t2),
          [q1a] "=&v"(q1a), [q1b] "=&v"(q1b), [q2] "=&v"(q2), [ga] "=&v"(ga), [gb] "=&v"(gb),
          "=&{v[176:191]}"(wt0), "=&{v[192:207]}"(wt1), "=&{v[208:223]}"(wt2), "=&{v[224:239]}"(wt3), "=&{v[240:255]}"(wt4),
          [cnt] "=&s"(cnt), [inc] "=&s"(inc), [sv] "=&s"(svm), [dm] "+s"(dm)
        : [vmask] "s"(0x000fffff000fffffull), [kvp] "s"(kvp), [wp] "s"(wp), [qp] "s"(qp), [gp] "s"(gp)
        : "memory", "scc", "vcc", "v56", "v57", "v58", "v59", CL10(6), CL10(7), "v80", "v81", "v82", "v83", "v84", "v85", "v86", "v87",
          "v91", "v92", "v93", "v94", "v95", "v96", "v97", "v98", "v99", CL10(10), CL10(11), CL10(12), CL10(13), CL10(14), "v150", "v151",
          "v168", "v169", "v170", "v171", "v172", "v173", "v174", "v175"
          );
    asm volatile("v_mbcnt_lo_u32_b32 %0, -1, 0\n\tv_mbcnt_hi_u32_b32 %0, -1, %0" : "=v"(l));
    r32 = l & 31; h = l >> 5;
    STAMP(3);
    const char* cp = ws + WS_CNT + (((mb >> 3) * 8 + w) * 8 + nb) * CNT_STRIDE;
    if (__builtin_expect(!__all(attn_ok(oa) & attn_ok(ob)), 0)) {
        attn_fallback(lds, l, q1a, q2, 0, oa);
        attn_fallback(lds, l, q1b, q2, 1, ob);
    }
    bf16x8 wl[5], af[5][4]; unsigned seen;
    {
        const float fa = (head < 32 ? ga : 1.0f - ga) * __builtin_amdgcn_rcpf(oa[12]), fb = (head < 32 ? gb : 1.0f - gb) * __builtin_amdgcn_rcpf(ob[12]);
        const char* o1 = ws + WS_O1 + (size_t)slot * 65536 + qc * 2048;
        const char* o2 = ws + WS_O2 + (size_t)(slot >> 1) * 32768 + qc * 1024 + (slot & 1) * 8;
        const u32x4 va = {pk_bf16(oa[0] * fa, oa[1] * fa), pk_bf16(oa[2] * fa, oa[3] * fa), pk_bf16(oa[4] * fa, oa[5] * fa), pk_bf16(oa[6] * fa, oa[7] * fa)};
        const u32x4 vb = {pk_bf16(ob[0] * fb, ob[1] * fb), pk_bf16(ob[2] * fb, ob[3] * fb), pk_bf16(ob[4] * fb, ob[5] * fb), pk_bf16(ob[6] * fb, ob[7] * fb)};
        auto s0 = __builtin_amdgcn_permlane32_swap(pk_bf16(oa[8] * fa, oa[9] * fa), pk_bf16(ob[8] * fb, ob[9] * fb), false, false);
        auto s1 = __builtin_amdgcn_permlane32_swap(pk_bf16(oa[10] * fa, oa[11] * fa), pk_bf16(ob[10] * fb, ob[11] * fb), false, false);
        const u32x2 vt = {s0[0], s1[0]};
        asm volatile(
            "global_store_dwordx4 %[lo], %[va], %[o1] sc1\n\tglobal_store_dwordx4 %[lo], %[vb], %[o1] offset:1024 sc1\n\tglobal_store_dwordx2 %[lo], %[vt], %[o2] sc1\n\t"
            "global_load_dword %[seen], %[z], %[cp] sc1\n\t"
            "s_waitcnt vmcnt(0)\n\ts_barrier"
            : [seen] "=&v"(seen)
            : [lo] "v"(l * 16), [va] "v"(va), [vb] "v"(vb), [vt] "v"(vt), [o1] "s"(o1), [o2] "s"(o2), [z] "v"(0), [cp] "s"(cp)
            : "memory");
    }
    if (w == (xcd ^ 1) && l < 8) __hip_atomic_fetch_add((__attribute__((address_space(1))) unsigned*)(ws + WS_CNT + ((qb * 8 + xcd) * 8 + l) * CNT_STRIDE), 1u, __ATOMIC_RELAXED, __HIP_MEMORY_SCOPE_AGENT);
    STAMP(4);
    const int r = l & 15, g = l >> 4;
#pragma unroll 1
    for (int spin = 0; spin < 400000 && __builtin_amdgcn_readfirstlane(seen) < 8; ++spin) {
        if (spin) __builtin_amdgcn_s_sleep(POLL_SLEEP);
        asm volatile("global_load_dword %0, %1, %2 sc1\n\ts_waitcnt vmcnt(0)" : "=v"(seen) : "v"(0), "s"(cp) : "memory");
    }
    STAMP(5);
    {
        const char* a1 = ws + WS_O1 + (size_t)w * 8 * 65536 + mb * 2048;
        const char* a2 = ws + WS_O2 + (size_t)w * 4 * 32768 + mb * 1024;
        const unsigned o1 = (g >> 1) * 65536 + (g & 1) * 512 + r * 16, o2 = g * 32768 + r * 16;
#define ALD(s, base, off, d1, d2) asm volatile( \
            "global_load_dwordx4 %0, %4, %5 sc1\n\tglobal_load_dwordx4 %1, %4, %5 offset:" #d1 " sc1\n\t" \
            "global_load_dwordx4 %2, %4, %5 offset:" #d2 " sc1\n\tglobal_load_dwordx4 %3, %4, %5 offset:" #d1 "+" #d2 " sc1" \
            : "=&v"(af[s][0]), "=&v"(af[s][1]), "=&v"(af[s][2]), "=&v"(af[s][3]) : "v"(off), "s"(base) : "memory")
        ALD(0, a1, o1, 256, 1024); ALD(1, a1 + 131072, o1, 256, 1024); ALD(2, a1 + 2 * 131072, o1, 256, 1024); ALD(3, a1 + 3 * 131072, o1, 256, 1024);
        ALD(4, a2, o2, 256, 512);
        asm volatile(
            "global_load_dwordx4 %0, %5, %6 offset:-2048\n\tglobal_load_dwordx4 %1, %5, %6 offset:-1024\n\tglobal_load_dwordx4 %2, %5, %6\n\t"
            "global_load_dwordx4 %3, %5, %6 offset:1024\n\tglobal_load_dwordx4 %4, %5, %6 offset:2048"
            : "=&v"(wl[0]), "=&v"(wl[1]), "=&v"(wl[2]), "=&v"(wl[3]), "=&v"(wl[4]) : "v"(wv), "s"(wp) : "memory");
    }
    f32x4 acc[4][5];
#pragma unroll
    for (int i = 0; i < 4; ++i)
#pragma unroll
        for (int j = 0; j < 5; ++j) acc[i][j] = f32x4{0.f, 0.f, 0.f, 0.f};
#define AWAIT(s, N) asm volatile("s_waitcnt vmcnt(" #N ")" : "+v"(af[s][0]), "+v"(af[s][1]), "+v"(af[s][2]), "+v"(af[s][3]) :: "memory")
#define OMM(s, w0, w1, w2, w3, w4) do { const bf16x8 wf_[5] = {w0, w1, w2, w3, w4}; \
        _Pragma("unroll") for (int j = 0; j < 5; ++j) _Pragma("unroll") for (int i = 0; i < 4; ++i) acc[i][j] = MFMA16(wf_[j], af[s][i], acc[i][j]); \
        __builtin_amdgcn_sched_barrier(0); } while (0)
    AWAIT(0, 21); OMM(0, WSUB(wt0, 0), WSUB(wt0, 1), WSUB(wt0, 2), WSUB(wt0, 3), WSUB(wt1, 0));
    AWAIT(1, 17); OMM(1, WSUB(wt1, 1), WSUB(wt1, 2), WSUB(wt1, 3), WSUB(wt2, 0), WSUB(wt2, 1));
    AWAIT(2, 13); OMM(2, WSUB(wt2, 2), WSUB(wt2, 3), WSUB(wt3, 0), WSUB(wt3, 1), WSUB(wt3, 2));
    AWAIT(3, 9); OMM(3, WSUB(wt3, 3), WSUB(wt4, 0), WSUB(wt4, 1), WSUB(wt4, 2), WSUB(wt4, 3));
    AWAIT(4, 0); asm volatile("" : "+v"(wl[0]), "+v"(wl[1]), "+v"(wl[2]), "+v"(wl[3]), "+v"(wl[4])); OMM(4, wl[0], wl[1], wl[2], wl[3], wl[4]);
    STAMP(6);
    {
        char* wb = lds + w * 20480 + (g * 16 + (r ^ ((g >> 1) << 3))) * 16;
#pragma unroll
        for (int i = 0; i < 4; ++i)
#pragma unroll
            for (int j = 0; j < 5; ++j) *(f32x4*)(wb + (i * 5 + j) * 1024) = acc[i][j];
        __syncthreads();
        const int r8 = l & 7, g2 = (l >> 3) & 3;
        const char* rb = lds + ((w >> 1) * 5 + h) * 1024 + (g2 * 16 + ((8 * (w & 1) + r8) ^ ((g2 >> 1) << 3))) * 16;
        char* op = out + (size_t)(mb * 64 + 8 * w + r8) * 1280 + nb * 160 + h * 32 + g2 * 8;
#pragma unroll
        for (int p = 0; p < 3; ++p) {
            if (p < 2 || h == 0) {
                f32x4 t = *(const f32x4*)(rb + p * 2048);
#pragma unroll
                for (int u = 1; u < 8; ++u) t += *(const f32x4*)(rb + p * 2048 + u * 20480);
                u32x2 v = {pk_bf16(t[0], t[1]), pk_bf16(t[2], t[3])};
                *(u32x2*)(op + p * 64) = v;
            }
        }
    }
    STAMP(7); STAMP_FLUSH(ws, 1);
}

extern "C" void kernel_launch(void* const* d_in, const int* in_sizes, int n_in, void* d_out, int out_size, void* d_ws, size_t ws_size,
                              hipStream_t stream) {
    const char* x = (const char*)d_in[0];
    const char* wq = (const char*)d_in[1];
    const char* wk = (const char*)d_in[2];
    const char* wv = (const char*)d_in[3];
    const char* woa = (const char*)d_in[4];
    const char* wqkv = (const char*)d_in[6];
    const char* wob = (const char*)d_in[7];
    const char* gw = (const char*)d_in[8];
    char* ws = (char*)d_ws;
    k_proj<<<256, 768, 0, stream>>>(x, wq, wk, wv, wqkv, gw, woa, wob, ws);
    k_attn<<<256, 512, 0, stream>>>(ws, (char*)d_out);
}
```

```cpp
#include <hip/hip_runtime.h>
#include <hip/hip_bf16.h>
#include <stdint.h>

typedef __attribute__((ext_vector_type(8))) short bf16x8;
typedef __attribute__((ext_vector_type(4))) short s16x4;
typedef __attribute__((ext_vector_type(4))) float f32x4;
typedef __attribute__((ext_vector_type(16))) float f32x16;
typedef __attribute__((ext_vector_type(32))) float f32x32;
typedef __attribute__((ext_vector_type(2))) unsigned u32x2;
typedef __attribute__((ext_vector_type(4))) unsigned u32x4;
typedef __attribute__((ext_vector_type(2))) __bf16 bf16x2_t;
typedef __attribute__((ext_vector_type(2))) float f32x2;
#define LDSP __attribute__((address_space(3)))
#define DI __device__ __forceinline__

#define STAMP_PARAMS
#define STAMP_ARGS
#define STAMP_DECL
#define STAMP(slot)
#define STAMP_FLUSH(wsp, kid)
constexpr int SEQ = 2048, DM = 640;
constexpr size_t Q_HEAD = 32 * 2560;
constexpr size_t KV_HEAD = 32 * 5120;
constexpr size_t WS_Q = 0;
constexpr size_t WS_KV = WS_Q + 64 * Q_HEAD;
constexpr size_t WS_G = WS_KV + 33 * KV_HEAD;
constexpr size_t WS_O1 = WS_G + 2048 * 4;
constexpr size_t WS_O2 = WS_O1 + 64 * 65536;
constexpr size_t WS_WO = WS_O2 + 32 * 2048 * 16;
#define OUT_FLAGS ""
#define XT_WIN 0
#define CNT_STRIDE 128
#define POLL_SLEEP 4
constexpr size_t WS_CNT = WS_WO + 1600 * 1024;

DI unsigned pk_bf16(float a, float b) {
    f32x2 v = {a, b};
    return __builtin_bit_cast(unsigned, __builtin_convertvector(v, bf16x2_t));
}
#define ST_FLAGS "sc1"
DI void st16(char* p, u32x4 v) { asm volatile("global_store_dwordx4 %0, %1, off " ST_FLAGS "\n\ts_nop 1" :: "v"(p), "v"(v) : "memory"); }
DI void st8(char* p, u32x2 v) { asm volatile("global_store_dwordx2 %0, %1, off " ST_FLAGS :: "v"(p), "v"(v) : "memory"); }
DI s16x4 tr_read(const LDSP char* p) {
    typedef short v4i16_t __attribute__((ext_vector_type(4)));
    return __builtin_amdgcn_ds_read_tr16_b64_v4i16((LDSP v4i16_t*)p);
}
DI bf16x8 cat8(s16x4 lo, s16x4 hi) { return __builtin_shufflevector(lo, hi, 0, 1, 2, 3, 4, 5, 6, 7); }
#define MFMA16(a, b, c) __builtin_amdgcn_mfma_f32_16x16x32_bf16((a), (b), (c), 0, 0, 0)
#define MFMA32(a, b, c) __builtin_amdgcn_mfma_f32_32x32x16_bf16((a), (b), (c), 0, 0, 0)

constexpr int P_SLOT = 40960;

DI void proj_loader(unsigned lds0, const char* xb, const char* wb, int ldw2, const char* eb, int ebytes, int rot,
                    const char* woa, const char* wob, char* wsp) {
    const int l = threadIdx.x & 63, w = __builtin_amdgcn_readfirstlane(threadIdx.x >> 6) - 4;
    unsigned xo[2], wo[3];
#pragma unroll
    for (int n = 0; n < 2; ++n) { const int row = 8 * (w + 8 * n) + (l >> 3); xo[n] = row * (DM * 2) + (((l & 7) ^ ((row >> 1) & 7)) << 4); }
#pragma unroll
    for (int n = 0; n < 3; ++n) { const int cid = 64 * (w + 8 * n) + l, k = (cid * 3277) >> 16, c = cid - 20 * k; wo[n] = k * ldw2 + ((c ^ (((k >> 3) & 1) << 1)) << 4); }
    const char* p5 = wb; int st5 = 64 * ldw2; unsigned long long m5 = ~0ull;
    if (w >= 4) {
        const int nbytes = ebytes - (w - 4) * 1024;
        const int nl = nbytes >= 1024 ? 64 : (nbytes > 0 ? nbytes >> 4 : 1);
        m5 = nl == 64 ? ~0ull : (1ull << nl) - 1;
        p5 = nbytes > 0 ? eb + (w - 4) * 1024 : eb; st5 = ebytes; wo[2] = l * 16;
    }
    const unsigned dw = __builtin_amdgcn_readfirstlane(lds0 + w * 1024);
    int st = rot;
#pragma unroll 1
    for (int k = -2; k < 8; ++k) {
        if (k >= 0) asm volatile("s_waitcnt vmcnt(5)\n\ts_barrier" ::: "memory");
        {
            const unsigned d0 = dw + ((k + 2) & 3) * P_SLOT;
            const char* xs = xb + st * 128; const char* ws = wb + st * 64 * ldw2; const char* es = p5 + st * st5;
            unsigned sv;
            asm volatile(
                "s_mov_b32 %[sv], m0\n\ts_mov_b32 m0, %[d0]\n\ts_nop 0\n\t"
                "global_load_lds_dwordx4 %[x0], %[xs]\n\ts_add_u32 m0, m0, 8192\n\ts_nop 0\n\t"
                "global_load_lds_dwordx4 %[x1], %[xs]\n\ts_add_u32 m0, m0, 8192\n\ts_nop 0\n\t"
                "global_load_lds_dwordx4 %[w0], %[ws]\n\ts_add_u32 m0, m0, 8192\n\ts_nop 0\n\t"
                "global_load_lds_dwordx4 %[w1], %[ws]\n\ts_add_u32 m0, m0, 8192\n\ts_mov_b64 exec, %[m5]\n\t"
                "global_load_lds_dwordx4 %[w2], %[es]\n\ts_mov_b64 exec, -1\n\ts_mov_b32 m0, %[sv]"
                : [sv] "=&s"(sv)
                : [d0] "s"(d0), [m5] "s"(m5), [xs] "s"(xs), [ws] "s"(ws), [es] "s"(es),
                  [x0] "v"(xo[0]), [x1] "v"(xo[1]), [w0] "v"(wo[0]), [w1] "v"(wo[1]), [w2] "v"(wo[2])
                : "memory", "scc");
            st = st == 9 ? 0 : st + 1;
        }
    }
    const int gw = blockIdx.x * 8 + w, fid = gw < 1600 ? gw : 1599;
    const int fj = fid % 5, fs = (fid / 5) % 5, fg = (fid / 25) & 7, fn = fid / 200, r = l & 15, g = l >> 4;
    const int br = fs < 4 ? fs >> 1 : g >> 1;
    const int row = fs < 4 ? (4 * fg + 2 * (fs & 1) + (g >> 1)) * 20 + 4 * (g & 1) : (4 * fg + 2 * (g & 1)) * 20 + 16;
    const char* p0 = (br ? wob : woa) + row * 1280 + (80 * fn + 16 * fj + r) * 2;
    const char* p1 = p0 + (fs < 4 ? 8 : 20) * 1280;
    unsigned e0, e1, e2, e3, e4, e5, e6, e7;
    asm volatile(
        "global_load_ushort %0, %8, off\n\tglobal_load_ushort %1, %8, off offset:1280\n\tglobal_load_ushort %2, %8, off offset:2560\n\tglobal_load_ushort %3, %8, off offset:3840\n\t"
        "global_load_ushort %4, %9, off\n\tglobal_load_ushort %5, %9, off offset:1280\n\tglobal_load_ushort %6, %9, off offset:2560\n\tglobal_load_ushort %7, %9, off offset:3840\n\t"
        "s_waitcnt vmcnt(13)\n\ts_barrier\n\ts_waitcnt vmcnt(8)\n\ts_barrier\n\ts_waitcnt vmcnt(0)"
        : "=&v"(e0), "=&v"(e1), "=&v"(e2), "=&v"(e3), "=&v"(e4), "=&v"(e5), "=&v"(e6), "=&v"(e7) : "v"(p0), "v"(p1) : "memory");
    if (gw < 1600) st16(wsp + WS_WO + (size_t)fid * 1024 + l * 16, u32x4{e0 | (e1 << 16), e2 | (e3 << 16), e4 | (e5 << 16), e6 | (e7 << 16)});
    if (w == 0 && l == 0) *(unsigned*)(wsp + WS_CNT + blockIdx.x * CNT_STRIDE) = 0;
}

#define STR_(x) #x
#define STR(x) STR_(x)
#define PR4(b) "v[" STR(b) ":" STR(b) "+3]"
#define PM_F(j, i, X) "v_mfma_f32_16x16x32_bf16 " PR4(20 + 16 * j + 4 * i) ", " PR4(136 + 4 * j) ", " PR4(X + 4 * i) ", " PR4(20 + 16 * j + 4 * i) "\n\t"
#define PM_T(j, i, X) "v_mfma_f32_16x16x32_bf16 " PR4(20 + 16 * j + 4 * i) ", " PR4(X + 4 * i) ", " PR4(136 + 4 * j) ", " PR4(20 + 16 * j + 4 * i) "\n\t"
#define PE_F "s_waitcnt lgkmcnt(2)\n\tv_mfma_f32_16x16x32_bf16 v[100:103], v[160:163], v[156:159], v[100:103]\n\t"
#define PE_T "s_waitcnt lgkmcnt(2)\n\tv_mfma_f32_16x16x32_bf16 v[100:103], v[156:159], v[160:163], v[100:103]\n\t"
#define PTL(j, sl, ks) "ds_read_b64_tr_b16 v[136+4*" #j ":136+4*" #j "+1], %[aw" #j "] offset:" #sl "*40960+" #ks "*10240\n\t"
#define PTH(j, sl, ks) "ds_read_b64_tr_b16 v[136+4*" #j "+2:136+4*" #j "+3], %[aw" #j "] offset:" #sl "*40960+" #ks "*10240+1280\n\t"
#define PBX(X, i, sl, ks) "ds_read_b128 " PR4(X + 4 * i) ", %[ax" #ks "] offset:" #sl "*40960+" #i "*2048\n\t"
#define PX1_0(sl, ks) ""
#define PX2_0(sl, ks) ""
#define PX3_0(sl, ks) ""
#define PX1_1(sl, ks) "ds_read_b128 v[156:159], %[ae" #ks "] offset:" #sl "*40960\n\t"
#define PX2_1(sl, ks) "ds_read_b64_tr_b16 v[160:161], %[aew] offset:" #sl "*40960+" #ks "*1280\n\t"
#define PX3_1(sl, ks) "ds_read_b64_tr_b16 v[162:163], %[aew] offset:" #sl "*40960+" #ks "*1280+160\n\t"
#define PX1_2(sl, ks) PX1_1(sl, ks)
#define PX2_2(sl, ks) "ds_read_b128 v[160:163], %[aew] offset:" #sl "*40960+" #ks "*64\n\t"
#define PX3_2(sl, ks) ""
#define PTG(a) "v_add_u32 %[" #a "], %[tg], %[" #a "]\n\t"
#define PSTEP(M, EK, PE, WB, X, Y, cs, ck, ns, nk, BAR, F1, F2, F3) \
    "s_waitcnt lgkmcnt(4)\n\t" \
    M(0, 0, X) PTL(4, cs, ck) M(0, 1, X) PTH(4, cs, ck) M(0, 2, X) BAR PBX(Y, 0, ns, nk) M(0, 3, X) PBX(Y, 1, ns, nk) \
    M(1, 0, X) PTL(0, ns, nk) M(1, 1, X) PTH(0, ns, nk) M(1, 2, X) PBX(Y, 2, ns, nk) M(1, 3, X) PX1_##EK(cs, ck) \
    "s_waitcnt lgkmcnt(" #WB ")\n\t" \
    M(2, 0, X) PTL(1, ns, nk) M(2, 1, X) PTH(1, ns, nk) M(2, 2, X) PBX(Y, 3, ns, nk) M(2, 3, X) PX2_##EK(cs, ck) \
    M(3, 0, X) PTL(2, ns, nk) M(3, 1, X) PTH(2, ns, nk) M(3, 2, X) PX3_##EK(cs, ck) M(3, 3, X) F1 \
    M(4, 0, X) PTL(3, ns, nk) M(4, 1, X) PTH(3, ns, nk) M(4, 2, X) F2 M(4, 3, X) F3 PE
#define CL10(a) "v" #a "0", "v" #a "1", "v" #a "2", "v" #a "3", "v" #a "4", "v" #a "5", "v" #a "6", "v" #a "7", "v" #a "8", "v" #a "9"
#define PLOOP(M, EK, PE, WB) \
    asm volatile( \
        "s_mov_b32 %[cnt], 0\n\ts_mov_b32 %[tg], 81920\n\t" \
        "s_barrier\n\t" \
        PBX(104, 0, 0, 0) PBX(104, 1, 0, 0) PTL(0, 0, 0) PTH(0, 0, 0) PBX(104, 2, 0, 0) PTL(1, 0, 0) PTH(1, 0, 0) PBX(104, 3, 0, 0) \
        PTL(2, 0, 0) PTH(2, 0, 0) PTL(3, 0, 0) PTH(3, 0, 0) \
        "1:\n\t" \
        PSTEP(M, EK, PE, WB, 104, 120, 0, 0, 0, 1, "", "", "", "") \
        PSTEP(M, EK, PE, WB, 120, 104, 0, 1, 1, 0, "s_barrier\n\t", PTG(ax0), "s_add_u32 %[cnt], %[cnt], 1\n\t", "") \
        PSTEP(M, EK, PE, WB, 104, 120, 1, 0, 1, 1, "", PTG(aw0) PTG(ae0), PTG(aw1) PTG(ax1), PTG(aw2)) \
        PSTEP(M, EK, PE, WB, 120, 104, 1, 1, 0, 0, "s_cmp_eq_u32 %[cnt], 5\n\ts_cbranch_scc1 2f\n\ts_barrier\n\t2:\n\t", PTG(aw3) PTG(ae1), PTG(aw4), PTG(aew)) \
        "s_sub_u32 %[tg], 0, %[tg]\n\ts_cmp_lg_u32 %[cnt], 5\n\ts_cbranch_scc1 1b\n\t" \
        "s_waitcnt lgkmcnt(0)\n\ts_nop 7" \
        : "+{v[20:35]}"(acc[0]), "+{v[36:51]}"(acc[1]), "+{v[52:67]}"(acc[2]), "+{v[68:83]}"(acc[3]), "+{v[84:99]}"(acc[4]), "+{v[100:103]}"(eacc), \
          [ax0] "+v"(ax0), [ax1] "+v"(ax1), [aw0] "+v"(aw[0]), [aw1] "+v"(aw[1]), [aw2] "+v"(aw[2]), [aw3] "+v"(aw[3]), [aw4] "+v"(aw[4]), \
          [ae0] "+v"(ae0), [ae1] "+v"(ae1), [aew] "+v"(aew), [cnt] "=&s"(cnt), [tg] "=&s"(tg) \
        : \
        : "memory", "scc", "v104", "v105", "v106", "v107", "v108", "v109", CL10(11), CL10(12), CL10(13), CL10(14), CL10(15), "v160", "v161", "v162", "v163")

template <bool VT, int EK>
DI void proj_mma(unsigned lds0, int ehi, int ets, f32x16 (&acc)[5], f32x4& eacc) {
    const int tid = threadIdx.x, l = tid & 63, w = __builtin_amdgcn_readfirstlane(tid >> 6);
    const int wm = w >> 1, wn = w & 1, r = l & 15, g = l >> 4;
    const int xsw = (g ^ ((r >> 1) & 7)) << 4;
    unsigned ax0 = lds0 + (64 * wm + r) * 128 + xsw, ax1 = lds0 + (64 * wm + r) * 128 + (xsw ^ 64);
    unsigned ae0 = lds0 + (16 * ets + r) * 128 + xsw, ae1 = lds0 + (16 * ets + r) * 128 + (xsw ^ 64);
    unsigned aw[5];
    const int q = r >> 2, p = r & 3;
#pragma unroll
    for (int j = 0; j < 5; ++j) {
        int col;
        if (j == 4) col = (4 * wn + p) * 20 + 16;
        else if (VT) col = (4 * wn + j) * 20 + 4 * p;
        else col = (4 * wn + (j & 2) + (p >> 1)) * 20 + 8 * (p & 1) + 4 * (j & 1);
        aw[j] = lds0 + 16384 + (8 * g + q) * 320 + ((col * 2) ^ ((g & 1) << 5));
    }
    unsigned aew = lds0 + (EK == 1 ? 36864 + (8 * g + q) * 40 + (ehi ? 32 : 8 * p) : 36864 + g * 16);
    unsigned cnt, tg;
    if constexpr (VT) PLOOP(PM_T, 1, PE_T, 6);
    else if constexpr (EK == 1) PLOOP(PM_F, 1, PE_F, 6);
    else if constexpr (EK == 2) PLOOP(PM_F, 2, PE_F, 6);
    else PLOOP(PM_F, 0, "", 5);
}
#define ACC(i, j) f32x4{acc[j][4 * (i)], acc[j][4 * (i) + 1], acc[j][4 * (i) + 2], acc[j][4 * (i) + 3]}

DI u32x4 pk8(const f32x4& a, const f32x4& b, float sc) {
    return u32x4{pk_bf16(a[0] * sc, a[1] * sc), pk_bf16(a[2] * sc, a[3] * sc), pk_bf16(b[0] * sc, b[1] * sc), pk_bf16(b[2] * sc, b[3] * sc)};
}

#define CODE_PROJ "_Z6k_projPKcS0_S0_S0_S0_S0_S0_S0_Pc"
#define CODE_ATTN "_Z6k_attnPcS_"
#define CODE_BYTES 8192
__global__ __launch_bounds__(768, 3) void k_proj(const char* __restrict__ x, const char* __restrict__ wq, const char* __restrict__ wk,
                                                 const char* __restrict__ wv, const char* __restrict__ wqkv, const char* __restrict__ gw,
                                                 const char* __restrict__ woa, const char* __restrict__ wob, char* __restrict__ ws) {
    __shared__ __attribute__((aligned(16))) char lds[4 * P_SLOT];
    STAMP_DECL; STAMP(0);
    const int b = blockIdx.x, xcd = b & 7, ii = b >> 3;
    const int mb = (xcd & 1) * 8 + (ii & 7), type = xcd >> 1, nq = ii >> 3;
    const int tid = threadIdx.x, l = tid & 63, w = __builtin_amdgcn_readfirstlane(tid >> 6);
    const int ekind = type == 3 ? 2 + (nq >> 1) : (nq < 2 ? (type == 2 ? 4 : type) : 5);
    if (w >= 4) {
        int rot = (ii & 7) + ((nq * 5) >> 1); rot = rot >= 10 ? rot - 10 : rot;
        proj_loader((unsigned)(uintptr_t)lds, x + (size_t)mb * 128 * DM * 2, type == 0 ? wq + nq * 320 : wqkv + (type - 1) * 1280 + nq * 320,
                    type == 0 ? 1280 : 3840, ekind < 2 ? wk : (ekind < 4 ? wv : gw), ekind < 4 ? 2560 : 128, rot, woa, wob, ws);
        return;
    }
    if (w < 2) {
        const unsigned dp = (unsigned)(uintptr_t)lds + P_SLOT - 512, off = (ii * 3 + l) * 128; unsigned sv; unsigned long long pc;
        if (w == 0) asm volatile("s_getpc_b64 s[40:41]\n\ts_add_u32 s40, s40, " CODE_PROJ "@rel32@lo+4\n\ts_addc_u32 s41, s41, " CODE_PROJ "@rel32@hi+12" : "={s[40:41]}"(pc) :: "scc");
        else asm volatile("s_getpc_b64 s[40:41]\n\ts_add_u32 s40, s40, " CODE_ATTN "@rel32@lo+4\n\ts_addc_u32 s41, s41, " CODE_ATTN "@rel32@hi+12" : "={s[40:41]}"(pc) :: "scc");
        if (l < 3 && (w == 0 || off < CODE_BYTES))
            asm volatile("s_mov_b32 %0, m0\n\ts_mov_b32 m0, %1\n\ts_nop 0\n\tglobal_load_lds_dword %2, %3\n\ts_mov_b32 m0, %0" : "=&s"(sv) : "s"(dp), "v"(off), "s"(pc) : "memory");
    }
    const int wm = w >> 1, wn = w & 1, r = l & 15, g = l >> 4;
    const int ets = 4 * (nq & 1) + w;
    f32x16 acc[5]; f32x4 eacc = {0.f, 0.f, 0.f, 0.f};
#pragma unroll
    for (int j = 0; j < 5; ++j)
#pragma unroll
        for (int i = 0; i < 16; ++i) acc[j][i] = 0.f;
    const unsigned lds0 = (unsigned)(uintptr_t)lds;
    const int chunk = mb * 2 + wm;
    char* kva = ws + WS_KV + 32 * KV_HEAD + (mb * 2 + (nq & 1)) * 5120;
    if (type == 3) {
        proj_mma<true, 1>(lds0, ekind & 1, ets, acc, eacc);
        STAMP(3);
        char* base = ws + WS_KV + (size_t)(nq * 8 + 4 * wn) * KV_HEAD + chunk * 5120 + 2560 + g * 320;
#pragma unroll
        for (int t = 0; t < 2; ++t) {
#pragma unroll
            for (int j = 0; j < 4; ++j) st16(base + (size_t)j * KV_HEAD + t * 1280 + r * 16, pk8(ACC(2 * t, j), ACC(2 * t + 1, j), 1.0f));
            st16(base + (size_t)(r >> 2) * KV_HEAD + t * 1280 + (16 + (r & 3)) * 16, pk8(ACC(2 * t, 4), ACC(2 * t + 1, 4), 1.0f));
        }
        const int d = (ekind & 1) * 16 + r;
        if (d < 20) {
            u32x2 v = {pk_bf16(eacc[0], eacc[1]), pk_bf16(eacc[2], eacc[3])};
            st8(kva + 2560 + (w >> 1) * 1280 + g * 320 + d * 16 + (w & 1) * 8, v);
        }
    } else {
        if (ekind < 4) proj_mma<false, 1>(lds0, ekind & 1, ets, acc, eacc);
        else if (ekind == 4) proj_mma<false, 2>(lds0, 0, ets, acc, eacc);
        else proj_mma<false, 0>(lds0, 0, ets, acc, eacc);
        STAMP(3);
        const float sc = type == 2 ? 1.0f : 0.32259784f;
        const size_t hstride = type == 2 ? KV_HEAD : Q_HEAD;
        const int row0 = type == 2 ? 16 * (r >> 3) + (r & 7) : r, ustep = type == 2 ? 128 : 256;
        char* base = (type == 2 ? ws + WS_KV + chunk * 5120 : ws + WS_Q + (size_t)type * 32 * Q_HEAD + chunk * 2560) + (size_t)(nq * 8 + 4 * wn) * hstride + row0 * 16;
#pragma unroll
        for (int i = 0; i < 4; ++i)
#pragma unroll
            for (int jj = 0; jj < 2; ++jj)
                st16(base + (2 * jj + (g >> 1)) * hstride + (i >> 1) * 1024 + (g & 1) * 512 + (i & 1) * ustep, pk8(ACC(i, 2 * jj), ACC(i, 2 * jj + 1), sc));
#pragma unroll
        for (int u = 0; u < 2; ++u) st16(base + g * hstride + 2048 + u * ustep, pk8(ACC(u, 4), ACC(u + 2, 4), sc));
        u32x2 v = {pk_bf16(eacc[0], eacc[1]), pk_bf16(eacc[2], eacc[3])};
        const int erow = 16 * (r >> 3) + 8 * (w & 1) + (r & 7);
        if (ekind == 0) st8(kva + (w >> 1) * 1024 + (g >> 1) * 512 + erow * 16 + (g & 1) * 8, v);
        else if (ekind == 1) { if (g == 0) st8(kva + 2048 + erow * 16 + (w >> 1) * 8, v); }
        else if (ekind == 4) { if (g == 0) ((float*)(ws + WS_G))[mb * 128 + 16 * ets + r] = 1.0f / (1.0f + __expf(-eacc[0])); }
    }
    STAMP(4); STAMP_FLUSH(ws, 0);
}

#define S0R 56
#define S1R 88
#define NMR 120
#define VAR 152
#define VBR 160
#define KAR 168
#define KBR 172
#define WFR 176
#define E_(X, i) "v_exp_f32 v[" STR(X) "+" #i "], v[" STR(X) "+" #i "]\n\t"
#define C_(X, d, a, b) "v_cvt_pk_bf16_f32 v[" STR(X) "+" #d "], v[" STR(X) "+" #a "], v[" STR(X) "+" #b "]\n\t"
#define PV_(O, V, vo, X, po) "v_mfma_f32_32x32x16_bf16 %[" #O "], v[" STR(V) "+" #vo ":" STR(V) "+" #vo "+3], v[" STR(X) "+" #po ":" STR(X) "+" #po "+3], %[" #O "]\n\t"
#define M1_(Y, KB) "v_mfma_f32_32x32x4_2b_bf16 v[" STR(Y) ":" STR(Y) "+31], " KB ", %[q2], v[" STR(NMR) ":" STR(NMR) "+31]\n\t"
#define M2_(Y, o, Q) "v_mfma_f32_32x32x16_bf16 v[" STR(Y) "+" #o ":" STR(Y) "+" #o "+15], " KA_ ", %[" #Q "], v[" STR(Y) "+" #o ":" STR(Y) "+" #o "+15]\n\t"
#define KA_ "v[" STR(KAR) ":" STR(KAR) "+3]"
#define KB0_ "v[" STR(KBR) ":" STR(KBR) "+1]"
#define KB1_ "v[" STR(KBR) "+2:" STR(KBR) "+3]"
#define RDKA(off) "ds_read_b128 " KA_ ", %[ak1] offset:" STR(off) "\n\t"
#define RDKB(off) "ds_read_b128 v[" STR(KBR) ":" STR(KBR) "+3], %[ak2] offset:" STR(off) "\n\t"
#define RDV(V, off) "s_mov_b64 exec, %[vmask]\n\tds_read_b128 v[" STR(V) ":" STR(V) "+3], %[av] offset:" STR(off) "\n\t" \
                    "ds_read_b128 v[" STR(V) "+4:" STR(V) "+7], %[av] offset:" STR(off) "+640\n\ts_mov_b64 exec, -1\n\t"
#define MX3(d, a, i) "v_max3_f32 v[" STR(NMR) "+" #d "], v[" STR(a) "], v[" STR(S0R) "+" #i "], v[" STR(S0R) "+" #i "+1]\n\t"
#define PRIO(n) "s_setprio " #n "\n\t"
#define DMA1 "s_mov_b32 m0, %[dm]\n\ts_add_u32 %[dm], %[dm], 8192\n\tglobal_load_lds_dwordx4 %[dv], %[kvp]\n\tv_add_u32 %[dv], 0x2000, %[dv]\n\t"
#define WLD1(s, j) "global_load_dwordx4 v[" STR(WFR) "+20*" #s "+4*" #j ":" STR(WFR) "+20*" #s "+4*" #j "+3], %[wv], %[wp] offset:" #j "*1024-2048\n\t"
#define WLD(s) WLD1(s, 0) WLD1(s, 1) WLD1(s, 2) WLD1(s, 3) WLD1(s, 4) "v_add_u32 %[wv], 0x1400, %[wv]\n\ts_branch 1b\n\t"
#define TILE(X, Y, VC, VP, KAOFF, KB, KBRD, VOFF, PRE) \
    PRE E_(X, 0) E_(X, 1) \
    PV_(ob, VP, 4, Y, 24) \
    E_(X, 2) E_(X, 3) C_(X, 0, 0, 1) E_(X, 4) E_(X, 5) C_(X, 1, 2, 3) \
    "s_waitcnt lgkmcnt(0)\n\t" M1_(Y, KB) KBRD \
    E_(X, 6) E_(X, 7) C_(X, 2, 4, 5) E_(X, 8) C_(X, 3, 6, 7) E_(X, 9) E_(X, 10) E_(X, 11) C_(X, 8, 8, 9) \
    PV_(oa, VC, 0, X, 0) RDV(VP, VOFF) \
    E_(X, 12) C_(X, 9, 10, 11) E_(X, 13) E_(X, 14) E_(X, 15) C_(X, 10, 12, 13) E_(X, 16) C_(X, 11, 14, 15) E_(X, 17) \
    M2_(Y, 0, q1a) \
    E_(X, 18) E_(X, 19) C_(X, 16, 16, 17) E_(X, 20) E_(X, 21) C_(X, 17, 18, 19) \
    PV_(oa, VC, 4, X, 8) \
    E_(X, 22) E_(X, 23) C_(X, 18, 20, 21) E_(X, 24) C_(X, 19, 22, 23) E_(X, 25) \
    M2_(Y, 16, q1b) RDKA(KAOFF) \
    E_(X, 26) E_(X, 27) C_(X, 24, 24, 25) E_(X, 28) E_(X, 29) C_(X, 25, 26, 27) \
    PV_(ob, VC, 0, X, 16) \
    E_(X, 30) E_(X, 31) C_(X, 26, 28, 29) C_(X, 27, 30, 31)

DI float max_halves(float m) {
    auto rr = __builtin_amdgcn_permlane32_swap(__float_as_uint(m), __float_as_uint(m), false, false);
    return fmaxf(__uint_as_float(rr[0]), __uint_as_float(rr[1]));
}
DI void attn_fallback(const char* lds, int l, const char* qp, int tile, f32x16& o) {
    const int r32 = l & 31, h = l >> 5;
    const s16x4 z4 = {0, 0, 0, 0};
    const bf16x8 q1 = *(const bf16x8*)(qp + tile * 1024 + l * 16);
    const s16x4 q2 = *(const s16x4*)(qp + 2048 + r32 * 16 + tile * 8);
    const bf16x8 q2f = cat8(h == 0 ? q2 : z4, z4);
    const bf16x8 ones = {0x3f80, 0x3f80, 0x3f80, 0x3f80, 0x3f80, 0x3f80, 0x3f80, 0x3f80};
    float m = -INFINITY;
#pragma unroll
    for (int i = 0; i < 16; ++i) o[i] = 0.f;
#pragma unroll 1
    for (int t = 0; t < 64; ++t) {
        const LDSP char* cb = (const LDSP char*)lds + (t >> 1) * 5120;
        const bf16x8 k1 = *(const LDSP bf16x8*)(cb + (t & 1) * 1024 + l * 16);
        const s16x4 k2 = *(const LDSP s16x4*)(cb + 2048 + r32 * 16 + (t & 1) * 8);
        f32x16 s;
#pragma unroll
        for (int i = 0; i < 16; ++i) s[i] = 0.f;
        s = MFMA32(k1, q1, s);
        s = MFMA32(cat8(k2, z4), q2f, s);
        float tm = s[0];
#pragma unroll
        for (int i = 1; i < 16; ++i) tm = fmaxf(tm, s[i]);
        tm = max_halves(tm);
        const float mn = fmaxf(m, tm), alpha = __builtin_amdgcn_exp2f(m - mn);
        m = mn;
        u32x4 p0, p1;
#pragma unroll
        for (int i = 0; i < 4; ++i) {
            p0[i] = pk_bf16(__builtin_amdgcn_exp2f(s[2 * i] - m), __builtin_amdgcn_exp2f(s[2 * i + 1] - m));
            p1[i] = pk_bf16(__builtin_amdgcn_exp2f(s[8 + 2 * i] - m), __builtin_amdgcn_exp2f(s[9 + 2 * i] - m));
        }
#pragma unroll
        for (int i = 0; i < 16; ++i) o[i] *= alpha;
        const int dv = r32 < 20 ? r32 : 0;
        bf16x8 v0 = *(const LDSP bf16x8*)(cb + 2560 + (t & 1) * 1280 + h * 320 + dv * 16);
        bf16x8 v1 = *(const LDSP bf16x8*)(cb + 2560 + (t & 1) * 1280 + 640 + h * 320 + dv * 16);
        if (r32 >= 20) { v0 = ones; v1 = ones; }
        o = MFMA32(v0, __builtin_bit_cast(bf16x8, p0), o);
        o = MFMA32(v1, __builtin_bit_cast(bf16x8, p1), o);
    }
}

DI bool attn_ok(const f32x16& a, const f32x16& b) {
    f32x2 s = {0.f, 0.f};
#pragma unroll
    for (int i = 0; i < 12; i += 2) s += f32x2{a[i], a[i + 1]} + f32x2{b[i], b[i + 1]};
    return (fmaxf(a[12], b[12]) < 0x1p80f) & (fabsf(s[0] + s[1] + a[12] + b[12]) < 0x1p126f);
}
DI void attn_pack(const f32x16& oa, const f32x16& ob, float ga, float gb, u32x4& va, u32x4& vb, u32x2& vt) {
    const float fa = ga * __builtin_amdgcn_rcpf(oa[12]), fb = gb * __builtin_amdgcn_rcpf(ob[12]);
    va = u32x4{pk_bf16(oa[0] * fa, oa[1] * fa), pk_bf16(oa[2] * fa, oa[3] * fa), pk_bf16(oa[4] * fa, oa[5] * fa), pk_bf16(oa[6] * fa, oa[7] * fa)};
    vb = u32x4{pk_bf16(ob[0] * fb, ob[1] * fb), pk_bf16(ob[2] * fb, ob[3] * fb), pk_bf16(ob[4] * fb, ob[5] * fb), pk_bf16(ob[6] * fb, ob[7] * fb)};
    auto s0 = __builtin_amdgcn_permlane32_swap(pk_bf16(oa[8] * fa, oa[9] * fa), pk_bf16(ob[8] * fb, ob[9] * fb), false, false);
    auto s1 = __builtin_amdgcn_permlane32_swap(pk_bf16(oa[10] * fa, oa[11] * fa), pk_bf16(ob[10] * fb, ob[11] * fb), false, false);
    vt = u32x2{s0[0], s1[0]};
}

typedef __attribute__((ext_vector_type(16))) unsigned u32x16;
#define WSUB(v, i) __builtin_bit_cast(bf16x8, __builtin_shufflevector(v, v, 4 * (i), 4 * (i) + 1, 4 * (i) + 2, 4 * (i) + 3))

__global__ __launch_bounds__(512, 2) void k_attn(char* __restrict__ ws, char* __restrict__ out) {
    __shared__ __attribute__((aligned(16))) char lds[163840];
    STAMP_DECL; STAMP(0);
    const int b = blockIdx.x, xcd = b & 7, ii = b >> 3;
    const int pos = ii >> 2, head = (pos >> 2) * 32 + xcd * 4 + (pos & 3), slot = xcd * 8 + pos, qb = ii & 3;
    const int w = __builtin_amdgcn_readfirstlane(threadIdx.x >> 6);
    int l = threadIdx.x & 63, r32 = l & 31, h = l >> 5;
    const int qc = qb * 8 + w;
    const char* qp = ws + WS_Q + (size_t)head * Q_HEAD + qc * 2560;
    const char* kvp = ws + WS_KV + (size_t)(head < 32 ? 32 : head - 32) * KV_HEAD;
    const char* gp = ws + WS_G + qc * 256;
    const unsigned lds0 = (unsigned)(uintptr_t)lds;
    bf16x8 q1a, q1b; s16x4 q2; float ga, gb;
    unsigned dv = w * 1024 + l * 16, dm = __builtin_amdgcn_readfirstlane(lds0 + w * 1024);
    unsigned t0 = r32 * 16 + h * 8, t1 = r32 * 16, t2 = r32 * 4;
    const int mb = xcd * 4 + (ii & 3), nb = ii >> 2;
    const char* wp = ws + WS_WO + (size_t)(nb * 8 + w) * 25600 + 2048;
    const char* cp = ws + WS_CNT + (((mb >> 3) * 8 + w) * 8 + nb) * CNT_STRIDE;
    const char* sgp = ws + WS_CNT + (qb * 8 + xcd) * 8 * CNT_STRIDE;
    const char* a1 = ws + WS_O1 + (size_t)w * 8 * 65536 + mb * 2048;
    const char* a2 = ws + WS_O2 + (size_t)w * 4 * 32768 + mb * 1024;
    const char* op = out + (size_t)mb * 64 * 1280 + nb * 160;
    const char* po1 = ws + WS_O1 + (size_t)slot * 65536 + qc * 2048;
    const char* po2 = ws + WS_O2 + (size_t)(slot >> 1) * 32768 + qc * 1024 + (slot & 1) * 8;
    f32x16 oa, ob; u32x16 vini;
#pragma unroll
    for (int i = 0; i < 16; ++i) { oa[i] = 0.f; ob[i] = 0.f; vini[i] = (r32 & 27) == 24 ? 0x3f803f80u : 0u; }
    unsigned ak1 = lds0 + l * 16, ak2 = lds0 + 2048 + r32 * 16, av = lds0 + 2560 + h * 320 + r32 * 16, wv = l * 16;
    unsigned cnt, inc, svm;
    u32x16 wt0, wt1, wt2, wt3, wt4;
    STAMP(2);
#define LSTAMP ""
    asm volatile(
        "global_load_dwordx4 %[q1a], %[wv], %[qp]\n\t"
        "global_load_dwordx4 %[q1b], %[wv], %[qp] offset:1024\n\t"
        "global_load_dwordx2 %[q2], v88, %[qp] offset:2048\n\t"
        "global_load_dwordx4 " KA_ ", %[wv], %[kvp]\n\t"
        "global_load_dwordx4 v[" STR(KBR) ":" STR(KBR) "+3], v89, %[kvp] offset:2048\n\t"
        "global_load_dword %[ga], v90, %[gp]\n\t"
        "global_load_dword %[gb], v90, %[gp] offset:128\n\t"
        "s_mov_b32 %[sv], m0\n\t"
        DMA1 DMA1 DMA1 DMA1
        ".irp n,24,25,26,27\n\tv_mov_b32 v[" STR(S1R) "+\\n], 0\n\t.endr\n\t"
        "s_mov_b32 %[cnt], 0\n\ts_mov_b32 %[inc], 20480\n\ts_mov_b64 vcc, -1\n\t"
        "s_waitcnt vmcnt(6)\n\t"
        "v_mfma_f32_32x32x4_2b_bf16 v[" STR(S0R) ":" STR(S0R) "+31], " KB0_ ", %[q2], 0\n\ts_nop 15\n\ts_nop 1\n\t"
        M2_(S0R, 0, q1a) M2_(S0R, 16, q1b)
        "s_nop 10\n\t"
        MX3(0, S0R + 0, 1) MX3(16, S0R + 16, 17) MX3(0, NMR, 3) MX3(16, NMR + 16, 19) MX3(0, NMR, 5) MX3(16, NMR + 16, 21) MX3(0, NMR, 7) MX3(16, NMR + 16, 23)
        MX3(0, NMR, 9) MX3(16, NMR + 16, 25) MX3(0, NMR, 11) MX3(16, NMR + 16, 27) MX3(0, NMR, 13) MX3(16, NMR + 16, 29) MX3(0, NMR, 14) MX3(16, NMR + 16, 30)
        "v_mov_b32 v[" STR(NMR) "+1], v[" STR(NMR) "]\n\tv_mov_b32 v[" STR(NMR) "+17], v[" STR(NMR) "+16]\n\ts_nop 1\n\t"
        "v_permlane32_swap_b32 v[" STR(NMR) "], v[" STR(NMR) "+1]\n\tv_permlane32_swap_b32 v[" STR(NMR) "+16], v[" STR(NMR) "+17]\n\ts_nop 1\n\t"
        "v_min_f32 v[" STR(NMR) "], -v[" STR(NMR) "], -v[" STR(NMR) "+1]\n\tv_min_f32 v[" STR(NMR) "+16], -v[" STR(NMR) "+16], -v[" STR(NMR) "+17]\n\t"
        ".irp n,1,2,3,4,5,6,7,8,9,10,11,12,13,14,15\n\tv_mov_b32 v[" STR(NMR) "+\\n], v[" STR(NMR) "]\n\tv_mov_b32 v[" STR(NMR) "+16+\\n], v[" STR(NMR) "+16]\n\t.endr\n\t"
        ".irp n,0,2,4,6,8,10,12,14,16,18,20,22,24,26,28,30\n\tv_pk_add_f32 v[" STR(S0R) "+\\n:" STR(S0R) "+\\n+1], v[" STR(S0R) "+\\n:" STR(S0R) "+\\n+1], v[" STR(NMR) "+\\n:" STR(NMR) "+\\n+1]\n\t.endr\n\t"
        "s_waitcnt vmcnt(0)\n\ts_barrier\n\t"
        RDKA(1024) RDV(VAR, 0)
        "1:\n\t"
        LSTAMP
        TILE(S0R, S1R, VAR, VBR, 5120, KB1_, RDKB(5120), 1280, PRIO(3) "s_cbranch_vccnz 11f\n\t21:\n\t")
        TILE(S1R, S0R, VBR, VAR, 5120 + 1024, KB0_, "", 5120, "s_cbranch_vccnz 12f\n\t22:\n\t")
        TILE(S0R, S1R, VAR, VBR, 10240, KB1_, RDKB(10240), 5120 + 1280, "s_cbranch_vccnz 13f\n\t23:\n\t")
        TILE(S1R, S0R, VBR, VAR, 10240 + 1024, KB0_, "", 10240, "s_cbranch_vccnz 14f\n\t24:\n\t")
        TILE(S0R, S1R, VAR, VBR, 15360, KB1_, RDKB(15360), 10240 + 1280, PRIO(2))
        TILE(S1R, S0R, VBR, VAR, 15360 + 1024, KB0_, "", 15360, "s_cmp_eq_u32 %[cnt], 7\n\ts_cselect_b32 %[inc], -143360, %[inc]\n\t")
        TILE(S0R, S1R, VAR, VBR, 0, KB1_, RDKB(0), 15360 + 1280, PRIO(1) "v_add_u32 %[ak1], %[inc], %[ak1]\n\tv_add_u32 %[ak2], %[inc], %[ak2]\n\t")
        TILE(S1R, S0R, VBR, VAR, 1024, KB0_, "", 0, PRIO(0) "v_add_u32 %[av], %[inc], %[av]\n\t")
        "s_add_u32 %[cnt], %[cnt], 1\n\t"
        "s_cmp_eq_u32 %[cnt], 1\n\ts_cbranch_scc1 2f\n\t"
        "s_cmp_eq_u32 %[cnt], 3\n\ts_cbranch_scc1 3f\n\t"
        "s_cmp_eq_u32 %[cnt], 4\n\ts_cbranch_scc1 5f\n\t"
        "s_cmp_eq_u32 %[cnt], 5\n\ts_cbranch_scc1 6f\n\t"
        "s_cmp_eq_u32 %[cnt], 6\n\ts_cbranch_scc1 8f\n\t"
        "s_cmp_lt_u32 %[cnt], 8\n\ts_cbranch_scc1 1b\n\t"
        "s_branch 4f\n\t"
        "11:\n\t" DMA1 DMA1 DMA1 DMA1 "s_branch 21b\n\t"
        "12:\n\t" DMA1 DMA1 DMA1 DMA1 "s_branch 22b\n\t"
        "13:\n\t" DMA1 DMA1 DMA1 DMA1 "s_branch 23b\n\t"
        "14:\n\t" DMA1 DMA1 DMA1 DMA1 "s_branch 24b\n\t"
        "2:\n\ts_mov_b64 vcc, 0\n\ts_waitcnt vmcnt(0)\n\ts_barrier\n\ts_branch 1b\n\t"
        "3:\n\t" WLD(0)
        "5:\n\t" WLD(1)
        "6:\n\t" WLD(2)
        "8:\n\tv_and_b32 %[dv], 0x3ff, %[wv]\n\tv_lshlrev_b32 %[dv], 3, %[dv]\n\tv_min_u32 %[dv], 0x1380, %[dv]\n\tv_add_u32 %[dv], 0x5000, %[dv]\n\t"
        "global_load_dword %[dv], %[dv], %[wp] offset:-2048\n\t" WLD(3)
        "4:\n\ts_nop 1\n\t"
        LSTAMP
        PV_(ob, VBR, 4, S1R, 24)
        "s_mov_b32 m0, %[sv]\n\ts_waitcnt vmcnt(0) lgkmcnt(0)\n\ts_nop 15\n\ts_nop 7"
        : [oa] "+{v[24:39]}"(oa), [ob] "+{v[40:55]}"(ob), [ak1] "+v"(ak1), [ak2] "+v"(ak2), [av] "+v"(av), [dv] "+v"(dv), [wv] "+v"(wv),
          "+{v[152:167]}"(vini), "+{v88}"(t0), "+{v89}"(t1), "+{v90}"(t2),
          [q1a] "=&v"(q1a), [q1b] "=&v"(q1b), [q2] "=&v"(q2), [ga] "=&v"(ga), [gb] "=&v"(gb),
          "=&{v[176:191]}"(wt0), "=&{v[192:207]}"(wt1), "=&{v[208:223]}"(wt2), "=&{v[224:239]}"(wt3), "=&{v[240:255]}"(wt4),
          [cnt] "=&s"(cnt), [inc] "=&s"(inc), [sv] "=&s"(svm), [dm] "+s"(dm)
        : [vmask] "s"(0x000fffff000fffffull), [kvp] "s"(kvp), [wp] "s"(wp), [qp] "s"(qp), [gp] "s"(gp),
          "s"(cp), "s"(sgp), "s"(a1), "s"(a2), "s"(op), "s"(po1), "s"(po2)
        : "memory", "scc", "vcc", "v56", "v57", "v58", "v59", CL10(6), CL10(7), "v80", "v81", "v82", "v83", "v84", "v85", "v86", "v87",
          "v91", "v92", "v93", "v94", "v95", "v96", "v97", "v98", "v99", CL10(10), CL10(11), CL10(12), CL10(13), CL10(14), "v150", "v151",
          "v168", "v169", "v170", "v171", "v172", "v173", "v174", "v175"
          );
    asm volatile("v_mbcnt_lo_u32_b32 %0, -1, 0\n\tv_mbcnt_hi_u32_b32 %0, -1, %0" : "=v"(l));
    r32 = l & 31; h = l >> 5;
    STAMP(3);
    bf16x8 wl[5], af[5][4]; unsigned seen;
    {
        if (head >= 32) { ga = 1.0f - ga; gb = 1.0f - gb; }
        u32x4 va, vb; u32x2 vt;
        attn_pack(oa, ob, ga, gb, va, vb, vt);
        if (__builtin_expect(!__all(attn_ok(oa, ob)), 0)) {
#pragma unroll 1
            for (int t = 0; t < 2; ++t) {
                f32x16 ro; u32x4 v0, v1; u32x2 v2;
                attn_fallback(lds, l, qp, t, ro);
                attn_pack(ro, ro, t ? gb : ga, t ? gb : ga, v0, v1, v2);
                if (t == 0) va = v0; else vb = v0;
                if (h == t) vt = v2;
            }
        }
        asm volatile(
            "global_store_dwordx4 %[lo], %[va], %[o1] sc1\n\tglobal_store_dwordx4 %[lo], %[vb], %[o1] offset:1024 sc1\n\tglobal_store_dwordx2 %[lo], %[vt], %[o2] sc1\n\t"
            "s_nop 0"
            :: [lo] "v"(l * 16), [va] "v"(va), [vb] "v"(vb), [vt] "v"(vt), [o1] "s"(po1), [o2] "s"(po2)
            : "memory");
    }
    const int r = l & 15, g = l >> 4, r8 = l & 7, g2 = (l >> 3) & 3;
    unsigned o1 = (g >> 1) * 65536 + (g & 1) * 512 + r * 16, o2 = g * 32768 + r * 16;
    unsigned wbo = w * 20480 + (g * 16 + (r ^ ((g >> 1) << 3))) * 16;
    unsigned rbo = ((w >> 1) * 5 + h) * 1024 + (g2 * 16 + ((8 * (w & 1) + r8) ^ ((g2 >> 1) << 3))) * 16;
    unsigned opo = (8 * w + r8) * 1280 + h * 32 + g2 * 8, sgo = l * CNT_STRIDE;
    asm volatile("s_waitcnt vmcnt(0)\n\ts_barrier\n\t"
                 "s_cmp_lg_u32 %[w], %[sw]\n\ts_cbranch_scc1 1f\n\ts_mov_b64 exec, 0xff\n\tglobal_atomic_add %[so], %[one], %[sp]\n\ts_mov_b64 exec, -1\n\t1:"
                 : "+v"(o1), "+v"(o2), "+v"(wbo), "+v"(rbo), "+v"(opo)
                 : [w] "s"(w), [sw] "s"(xcd ^ 1), [so] "v"(sgo), [one] "v"(1), [sp] "s"(sgp) : "memory", "scc");
    STAMP(4);
    asm volatile(
        "global_load_dwordx4 %0, %6, %7 offset:-2048\n\tglobal_load_dwordx4 %1, %6, %7 offset:-1024\n\tglobal_load_dwordx4 %2, %6, %7\n\t"
        "global_load_dwordx4 %3, %6, %7 offset:1024\n\tglobal_load_dwordx4 %4, %6, %7 offset:2048\n\tglobal_load_dword %5, %8, %9 sc1\n\ts_waitcnt vmcnt(0)"
        : "=&v"(wl[0]), "=&v"(wl[1]), "=&v"(wl[2]), "=&v"(wl[3]), "=&v"(wl[4]), "=&v"(seen) : "v"(wv), "s"(wp), "v"(0), "s"(cp) : "memory");
    const unsigned late = __builtin_amdgcn_readfirstlane(seen);
#pragma unroll 1
    for (int spin = 0; spin < 400000 && __builtin_amdgcn_readfirstlane(seen) < 8; ++spin) {
        __builtin_amdgcn_s_sleep(POLL_SLEEP);
        asm volatile("global_load_dword %0, %1, %2 sc1\n\ts_waitcnt vmcnt(0)" : "=v"(seen) : "v"(0), "s"(cp) : "memory");
    }
    STAMP(5);
#define ALD(s, base, off, d1, d2) asm volatile( \
        "global_load_dwordx4 %0, %4, %5 sc1\n\tglobal_load_dwordx4 %1, %4, %5 offset:" #d1 " sc1\n\t" \
        "global_load_dwordx4 %2, %4, %5 offset:" #d2 " sc1\n\tglobal_load_dwordx4 %3, %4, %5 offset:" #d1 "+" #d2 " sc1" \
        : "=&v"(af[s][0]), "=&v"(af[s][1]), "=&v"(af[s][2]), "=&v"(af[s][3]) : "v"(off), "s"(base) : "memory")
#define AWAIT(s, N) asm volatile("s_waitcnt vmcnt(" STR(N) ")" : "+v"(af[s][0]), "+v"(af[s][1]), "+v"(af[s][2]), "+v"(af[s][3]) :: "memory")
#define OMM(s, w0, w1, w2, w3, w4) do { const bf16x8 wf_[5] = {w0, w1, w2, w3, w4}; \
        _Pragma("unroll") for (int j = 0; j < 5; ++j) _Pragma("unroll") for (int i = 0; i < 4; ++i) acc[i][j] = MFMA16(wf_[j], af[s][i], acc[i][j]); \
        __builtin_amdgcn_sched_barrier(0); } while (0)
    f32x4 acc[4][5];
#pragma unroll
    for (int i = 0; i < 4; ++i)
#pragma unroll
        for (int j = 0; j < 5; ++j) acc[i][j] = f32x4{0.f, 0.f, 0.f, 0.f};
#define LD4(a, b, c, d, off, base, d1, d2) \
        "global_load_dwordx4 %" #a ", %" #off ", %" #base " sc1\n\tglobal_load_dwordx4 %" #b ", %" #off ", %" #base " offset:" #d1 " sc1\n\t" \
        "global_load_dwordx4 %" #c ", %" #off ", %" #base " offset:" #d2 " sc1\n\tglobal_load_dwordx4 %" #d ", %" #off ", %" #base " offset:" #d1 "+" #d2 " sc1\n\t"
    asm volatile(
        LD4(0, 1, 2, 3, 20, 22, 256, 1024) LD4(4, 5, 6, 7, 20, 23, 256, 1024)
        "s_cmp_ge_u32 %27, 8\n\ts_cbranch_scc1 1f\n\t"
        LD4(8, 9, 10, 11, 20, 24, 256, 1024) LD4(12, 13, 14, 15, 20, 25, 256, 1024) LD4(16, 17, 18, 19, 21, 26, 256, 512)
        "s_waitcnt vmcnt(16)\n\ts_branch 2f\n\t"
        "1:\n\ts_waitcnt vmcnt(4)\n\t" LD4(8, 9, 10, 11, 20, 24, 256, 1024)
        "2:"
        : "=&v"(af[0][0]), "=&v"(af[0][1]), "=&v"(af[0][2]), "=&v"(af[0][3]), "=&v"(af[1][0]), "=&v"(af[1][1]), "=&v"(af[1][2]), "=&v"(af[1][3]),
          "=&v"(af[2][0]), "=&v"(af[2][1]), "=&v"(af[2][2]), "=&v"(af[2][3]), "=&v"(af[3][0]), "=&v"(af[3][1]), "=&v"(af[3][2]), "=&v"(af[3][3]),
          "=&v"(af[4][0]), "=&v"(af[4][1]), "=&v"(af[4][2]), "=&v"(af[4][3])
        : "v"(o1), "v"(o2), "s"(a1), "s"(a1 + 131072), "s"(a1 + 2 * 131072), "s"(a1 + 3 * 131072), "s"(a2), "s"(late) : "memory", "scc");
#define ANEXT(s, n, N, off, base, d2) asm volatile( \
        "s_cmp_ge_u32 %10, 8\n\ts_cbranch_scc1 1f\n\ts_waitcnt vmcnt(" #N ")\n\ts_branch 2f\n\t1:\n\ts_waitcnt vmcnt(4)\n\t" LD4(4, 5, 6, 7, 8, 9, 256, d2) "2:" \
        : "+v"(af[s][0]), "+v"(af[s][1]), "+v"(af[s][2]), "+v"(af[s][3]), "+v"(af[n][0]), "+v"(af[n][1]), "+v"(af[n][2]), "+v"(af[n][3]) \
        : "v"(off), "s"(base), "s"(late) : "memory", "scc")
    OMM(0, WSUB(wt0, 0), WSUB(wt0, 1), WSUB(wt0, 2), WSUB(wt0, 3), WSUB(wt1, 0));
    ANEXT(1, 3, 12, o1, a1 + 3 * 131072, 1024); OMM(1, WSUB(wt1, 1), WSUB(wt1, 2), WSUB(wt1, 3), WSUB(wt2, 0), WSUB(wt2, 1));
    ANEXT(2, 4, 8, o2, a2, 512); OMM(2, WSUB(wt2, 2), WSUB(wt2, 3), WSUB(wt3, 0), WSUB(wt3, 1), WSUB(wt3, 2));
    AWAIT(3, 4); OMM(3, WSUB(wt3, 3), WSUB(wt4, 0), WSUB(wt4, 1), WSUB(wt4, 2), WSUB(wt4, 3));
    AWAIT(4, 0);
#pragma unroll
    for (int j = 0; j < 6; ++j) {
        if (j < 5) {
#pragma unroll
            for (int i = 0; i < 4; ++i) acc[i][j] = MFMA16(wl[j], af[4][i], acc[i][j]);
        }
        if (j > 0) {
#pragma unroll
            for (int i = 0; i < 4; ++i) *(f32x4*)(lds + wbo + (i * 5 + j - 1) * 1024) = acc[i][j - 1];
        }
        __builtin_amdgcn_sched_barrier(0);
    }
    STAMP(6);
    {
        __syncthreads();
        f32x4 t[3][8];
#pragma unroll
        for (int u = 0; u < 8; ++u)
#pragma unroll
            for (int p = 0; p < 3; ++p) t[p][u] = *(const f32x4*)(lds + rbo + u * 20480 + (p < 2 ? p * 2048 : 4096 - h * 1024));
        __builtin_amdgcn_sched_barrier(0);
#pragma unroll
        for (int p = 0; p < 3; ++p) {
            const f32x4 z = ((t[p][0] + t[p][1]) + (t[p][2] + t[p][3])) + ((t[p][4] + t[p][5]) + (t[p][6] + t[p][7]));
            const u32x2 v = {pk_bf16(z[0], z[1]), pk_bf16(z[2], z[3])};
            if (p < 2 || h == 0) asm volatile("global_store_dwordx2 %0, %1, %2 offset:%3 " OUT_FLAGS :: "v"(opo), "v"(v), "s"(op), "i"(p * 64) : "memory");
        }
    }
    STAMP(7); STAMP_FLUSH(ws, 1);
}

extern "C" void kernel_launch(void* const* d_in, const int* in_sizes, int n_in, void* d_out, int out_size, void* d_ws, size_t ws_size,
                              hipStream_t stream) {
    const char* x = (const char*)d_in[0];
    const char* wq = (const char*)d_in[1];
    const char* wk = (const char*)d_in[2];
    const char* wv = (const char*)d_in[3];
    const char* woa = (const char*)d_in[4];
    const char* wqkv = (const char*)d_in[6];
    const char* wob = (const char*)d_in[7];
    const char* gw = (const char*)d_in[8];
    char* ws = (char*)d_ws;
    k_proj<<<256, 768, 0, stream>>>(x, wq, wk, wv, wqkv, gw, woa, wob, ws);
    k_attn<<<256, 512, 0, stream>>>(ws, (char*)d_out);
}
```

```cpp
#include <hip/hip_runtime.h>
#include <hip/hip_bf16.h>
#include <stdint.h>

typedef __attribute__((ext_vector_type(8))) short bf16x8;
typedef __attribute__((ext_vector_type(4))) short s16x4;
typedef __attribute__((ext_vector_type(4))) float f32x4;
typedef __attribute__((ext_vector_type(16))) float f32x16;
typedef __attribute__((ext_vector_type(32))) float f32x32;
typedef __attribute__((ext_vector_type(2))) unsigned u32x2;
typedef __attribute__((ext_vector_type(4))) unsigned u32x4;
typedef __attribute__((ext_vector_type(2))) __bf16 bf16x2_t;
typedef __attribute__((ext_vector_type(2))) float f32x2;
#define LDSP __attribute__((address_space(3)))
#define DI __device__ __forceinline__

#define STAMP_PARAMS
#define STAMP_ARGS
#define STAMP_DECL
#define STAMP(slot)
#define STAMP_FLUSH(wsp, kid)
constexpr int SEQ = 2048, DM = 640;
constexpr size_t Q_HEAD = 32 * 2560;
constexpr size_t KV_HEAD = 32 * 5120;
constexpr size_t WS_Q = 0;
constexpr size_t WS_KV = WS_Q + 64 * Q_HEAD;
constexpr size_t WS_G = WS_KV + 33 * KV_HEAD;
constexpr size_t WS_O1 = WS_G + 2048 * 4;
constexpr size_t WS_O2 = WS_O1 + 64 * 65536;
constexpr size_t WS_WO = WS_O2 + 32 * 2048 * 16;
#define OUT_FLAGS ""
#define XT_WIN 0
#define CNT_STRIDE 128
#define POLL_SLEEP 4
constexpr size_t WS_CNT = WS_WO + 1600 * 1024;

DI unsigned pk_bf16(float a, float b) {
    f32x2 v = {a, b};
    return __builtin_bit_cast(unsigned, __builtin_convertvector(v, bf16x2_t));
}
#define ST_FLAGS "sc1"
DI void st16(char* p, u32x4 v) { asm volatile("global_store_dwordx4 %0, %1, off " ST_FLAGS "\n\ts_nop 1" :: "v"(p), "v"(v) : "memory"); }
DI void st8(char* p, u32x2 v) { asm volatile("global_store_dwordx2 %0, %1, off " ST_FLAGS :: "v"(p), "v"(v) : "memory"); }
DI s16x4 tr_read(const LDSP char* p) {
    typedef short v4i16_t __attribute__((ext_vector_type(4)));
    return __builtin_amdgcn_ds_read_tr16_b64_v4i16((LDSP v4i16_t*)p);
}
DI bf16x8 cat8(s16x4 lo, s16x4 hi) { return __builtin_shufflevector(lo, hi, 0, 1, 2, 3, 4, 5, 6, 7); }
#define MFMA16(a, b, c) __builtin_amdgcn_mfma_f32_16x16x32_bf16((a), (b), (c), 0, 0, 0)
#define MFMA32(a, b, c) __builtin_amdgcn_mfma_f32_32x32x16_bf16((a), (b), (c), 0, 0, 0)

constexpr int P_SLOT = 40960;

DI void proj_loader(char* lds, const char* xs, const char* ws, const char* es, int sw, int se, unsigned long long m5, int st, int ekind, int ehalf, int mb,
                    const char* woa, const char* wob, char* wsp, const unsigned (&xo)[2], const unsigned (&wo)[3]) {
    const int l = threadIdx.x & 63, w = __builtin_amdgcn_readfirstlane(threadIdx.x >> 6), r = l & 15, g = l >> 4;
    const unsigned dw = __builtin_amdgcn_readfirstlane((unsigned)(uintptr_t)lds + w * 1024);
    auto issue = [&](int slot) {
        const unsigned d0 = dw + slot * P_SLOT;
        unsigned sv;
        asm volatile(
            "s_mov_b32 %[sv], m0\n\ts_mov_b32 m0, %[d0]\n\ts_nop 0\n\t"
            "global_load_lds_dwordx4 %[x0], %[xs]\n\ts_add_u32 m0, m0, 8192\n\ts_nop 0\n\t"
            "global_load_lds_dwordx4 %[x1], %[xs]\n\ts_add_u32 m0, m0, 8192\n\ts_nop 0\n\t"
            "global_load_lds_dwordx4 %[w0], %[ws]\n\ts_add_u32 m0, m0, 8192\n\ts_nop 0\n\t"
            "global_load_lds_dwordx4 %[w1], %[ws]\n\ts_add_u32 m0, m0, 8192\n\ts_mov_b64 exec, %[m5]\n\t"
            "global_load_lds_dwordx4 %[w2], %[es]\n\ts_mov_b64 exec, -1\n\ts_mov_b32 m0, %[sv]"
            : [sv] "=&s"(sv)
            : [d0] "s"(d0), [m5] "s"(m5), [xs] "s"(xs), [ws] "s"(ws), [es] "s"(es),
              [x0] "v"(xo[0]), [x1] "v"(xo[1]), [w0] "v"(wo[0]), [w1] "v"(wo[1]), [w2] "v"(wo[2])
            : "memory", "scc");
        const int d = st == 9 ? -9 : 1;
        st += d; xs += d * 128; ws += d * sw; es += d * se;
    };
    issue(0); issue(1);
    __builtin_amdgcn_s_setprio(3);
    f32x4 eacc = {0.f, 0.f, 0.f, 0.f};
    const int ets = 4 * ehalf + w;
    if (w >= 4) ekind = 5;
    const int exr = (16 * ets + r) * 128 + ((g ^ ((r >> 1) & 7)) << 4);
    const int ewr = ekind < 4 ? 36864 + (8 * g + (r >> 2)) * 40 + ((ekind & 1) ? 32 : 8 * (r & 3)) : 36864 + g * 16;
    auto extra = [&](int k) {
        if (ekind < 5) {
            const LDSP char* sb = (const LDSP char*)lds + (k & 3) * P_SLOT;
            const bf16x8 x0 = *(const LDSP bf16x8*)(sb + exr), x1 = *(const LDSP bf16x8*)(sb + (exr ^ 64));
            bf16x8 w0, w1;
            if (ekind < 4) { w0 = cat8(tr_read(sb + ewr), tr_read(sb + ewr + 160)); w1 = cat8(tr_read(sb + ewr + 1280), tr_read(sb + ewr + 1440)); }
            else { w0 = *(const LDSP bf16x8*)(sb + ewr); w1 = *(const LDSP bf16x8*)(sb + ewr + 64); }
            if ((ekind >> 1) == 1) { eacc = MFMA16(x0, w0, eacc); eacc = MFMA16(x1, w1, eacc); }
            else { eacc = MFMA16(w0, x0, eacc); eacc = MFMA16(w1, x1, eacc); }
            asm volatile("" : "+v"(eacc));
        }
    };
#pragma unroll 1
    for (int k = 0; k < 8; ++k) {
        asm volatile("s_waitcnt vmcnt(5)\n\ts_barrier" ::: "memory");
        issue((k + 2) & 3);
        if (k < 7) extra(k);
    }
    const int gw = blockIdx.x * 8 + w, fid = gw < 1600 ? gw : 1599;
    const int fj = fid % 5, fs = (fid / 5) % 5, fg = (fid / 25) & 7, fn = fid / 200;
    const int br = fs < 4 ? fs >> 1 : g >> 1;
    const int row = fs < 4 ? (4 * fg + 2 * (fs & 1) + (g >> 1)) * 20 + 4 * (g & 1) : (4 * fg + 2 * (g & 1)) * 20 + 16;
    const char* p0 = (br ? wob : woa) + row * 1280 + (80 * fn + 16 * fj + r) * 2;
    const char* p1 = p0 + (fs < 4 ? 8 : 20) * 1280;
    unsigned e0, e1, e2, e3, e4, e5, e6, e7;
    asm volatile(
        "global_load_ushort %0, %8, off\n\tglobal_load_ushort %1, %8, off offset:1280\n\tglobal_load_ushort %2, %8, off offset:2560\n\tglobal_load_ushort %3, %8, off offset:3840\n\t"
        "global_load_ushort %4, %9, off\n\tglobal_load_ushort %5, %9, off offset:1280\n\tglobal_load_ushort %6, %9, off offset:2560\n\tglobal_load_ushort %7, %9, off offset:3840"
        : "=&v"(e0), "=&v"(e1), "=&v"(e2), "=&v"(e3), "=&v"(e4), "=&v"(e5), "=&v"(e6), "=&v"(e7) : "v"(p0), "v"(p1) : "memory");
    extra(7);
    asm volatile("s_waitcnt vmcnt(13)\n\ts_barrier" ::: "memory");
    extra(8);
    asm volatile("s_waitcnt vmcnt(8)\n\ts_barrier" ::: "memory");
    extra(9);
    asm volatile("s_waitcnt vmcnt(0)" : "+v"(e0), "+v"(e1), "+v"(e2), "+v"(e3), "+v"(e4), "+v"(e5), "+v"(e6), "+v"(e7) :: "memory");
    {
        char* kva = wsp + WS_KV + 32 * KV_HEAD + (mb * 2 + ehalf) * 5120;
        const u32x2 v = {pk_bf16(eacc[0], eacc[1]), pk_bf16(eacc[2], eacc[3])};
        const int t = (w >> 1) & 1, erow = 16 * (r >> 3) + 8 * (w & 1) + (r & 7);
        if (ekind == 0) st8(kva + t * 1024 + (g >> 1) * 512 + erow * 16 + (g & 1) * 8, v);
        else if (ekind == 1) { if (g == 0) st8(kva + 2048 + erow * 16 + t * 8, v); }
        else if (ekind < 4) { if (ekind == 2 || r < 4) st8(kva + 2560 + t * 1280 + g * 320 + ((ekind & 1) * 16 + r) * 16 + (w & 1) * 8, v); }
        else if (ekind == 4) { if (g == 0) ((float*)(wsp + WS_G))[mb * 128 + 16 * ets + r] = 1.0f / (1.0f + __expf(-eacc[0])); }
    }
    if (gw < 1600) st16(wsp + WS_WO + (size_t)fid * 1024 + l * 16, u32x4{e0 | (e1 << 16), e2 | (e3 << 16), e4 | (e5 << 16), e6 | (e7 << 16)});
    if (w == 0 && l == 0) *(unsigned*)(wsp + WS_CNT + blockIdx.x * CNT_STRIDE) = 0;
}

#define STR_(x) #x
#define STR(x) STR_(x)
#define PR4(b) "v[" STR(b) ":" STR(b) "+3]"
#define PM_F(j, i, X) "v_mfma_f32_16x16x32_bf16 " PR4(20 + 16 * j + 4 * i) ", " PR4(136 + 4 * j) ", " PR4(X + 4 * i) ", " PR4(20 + 16 * j + 4 * i) "\n\t"
#define PM_T(j, i, X) "v_mfma_f32_16x16x32_bf16 " PR4(20 + 16 * j + 4 * i) ", " PR4(X + 4 * i) ", " PR4(136 + 4 * j) ", " PR4(20 + 16 * j + 4 * i) "\n\t"
#define PTL(j, sl, ks) "ds_read_b64_tr_b16 v[136+4*" #j ":136+4*" #j "+1], %[aw" #j "] offset:" #sl "*40960+" #ks "*10240\n\t"
#define PTH(j, sl, ks) "ds_read_b64_tr_b16 v[136+4*" #j "+2:136+4*" #j "+3], %[aw" #j "] offset:" #sl "*40960+" #ks "*10240+1280\n\t"
#define PBX(X, i, sl, ks) "ds_read_b128 " PR4(X + 4 * i) ", %[ax" #ks "] offset:" #sl "*40960+" #i "*2048\n\t"
#define PTG(a) "v_add_u32 %[" #a "], %[tg], %[" #a "]\n\t"
#define PSTEP(M, X, Y, cs, ck, ns, nk, BAR, F0, F1, F2, F3) \
    M(0, 0, X) PTL(4, cs, ck) M(0, 1, X) PTH(4, cs, ck) M(0, 2, X) BAR PBX(Y, 0, ns, nk) M(0, 3, X) PBX(Y, 1, ns, nk) \
    M(1, 0, X) PTL(0, ns, nk) M(1, 1, X) PTH(0, ns, nk) M(1, 2, X) PBX(Y, 2, ns, nk) M(1, 3, X) "s_waitcnt lgkmcnt(5)\n\t" \
    M(2, 0, X) PTL(1, ns, nk) M(2, 1, X) PTH(1, ns, nk) M(2, 2, X) PBX(Y, 3, ns, nk) M(2, 3, X) F0 \
    M(3, 0, X) PTL(2, ns, nk) M(3, 1, X) PTH(2, ns, nk) M(3, 2, X) F1 M(3, 3, X) F2 \
    M(4, 0, X) PTL(3, ns, nk) M(4, 1, X) PTH(3, ns, nk) M(4, 2, X) F3 M(4, 3, X) "s_waitcnt lgkmcnt(4)\n\t"
#define CL10(a) "v" #a "0", "v" #a "1", "v" #a "2", "v" #a "3", "v" #a "4", "v" #a "5", "v" #a "6", "v" #a "7", "v" #a "8", "v" #a "9"
#define PLOOP(M) \
    asm volatile( \
        "s_mov_b32 %[cnt], 0\n\ts_mov_b32 %[tg], 81920\n\t" \
        "s_barrier\n\t" \
        PBX(104, 0, 0, 0) PBX(104, 1, 0, 0) PTL(0, 0, 0) PTH(0, 0, 0) PBX(104, 2, 0, 0) PTL(1, 0, 0) PTH(1, 0, 0) PBX(104, 3, 0, 0) \
        PTL(2, 0, 0) PTH(2, 0, 0) PTL(3, 0, 0) PTH(3, 0, 0) "s_waitcnt lgkmcnt(4)\n\t" \
        "1:\n\t" \
        PSTEP(M, 104, 120, 0, 0, 0, 1, "", "", "", "", "") \
        PSTEP(M, 120, 104, 0, 1, 1, 0, "s_barrier\n\t", PTG(ax0), "s_add_u32 %[cnt], %[cnt], 1\n\t", "", "") \
        PSTEP(M, 104, 120, 1, 0, 1, 1, "", PTG(aw0), PTG(aw1), PTG(aw2), PTG(ax1)) \
        PSTEP(M, 120, 104, 1, 1, 0, 0, "s_cmp_eq_u32 %[cnt], 5\n\ts_cbranch_scc1 2f\n\ts_barrier\n\t2:\n\t", PTG(aw3), PTG(aw4), "s_sub_u32 %[tg], 0, %[tg]\n\t", \
              "s_cmp_lg_u32 %[cnt], 5\n\t") \
        "s_cbranch_scc1 1b\n\t" \
        "s_waitcnt lgkmcnt(0)\n\ts_nop 7" \
        : "+{v[20:35]}"(acc[0]), "+{v[36:51]}"(acc[1]), "+{v[52:67]}"(acc[2]), "+{v[68:83]}"(acc[3]), "+{v[84:99]}"(acc[4]), \
          [ax0] "+v"(ax0), [ax1] "+v"(ax1), [aw0] "+v"(aw[0]), [aw1] "+v"(aw[1]), [aw2] "+v"(aw[2]), [aw3] "+v"(aw[3]), [aw4] "+v"(aw[4]), \
          [cnt] "=&s"(cnt), [tg] "=&s"(tg) \
        : \
        : "memory", "scc", "v104", "v105", "v106", "v107", "v108", "v109", CL10(11), CL10(12), CL10(13), CL10(14), "v150", "v151", "v152", "v153", "v154", "v155")

template <bool VT>
DI void proj_mma(unsigned lds0, f32x16 (&acc)[5]) {
    const int tid = threadIdx.x, l = tid & 63, w = __builtin_amdgcn_readfirstlane(tid >> 6) - 8;
    const int wm = w >> 1, wn = w & 1, r = l & 15, g = l >> 4;
    const int xsw = (g ^ ((r >> 1) & 7)) << 4;
    unsigned ax0 = lds0 + (64 * wm + r) * 128 + xsw, ax1 = lds0 + (64 * wm + r) * 128 + (xsw ^ 64);
    unsigned aw[5];
    const int q = r >> 2, p = r & 3;
#pragma unroll
    for (int j = 0; j < 5; ++j) {
        int col;
        if (j == 4) col = (4 * wn + p) * 20 + 16;
        else if (VT) col = (4 * wn + j) * 20 + 4 * p;
        else col = (4 * wn + (j & 2) + (p >> 1)) * 20 + 8 * (p & 1) + 4 * (j & 1);
        aw[j] = lds0 + 16384 + (8 * g + q) * 320 + ((col * 2) ^ ((g & 1) << 5));
    }
    unsigned cnt, tg;
    if constexpr (VT) PLOOP(PM_T); else PLOOP(PM_F);
}
#define ACC(i, j) f32x4{acc[j][4 * (i)], acc[j][4 * (i) + 1], acc[j][4 * (i) + 2], acc[j][4 * (i) + 3]}

DI u32x4 pk8(const f32x4& a, const f32x4& b, float sc) {
    return u32x4{pk_bf16(a[0] * sc, a[1] * sc), pk_bf16(a[2] * sc, a[3] * sc), pk_bf16(b[0] * sc, b[1] * sc), pk_bf16(b[2] * sc, b[3] * sc)};
}

#define CODE_PROJ "_Z6k_projPKcS0_S0_S0_S0_S0_S0_S0_Pc"
#define CODE_ATTN "_Z6k_attnPcS_"
#define CODE_BYTES 8192
__global__ __launch_bounds__(768, 3) void k_proj(const char* __restrict__ x, const char* __restrict__ wq, const char* __restrict__ wqkv,
                                                 const char* __restrict__ wk, const char* __restrict__ wv, const char* __restrict__ gw,
                                                 const char* __restrict__ woa, const char* __restrict__ wob, char* __restrict__ ws) {
    __shared__ __attribute__((aligned(16))) char lds[4 * P_SLOT];
    STAMP_DECL; STAMP(0);
    const int b = blockIdx.x, xcd = b & 7, ii = b >> 3;
    const int mb = (xcd & 1) * 8 + (ii & 7), type = xcd >> 1, nq = ii >> 3;
    const int tid = threadIdx.x, l = tid & 63, wv_ = __builtin_amdgcn_readfirstlane(tid >> 6);
    const int ldw2 = type == 0 ? 1280 : 3840;
    const int eidx = ((nq >> 1) | ((nq & 1) << 1)) * 4 + type, ehalf = eidx >= 5, ekind = eidx < 10 ? eidx - 5 * ehalf : 5;
    int rot = (ii & 7) + nq + type; rot = 2 * (rot >= 10 ? rot - 10 : (rot >= 5 ? rot - 5 : rot));
    const int ebytes = ekind < 4 ? 2560 : 128, nbytes = ebytes - (wv_ - 4) * 1024;
    const unsigned long long m5 = ~0ull >> (nbytes >= 1024 ? 0 : (nbytes > 0 ? 64 - (nbytes >> 4) : 63));
    const int sw = 64 * ldw2, se = wv_ < 4 ? sw : ebytes, wofs = (type > 1 ? (type - 1) * 1280 : 0) + nq * 320 + rot * sw;
    unsigned xo[2], wo[3];
    xo[0] = (mb * 128 + (tid >> 3)) * (DM * 2) + (((tid ^ (tid >> 4)) & 7) << 4) + rot * 128; xo[1] = xo[0] + 64 * DM * 2;
#pragma unroll
    for (int n = 0; n < 3; ++n) { const int cid = tid + 512 * n, k = (cid * 3277) >> 16, c = cid - 20 * k; wo[n] = k * ldw2 + ((c ^ (((k >> 3) & 1) << 1)) << 4) + wofs; }
    if (tid >= 256) wo[2] = l * 16 + (nbytes > 0 ? (wv_ - 4) * 1024 : 0) + rot * ebytes;
    asm volatile("" : "+v"(xo[0]), "+v"(xo[1]), "+v"(wo[0]), "+v"(wo[1]), "+v"(wo[2])
                 : "s"(x), "s"(wq), "s"(wqkv), "s"(wk), "s"(wv), "s"(gw), "s"(woa), "s"(wob), "s"(ws), "s"(m5), "s"(se), "s"(ekind));
    if (__builtin_expect(wv_ < 8, 1)) {
        const char* wsel = type == 0 ? wq : wqkv;
        proj_loader(lds, x, wsel, wv_ < 4 ? wsel : (ekind < 2 ? wk : (ekind < 4 ? wv : gw)), sw, se, m5, rot, ekind, ehalf, mb, woa, wob, ws, xo, wo);
        return;
    }
    const int w = wv_ - 8;
    if (w < 2) {
        const unsigned dp = (unsigned)(uintptr_t)lds + P_SLOT - 512, off = (ii * 3 + l) * 128; unsigned sv; unsigned long long pc;
        if (w == 0) asm volatile("s_getpc_b64 s[40:41]\n\ts_add_u32 s40, s40, " CODE_PROJ "@rel32@lo+4\n\ts_addc_u32 s41, s41, " CODE_PROJ "@rel32@hi+12" : "={s[40:41]}"(pc) :: "scc");
        else asm volatile("s_getpc_b64 s[40:41]\n\ts_add_u32 s40, s40, " CODE_ATTN "@rel32@lo+4\n\ts_addc_u32 s41, s41, " CODE_ATTN "@rel32@hi+12" : "={s[40:41]}"(pc) :: "scc");
        if (l < 3 && (w == 0 || off < CODE_BYTES))
            asm volatile("s_mov_b32 %0, m0\n\ts_mov_b32 m0, %1\n\ts_nop 0\n\tglobal_load_lds_dword %2, %3\n\ts_mov_b32 m0, %0" : "=&s"(sv) : "s"(dp), "v"(off), "s"(pc) : "memory");
    }
    const int wm = w >> 1, wn = w & 1, r = l & 15, g = l >> 4;
    f32x16 acc[5];
#pragma unroll
    for (int j = 0; j < 5; ++j)
#pragma unroll
        for (int i = 0; i < 16; ++i) acc[j][i] = 0.f;
    const unsigned lds0 = (unsigned)(uintptr_t)lds;
    const int chunk = mb * 2 + wm;
    if (type == 3) {
        proj_mma<true>(lds0, acc);
        STAMP(3);
        char* base = ws + WS_KV + (size_t)(nq * 8 + 4 * wn) * KV_HEAD + chunk * 5120 + 2560 + g * 320;
#pragma unroll
        for (int t = 0; t < 2; ++t) {
#pragma unroll
            for (int j = 0; j < 4; ++j) st16(base + (size_t)j * KV_HEAD + t * 1280 + r * 16, pk8(ACC(2 * t, j), ACC(2 * t + 1, j), 1.0f));
            st16(base + (size_t)(r >> 2) * KV_HEAD + t * 1280 + (16 + (r & 3)) * 16, pk8(ACC(2 * t, 4), ACC(2 * t + 1, 4), 1.0f));
        }
    } else {
        proj_mma<false>(lds0, acc);
        STAMP(3);
        const float sc = type == 2 ? 1.0f : 0.32259784f;
        const size_t hstride = type == 2 ? KV_HEAD : Q_HEAD;
        const int row0 = type == 2 ? 16 * (r >> 3) + (r & 7) : r, ustep = type == 2 ? 128 : 256;
        char* base = (type == 2 ? ws + WS_KV + chunk * 5120 : ws + WS_Q + (size_t)type * 32 * Q_HEAD + chunk * 2560) + (size_t)(nq * 8 + 4 * wn) * hstride + row0 * 16;
#pragma unroll
        for (int i = 0; i < 4; ++i)
#pragma unroll
            for (int jj = 0; jj < 2; ++jj)
                st16(base + (2 * jj + (g >> 1)) * hstride + (i >> 1) * 1024 + (g & 1) * 512 + (i & 1) * ustep, pk8(ACC(i, 2 * jj), ACC(i, 2 * jj + 1), sc));
#pragma unroll
        for (int u = 0; u < 2; ++u) st16(base + g * hstride + 2048 + u * ustep, pk8(ACC(u, 4), ACC(u + 2, 4), sc));
    }
    STAMP(4); STAMP_FLUSH(ws, 0);
}

#define S0R 56
#define S1R 88
#define NMR 120
#define VAR 152
#define VBR 160
#define KAR 168
#define KBR 172
#define WFR 176
#define E_(X, i) "v_exp_f32 v[" STR(X) "+" #i "], v[" STR(X) "+" #i "]\n\t"
#define C_(X, d, a, b) "v_cvt_pk_bf16_f32 v[" STR(X) "+" #d "], v[" STR(X) "+" #a "], v[" STR(X) "+" #b "]\n\t"
#define PV_(O, V, vo, X, po) "v_mfma_f32_32x32x16_bf16 %[" #O "], v[" STR(V) "+" #vo ":" STR(V) "+" #vo "+3], v[" STR(X) "+" #po ":" STR(X) "+" #po "+3], %[" #O "]\n\t"
#define M1_(Y, KB) "v_mfma_f32_32x32x4_2b_bf16 v[" STR(Y) ":" STR(Y) "+31], " KB ", %[q2], v[" STR(NMR) ":" STR(NMR) "+31]\n\t"
#define M2_(Y, o, Q) "v_mfma_f32_32x32x16_bf16 v[" STR(Y) "+" #o ":" STR(Y) "+" #o "+15], " KA_ ", %[" #Q "], v[" STR(Y) "+" #o ":" STR(Y) "+" #o "+15]\n\t"
#define KA_ "v[" STR(KAR) ":" STR(KAR) "+3]"
#define KB0_ "v[" STR(KBR) ":" STR(KBR) "+1]"
#define KB1_ "v[" STR(KBR) "+2:" STR(KBR) "+3]"
#define RDKA(off) "ds_read_b128 " KA_ ", %[ak1] offset:" STR(off) "\n\t"
#define RDKB(off) "ds_read_b128 v[" STR(KBR) ":" STR(KBR) "+3], %[ak2] offset:" STR(off) "\n\t"
#define RDV(V, off) "s_mov_b64 exec, %[vmask]\n\tds_read_b128 v[" STR(V) ":" STR(V) "+3], %[av] offset:" STR(off) "\n\t" \
                    "ds_read_b128 v[" STR(V) "+4:" STR(V) "+7], %[av] offset:" STR(off) "+640\n\ts_mov_b64 exec, -1\n\t"
#define MX3(d, a, i) "v_max3_f32 v[" STR(NMR) "+" #d "], v[" STR(a) "], v[" STR(S0R) "+" #i "], v[" STR(S0R) "+" #i "+1]\n\t"
#define PRIO(n) "s_setprio " #n "\n\t"
#define DMA1 "s_mov_b32 m0, %[dm]\n\ts_add_u32 %[dm], %[dm], 8192\n\tglobal_load_lds_dwordx4 %[dv], %[kvp]\n\tv_add_u32 %[dv], 0x2000, %[dv]\n\t"
#define WLD1(s, j) "global_load_dwordx4 v[" STR(WFR) "+20*" #s "+4*" #j ":" STR(WFR) "+20*" #s "+4*" #j "+3], %[wv], %[wp] offset:" #j "*1024-2048\n\t"
#define WLD(s) WLD1(s, 0) WLD1(s, 1) WLD1(s, 2) WLD1(s, 3) WLD1(s, 4) "v_add_u32 %[wv], 0x1400, %[wv]\n\ts_branch 1b\n\t"
#define TILE(X, Y, VC, VP, KAOFF, KB, KBRD, VOFF, PRE) \
    PRE E_(X, 0) E_(X, 1) \
    PV_(ob, VP, 4, Y, 24) \
    E_(X, 2) E_(X, 3) C_(X, 0, 0, 1) E_(X, 4) E_(X, 5) C_(X, 1, 2, 3) \
    "s_waitcnt lgkmcnt(0)\n\t" M1_(Y, KB) KBRD \
    E_(X, 6) E_(X, 7) C_(X, 2, 4, 5) E_(X, 8) C_(X, 3, 6, 7) E_(X, 9) E_(X, 10) E_(X, 11) C_(X, 8, 8, 9) \
    PV_(oa, VC, 0, X, 0) RDV(VP, VOFF) \
    E_(X, 12) C_(X, 9, 10, 11) E_(X, 13) E_(X, 14) E_(X, 15) C_(X, 10, 12, 13) E_(X, 16) C_(X, 11, 14, 15) E_(X, 17) \
    M2_(Y, 0, q1a) \
    E_(X, 18) E_(X, 19) C_(X, 16, 16, 17) E_(X, 20) E_(X, 21) C_(X, 17, 18, 19) \
    PV_(oa, VC, 4, X, 8) \
    E_(X, 22) E_(X, 23) C_(X, 18, 20, 21) E_(X, 24) C_(X, 19, 22, 23) E_(X, 25) \
    M2_(Y, 16, q1b) RDKA(KAOFF) \
    E_(X, 26) E_(X, 27) C_(X, 24, 24, 25) E_(X, 28) E_(X, 29) C_(X, 25, 26, 27) \
    PV_(ob, VC, 0, X, 16) \
    E_(X, 30) E_(X, 31) C_(X, 26, 28, 29) C_(X, 27, 30, 31)

DI float max_halves(float m) {
    auto rr = __builtin_amdgcn_permlane32_swap(__float_as_uint(m), __float_as_uint(m), false, false);
    return fmaxf(__uint_as_float(rr[0]), __uint_as_float(rr[1]));
}
DI void attn_fallback(const char* lds, int l, const char* qp, int tile, f32x16& o) {
    const int r32 = l & 31, h = l >> 5;
    const s16x4 z4 = {0, 0, 0, 0};
    const bf16x8 q1 = *(const bf16x8*)(qp + tile * 1024 + l * 16);
    const s16x4 q2 = *(const s16x4*)(qp + 2048 + r32 * 16 + tile * 8);
    const bf16x8 q2f = cat8(h == 0 ? q2 : z4, z4);
    const bf16x8 ones = {0x3f80, 0x3f80, 0x3f80, 0x3f80, 0x3f80, 0x3f80, 0x3f80, 0x3f80};
    float m = -INFINITY;
#pragma unroll
    for (int i = 0; i < 16; ++i) o[i] = 0.f;
#pragma unroll 1
    for (int t = 0; t < 64; ++t) {
        const LDSP char* cb = (const LDSP char*)lds + (t >> 1) * 5120;
        const bf16x8 k1 = *(const LDSP bf16x8*)(cb + (t & 1) * 1024 + l * 16);
        const s16x4 k2 = *(const LDSP s16x4*)(cb + 2048 + r32 * 16 + (t & 1) * 8);
        f32x16 s;
#pragma unroll
        for (int i = 0; i < 16; ++i) s[i] = 0.f;
        s = MFMA32(k1, q1, s);
        s = MFMA32(cat8(k2, z4), q2f, s);
        float tm = s[0];
#pragma unroll
        for (int i = 1; i < 16; ++i) tm = fmaxf(tm, s[i]);
        tm = max_halves(tm);
        const float mn = fmaxf(m, tm), alpha = __builtin_amdgcn_exp2f(m - mn);
        m = mn;
        u32x4 p0, p1;
#pragma unroll
        for (int i = 0; i < 4; ++i) {
            p0[i] = pk_bf16(__builtin_amdgcn_exp2f(s[2 * i] - m), __builtin_amdgcn_exp2f(s[2 * i + 1] - m));
            p1[i] = pk_bf16(__builtin_amdgcn_exp2f(s[8 + 2 * i] - m), __builtin_amdgcn_exp2f(s[9 + 2 * i] - m));
        }
#pragma unroll
        for (int i = 0; i < 16; ++i) o[i] *= alpha;
        const int dv = r32 < 20 ? r32 : 0;
        bf16x8 v0 = *(const LDSP bf16x8*)(cb + 2560 + (t & 1) * 1280 + h * 320 + dv * 16);
        bf16x8 v1 = *(const LDSP bf16x8*)(cb + 2560 + (t & 1) * 1280 + 640 + h * 320 + dv * 16);
        if (r32 >= 20) { v0 = ones; v1 = ones; }
        o = MFMA32(v0, __builtin_bit_cast(bf16x8, p0), o);
        o = MFMA32(v1, __builtin_bit_cast(bf16x8, p1), o);
    }
}

DI bool attn_ok(const f32x16& a, const f32x16& b) {
    f32x2 s = {0.f, 0.f};
#pragma unroll
    for (int i = 0; i < 12; i += 2) s += f32x2{a[i], a[i + 1]} + f32x2{b[i], b[i + 1]};
    return (fmaxf(a[12], b[12]) < 0x1p80f) & (fabsf(s[0] + s[1] + a[12] + b[12]) < 0x1p126f);
}
DI void attn_pack(const f32x16& oa, const f32x16& ob, float ga, float gb, u32x4& va, u32x4& vb, u32x2& vt) {
    const float fa = ga * __builtin_amdgcn_rcpf(oa[12]), fb = gb * __builtin_amdgcn_rcpf(ob[12]);
    va = u32x4{pk_bf16(oa[0] * fa, oa[1] * fa), pk_bf16(oa[2] * fa, oa[3] * fa), pk_bf16(oa[4] * fa, oa[5] * fa), pk_bf16(oa[6] * fa, oa[7] * fa)};
    vb = u32x4{pk_bf16(ob[0] * fb, ob[1] * fb), pk_bf16(ob[2] * fb, ob[3] * fb), pk_bf16(ob[4] * fb, ob[5] * fb), pk_bf16(ob[6] * fb, ob[7] * fb)};
    auto s0 = __builtin_amdgcn_permlane32_swap(pk_bf16(oa[8] * fa, oa[9] * fa), pk_bf16(ob[8] * fb, ob[9] * fb), false, false);
    auto s1 = __builtin_amdgcn_permlane32_swap(pk_bf16(oa[10] * fa, oa[11] * fa), pk_bf16(ob[10] * fb, ob[11] * fb), false, false);
    vt = u32x2{s0[0], s1[0]};
}

typedef __attribute__((ext_vector_type(16))) unsigned u32x16;
#define WSUB(v, i) __builtin_bit_cast(bf16x8, __builtin_shufflevector(v, v, 4 * (i), 4 * (i) + 1, 4 * (i) + 2, 4 * (i) + 3))

__global__ __launch_bounds__(512, 2) void k_attn(char* __restrict__ ws, char* __restrict__ out) {
    __shared__ __attribute__((aligned(16))) char lds[163840];
    STAMP_DECL; STAMP(0);
    const int b = blockIdx.x, xcd = b & 7, ii = b >> 3;
    const int pos = ii >> 2, head = (pos >> 2) * 32 + xcd * 4 + (pos & 3), slot = xcd * 8 + pos, qb = ii & 3;
    const int w = __builtin_amdgcn_readfirstlane(threadIdx.x >> 6);
    int l = threadIdx.x & 63, r32 = l & 31, h = l >> 5;
    const int qc = qb * 8 + w;
    const unsigned lds0 = (unsigned)(uintptr_t)lds;
    const unsigned qo = (unsigned)WS_Q + head * (unsigned)Q_HEAD + qc * 2560, kvo = (unsigned)WS_KV + (head < 32 ? 32 : head - 32) * (unsigned)KV_HEAD;
    bf16x8 q1a, q1b, k1f, k2f; s16x4 q2; float ga, gb;
    unsigned dv = kvo + w * 1024 + l * 16;
    {
        unsigned sv, dm = __builtin_amdgcn_readfirstlane(lds0 + w * 1024);
        asm volatile(
            "global_load_dwordx4 v[4:7], %[a0], %[kvp]\n\tglobal_load_dwordx4 v[8:11], %[a0], %[kvp] offset:1024\n\tglobal_load_dwordx2 v[12:13], %[a1], %[kvp] offset:2048\n\t"
            "global_load_dwordx4 " KA_ ", %[a2], %[kvp]\n\tglobal_load_dwordx4 v[" STR(KBR) ":" STR(KBR) "+3], %[a3], %[kvp] offset:2048\n\t"
            "global_load_dword v14, %[a4], %[kvp]\n\tglobal_load_dword v15, %[a4], %[kvp] offset:128\n\t"
            "s_mov_b32 %[sv], m0\n\t" DMA1 DMA1 DMA1 DMA1 "s_mov_b32 m0, %[sv]"
            : "=&{v[4:7]}"(q1a), "=&{v[8:11]}"(q1b), "=&{v[12:13]}"(q2), "=&{v14}"(ga), "=&{v15}"(gb), "=&{v[168:171]}"(k1f), "=&{v[172:175]}"(k2f),
              [sv] "=&s"(sv), [dv] "+v"(dv), [dm] "+s"(dm)
            : [kvp] "s"(ws), [a0] "v"(qo + l * 16), [a1] "v"(qo + r32 * 16 + h * 8), [a2] "v"(kvo + l * 16), [a3] "v"(kvo + r32 * 16), [a4] "v"((unsigned)WS_G + qc * 256 + r32 * 4)
            : "memory", "scc");
    }
    const int mb = xcd * 4 + (ii & 3), nb = ii >> 2;
    const char* wp = ws + WS_WO + (size_t)(nb * 8 + w) * 25600 + 2048;
    const char* cp = ws + WS_CNT + (((mb >> 3) * 8 + w) * 8 + nb) * CNT_STRIDE;
    const char* sgp = ws + WS_CNT + (qb * 8 + xcd) * 8 * CNT_STRIDE;
    const char* a1 = ws + WS_O1 + (size_t)w * 8 * 65536 + mb * 2048;
    const char* a2 = ws + WS_O2 + (size_t)w * 4 * 32768 + mb * 1024;
    const char* op = out + (size_t)mb * 64 * 1280 + nb * 160;
    const char* po1 = ws + WS_O1 + (size_t)slot * 65536 + qc * 2048;
    const char* po2 = ws + WS_O2 + (size_t)(slot >> 1) * 32768 + qc * 1024 + (slot & 1) * 8;
    f32x16 oa, ob; u32x16 vini;
#pragma unroll
    for (int i = 0; i < 16; ++i) { oa[i] = 0.f; ob[i] = 0.f; vini[i] = (r32 & 27) == 24 ? 0x3f803f80u : 0u; }
    unsigned ak1 = lds0 + l * 16, ak2 = lds0 + 2048 + r32 * 16, av = lds0 + 2560 + h * 320 + r32 * 16, wv = l * 16;
    unsigned cnt, inc, svm, dm = __builtin_amdgcn_readfirstlane(lds0 + w * 1024 + 32768);
    u32x16 wt0, wt1, wt2, wt3, wt4;
    STAMP(2);
#define LSTAMP ""
    asm volatile(
        "s_mov_b32 %[sv], m0\n\t"
        ".irp n,24,25,26,27\n\tv_mov_b32 v[" STR(S1R) "+\\n], 0\n\t.endr\n\t"
        "s_mov_b32 %[cnt], 0\n\ts_mov_b32 %[inc], 20480\n\ts_mov_b64 vcc, -1\n\t"
        "s_waitcnt vmcnt(6)\n\t"
        "v_mfma_f32_32x32x4_2b_bf16 v[" STR(S0R) ":" STR(S0R) "+31], " KB0_ ", %[q2], 0\n\ts_nop 15\n\ts_nop 1\n\t"
        M2_(S0R, 0, q1a) M2_(S0R, 16, q1b)
        "s_nop 10\n\t"
        MX3(0, S0R + 0, 1) MX3(16, S0R + 16, 17) MX3(0, NMR, 3) MX3(16, NMR + 16, 19) MX3(0, NMR, 5) MX3(16, NMR + 16, 21) MX3(0, NMR, 7) MX3(16, NMR + 16, 23)
        MX3(0, NMR, 9) MX3(16, NMR + 16, 25) MX3(0, NMR, 11) MX3(16, NMR + 16, 27) MX3(0, NMR, 13) MX3(16, NMR + 16, 29) MX3(0, NMR, 14) MX3(16, NMR + 16, 30)
        "v_mov_b32 v[" STR(NMR) "+1], v[" STR(NMR) "]\n\tv_mov_b32 v[" STR(NMR) "+17], v[" STR(NMR) "+16]\n\ts_nop 1\n\t"
        "v_permlane32_swap_b32 v[" STR(NMR) "], v[" STR(NMR) "+1]\n\tv_permlane32_swap_b32 v[" STR(NMR) "+16], v[" STR(NMR) "+17]\n\ts_nop 1\n\t"
        "v_min_f32 v[" STR(NMR) "], -v[" STR(NMR) "], -v[" STR(NMR) "+1]\n\tv_min_f32 v[" STR(NMR) "+16], -v[" STR(NMR) "+16], -v[" STR(NMR) "+17]\n\t"
        ".irp n,1,2,3,4,5,6,7,8,9,10,11,12,13,14,15\n\tv_mov_b32 v[" STR(NMR) "+\\n], v[" STR(NMR) "]\n\tv_mov_b32 v[" STR(NMR) "+16+\\n], v[" STR(NMR) "+16]\n\t.endr\n\t"
        ".irp n,0,2,4,6,8,10,12,14,16,18,20,22,24,26,28,30\n\tv_pk_add_f32 v[" STR(S0R) "+\\n:" STR(S0R) "+\\n+1], v[" STR(S0R) "+\\n:" STR(S0R) "+\\n+1], v[" STR(NMR) "+\\n:" STR(NMR) "+\\n+1]\n\t.endr\n\t"
        "s_waitcnt vmcnt(0)\n\ts_barrier\n\t"
        RDKA(1024) RDV(VAR, 0)
        "1:\n\t"
        LSTAMP
        TILE(S0R, S1R, VAR, VBR, 5120, KB1_, RDKB(5120), 1280, PRIO(3) "s_cbranch_vccnz 11f\n\t21:\n\t")
        TILE(S1R, S0R, VBR, VAR, 5120 + 1024, KB0_, "", 5120, "s_cbranch_vccnz 12f\n\t22:\n\t")
        TILE(S0R, S1R, VAR, VBR, 10240, KB1_, RDKB(10240), 5120 + 1280, "s_cbranch_vccnz 13f\n\t23:\n\t")
        TILE(S1R, S0R, VBR, VAR, 10240 + 1024, KB0_, "", 10240, "s_cbranch_vccnz 14f\n\t24:\n\t")
        TILE(S0R, S1R, VAR, VBR, 15360, KB1_, RDKB(15360), 10240 + 1280, PRIO(2))
        TILE(S1R, S0R, VBR, VAR, 15360 + 1024, KB0_, "", 15360, "s_cmp_eq_u32 %[cnt], 7\n\ts_cselect_b32 %[inc], -143360, %[inc]\n\t")
        TILE(S0R, S1R, VAR, VBR, 0, KB1_, RDKB(0), 15360 + 1280, PRIO(1) "v_add_u32 %[ak1], %[inc], %[ak1]\n\tv_add_u32 %[ak2], %[inc], %[ak2]\n\t")
        TILE(S1R, S0R, VBR, VAR, 1024, KB0_, "", 0, PRIO(0) "v_add_u32 %[av], %[inc], %[av]\n\t")
        "s_add_u32 %[cnt], %[cnt], 1\n\t"
        "s_cmp_eq_u32 %[cnt], 1\n\ts_cbranch_scc1 2f\n\t"
        "s_cmp_eq_u32 %[cnt], 3\n\ts_cbranch_scc1 3f\n\t"
        "s_cmp_eq_u32 %[cnt], 4\n\ts_cbranch_scc1 5f\n\t"
        "s_cmp_eq_u32 %[cnt], 5\n\ts_cbranch_scc1 6f\n\t"
        "s_cmp_eq_u32 %[cnt], 6\n\ts_cbranch_scc1 8f\n\t"
        "s_cmp_lt_u32 %[cnt], 8\n\ts_cbranch_scc1 1b\n\t"
        "s_branch 4f\n\t"
        "11:\n\t" DMA1 DMA1 DMA1 DMA1 "s_branch 21b\n\t"
        "12:\n\t" DMA1 DMA1 DMA1 DMA1 "s_branch 22b\n\t"
        "13:\n\t" DMA1 DMA1 DMA1 DMA1 "s_branch 23b\n\t"
        "14:\n\t" DMA1 DMA1 DMA1 DMA1 "s_branch 24b\n\t"
        "2:\n\ts_mov_b64 vcc, 0\n\ts_waitcnt vmcnt(0)\n\ts_barrier\n\ts_branch 1b\n\t"
        "3:\n\t" WLD(0)
        "5:\n\t" WLD(1)
        "6:\n\t" WLD(2)
        "8:\n\tv_and_b32 %[dv], 0x3ff, %[wv]\n\tv_lshlrev_b32 %[dv], 3, %[dv]\n\tv_min_u32 %[dv], 0x1380, %[dv]\n\tv_add_u32 %[dv], 0x5000, %[dv]\n\t"
        "global_load_dword %[dv], %[dv], %[wp] offset:-2048\n\t" WLD(3)
        "4:\n\ts_nop 1\n\t"
        LSTAMP
        PV_(ob, VBR, 4, S1R, 24)
        "s_mov_b32 m0, %[sv]\n\ts_waitcnt vmcnt(0) lgkmcnt(0)\n\ts_nop 15\n\ts_nop 7"
        : [oa] "+{v[24:39]}"(oa), [ob] "+{v[40:55]}"(ob), [ak1] "+v"(ak1), [ak2] "+v"(ak2), [av] "+v"(av), [dv] "+v"(dv), [wv] "+v"(wv),
          "+{v[152:167]}"(vini), "+{v[168:171]}"(k1f), "+{v[172:175]}"(k2f),
          [q1a] "+{v[4:7]}"(q1a), [q1b] "+{v[8:11]}"(q1b), [q2] "+{v[12:13]}"(q2), "+{v14}"(ga), "+{v15}"(gb),
          "=&{v[176:191]}"(wt0), "=&{v[192:207]}"(wt1), "=&{v[208:223]}"(wt2), "=&{v[224:239]}"(wt3), "=&{v[240:255]}"(wt4),
          [cnt] "=&s"(cnt), [inc] "=&s"(inc), [sv] "=&s"(svm), [dm] "+s"(dm)
        : [vmask] "s"(0x000fffff000fffffull), [kvp] "s"(ws), [wp] "s"(wp),
          "s"(cp), "s"(sgp), "s"(a1), "s"(a2), "s"(op), "s"(po1), "s"(po2)
        : "memory", "scc", "vcc", "v56", "v57", "v58", "v59", CL10(6), CL10(7), CL10(8), CL10(9), CL10(10), CL10(11), CL10(12), CL10(13), CL10(14), "v150", "v151"
          );
    asm volatile("v_mbcnt_lo_u32_b32 %0, -1, 0\n\tv_mbcnt_hi_u32_b32 %0, -1, %0" : "=v"(l));
    r32 = l & 31; h = l >> 5;
    STAMP(3);
    bf16x8 wl[5], af[5][4]; unsigned seen;
    {
        if (head >= 32) { ga = 1.0f - ga; gb = 1.0f - gb; }
        u32x4 va, vb; u32x2 vt;
        attn_pack(oa, ob, ga, gb, va, vb, vt);
        if (__builtin_expect(!__all(attn_ok(oa, ob)), 0)) {
#pragma unroll 1
            for (int t = 0; t < 2; ++t) {
                f32x16 ro; u32x4 v0, v1; u32x2 v2;
                attn_fallback(lds, l, ws + qo, t, ro);
                attn_pack(ro, ro, t ? gb : ga, t ? gb : ga, v0, v1, v2);
                if (t == 0) va = v0; else vb = v0;
                if (h == t) vt = v2;
            }
        }
        asm volatile(
            "global_store_dwordx4 %[lo], %[va], %[o1] sc1\n\tglobal_store_dwordx4 %[lo], %[vb], %[o1] offset:1024 sc1\n\tglobal_store_dwordx2 %[lo], %[vt], %[o2] sc1\n\t"
            "s_nop 0"
            :: [lo] "v"(l * 16), [va] "v"(va), [vb] "v"(vb), [vt] "v"(vt), [o1] "s"(po1), [o2] "s"(po2)
            : "memory");
    }
    const int r = l & 15, g = l >> 4, r8 = l & 7, g2 = (l >> 3) & 3;
    unsigned o1 = (g >> 1) * 65536 + (g & 1) * 512 + r * 16, o2 = g * 32768 + r * 16;
    unsigned wbo = w * 20480 + (g * 16 + (r ^ ((g >> 1) << 3))) * 16;
    unsigned rbo = ((w >> 1) * 5 + h) * 1024 + (g2 * 16 + ((8 * (w & 1) + r8) ^ ((g2 >> 1) << 3))) * 16;
    unsigned opo = (8 * w + r8) * 1280 + h * 32 + g2 * 8, sgo = l * CNT_STRIDE;
    asm volatile("s_waitcnt vmcnt(0)\n\ts_barrier\n\t"
                 "s_cmp_lg_u32 %[w], %[sw]\n\ts_cbranch_scc1 1f\n\ts_mov_b64 exec, 0xff\n\tglobal_atomic_add %[so], %[one], %[sp]\n\ts_mov_b64 exec, -1\n\t1:"
                 : "+v"(o1), "+v"(o2), "+v"(wbo), "+v"(rbo), "+v"(opo)
                 : [w] "s"(w), [sw] "s"(xcd ^ 1), [so] "v"(sgo), [one] "v"(1), [sp] "s"(sgp) : "memory", "scc");
    STAMP(4);
    asm volatile(
        "global_load_dwordx4 %0, %6, %7 offset:-2048\n\tglobal_load_dwordx4 %1, %6, %7 offset:-1024\n\tglobal_load_dwordx4 %2, %6, %7\n\t"
        "global_load_dwordx4 %3, %6, %7 offset:1024\n\tglobal_load_dwordx4 %4, %6, %7 offset:2048\n\tglobal_load_dword %5, %8, %9 sc1\n\ts_waitcnt vmcnt(0)"
        : "=&v"(wl[0]), "=&v"(wl[1]), "=&v"(wl[2]), "=&v"(wl[3]), "=&v"(wl[4]), "=&v"(seen) : "v"(wv), "s"(wp), "v"(0), "s"(cp) : "memory");
    const unsigned late = __builtin_amdgcn_readfirstlane(seen);
#pragma unroll 1
    for (int spin = 0; spin < 400000 && __builtin_amdgcn_readfirstlane(seen) < 8; ++spin) {
        __builtin_amdgcn_s_sleep(POLL_SLEEP);
        asm volatile("global_load_dword %0, %1, %2 sc1\n\ts_waitcnt vmcnt(0)" : "=v"(seen) : "v"(0), "s"(cp) : "memory");
    }
    STAMP(5);
#define ALD(s, base, off, d1, d2) asm volatile( \
        "global_load_dwordx4 %0, %4, %5 sc1\n\tglobal_load_dwordx4 %1, %4, %5 offset:" #d1 " sc1\n\t" \
        "global_load_dwordx4 %2, %4, %5 offset:" #d2 " sc1\n\tglobal_load_dwordx4 %3, %4, %5 offset:" #d1 "+" #d2 " sc1" \
        : "=&v"(af[s][0]), "=&v"(af[s][1]), "=&v"(af[s][2]), "=&v"(af[s][3]) : "v"(off), "s"(base) : "memory")
#define AWAIT(s, N) asm volatile("s_waitcnt vmcnt(" STR(N) ")" : "+v"(af[s][0]), "+v"(af[s][1]), "+v"(af[s][2]), "+v"(af[s][3]) :: "memory")
#define OMM(s, w0, w1, w2, w3, w4) do { const bf16x8 wf_[5] = {w0, w1, w2, w3, w4}; \
        _Pragma("unroll") for (int j = 0; j < 5; ++j) _Pragma("unroll") for (int i = 0; i < 4; ++i) acc[i][j] = MFMA16(wf_[j], af[s][i], acc[i][j]); \
        __builtin_amdgcn_sched_barrier(0); } while (0)
    f32x4 acc[4][5];
#pragma unroll
    for (int i = 0; i < 4; ++i)
#pragma unroll
        for (int j = 0; j < 5; ++j) acc[i][j] = f32x4{0.f, 0.f, 0.f, 0.f};
#define LD4(a, b, c, d, off, base, d1, d2) \
        "global_load_dwordx4 %" #a ", %" #off ", %" #base " sc1\n\tglobal_load_dwordx4 %" #b ", %" #off ", %" #base " offset:" #d1 " sc1\n\t" \
        "global_load_dwordx4 %" #c ", %" #off ", %" #base " offset:" #d2 " sc1\n\tglobal_load_dwordx4 %" #d ", %" #off ", %" #base " offset:" #d1 "+" #d2 " sc1\n\t"
    asm volatile(
        LD4(0, 1, 2, 3, 20, 22, 256, 1024) LD4(4, 5, 6, 7, 20, 23, 256, 1024)
        "s_cmp_ge_u32 %27, 8\n\ts_cbranch_scc1 1f\n\t"
        LD4(8, 9, 10, 11, 20, 24, 256, 1024) LD4(12, 13, 14, 15, 20, 25, 256, 1024) LD4(16, 17, 18, 19, 21, 26, 256, 512)
        "s_waitcnt vmcnt(16)\n\ts_branch 2f\n\t"
        "1:\n\ts_waitcnt vmcnt(4)\n\t" LD4(8, 9, 10, 11, 20, 24, 256, 1024)
        "2:"
        : "=&v"(af[0][0]), "=&v"(af[0][1]), "=&v"(af[0][2]), "=&v"(af[0][3]), "=&v"(af[1][0]), "=&v"(af[1][1]), "=&v"(af[1][2]), "=&v"(af[1][3]),
          "=&v"(af[2][0]), "=&v"(af[2][1]), "=&v"(af[2][2]), "=&v"(af[2][3]), "=&v"(af[3][0]), "=&v"(af[3][1]), "=&v"(af[3][2]), "=&v"(af[3][3]),
          "=&v"(af[4][0]), "=&v"(af[4][1]), "=&v"(af[4][2]), "=&v"(af[4][3])
        : "v"(o1), "v"(o2), "s"(a1), "s"(a1 + 131072), "s"(a1 + 2 * 131072), "s"(a1 + 3 * 131072), "s"(a2), "s"(late) : "memory", "scc");
#define ANEXT(s, n, N, off, base, d2) asm volatile( \
        "s_cmp_ge_u32 %10, 8\n\ts_cbranch_scc1 1f\n\ts_waitcnt vmcnt(" #N ")\n\ts_branch 2f\n\t1:\n\ts_waitcnt vmcnt(4)\n\t" LD4(4, 5, 6, 7, 8, 9, 256, d2) "2:" \
        : "+v"(af[s][0]), "+v"(af[s][1]), "+v"(af[s][2]), "+v"(af[s][3]), "+v"(af[n][0]), "+v"(af[n][1]), "+v"(af[n][2]), "+v"(af[n][3]) \
        : "v"(off), "s"(base), "s"(late) : "memory", "scc")
    OMM(0, WSUB(wt0, 0), WSUB(wt0, 1), WSUB(wt0, 2), WSUB(wt0, 3), WSUB(wt1, 0));
    ANEXT(1, 3, 12, o1, a1 + 3 * 131072, 1024); OMM(1, WSUB(wt1, 1), WSUB(wt1, 2), WSUB(wt1, 3), WSUB(wt2, 0), WSUB(wt2, 1));
    ANEXT(2, 4, 8, o2, a2, 512); OMM(2, WSUB(wt2, 2), WSUB(wt2, 3), WSUB(wt3, 0), WSUB(wt3, 1), WSUB(wt3, 2));
    AWAIT(3, 4); OMM(3, WSUB(wt3, 3), WSUB(wt4, 0), WSUB(wt4, 1), WSUB(wt4, 2), WSUB(wt4, 3));
    AWAIT(4, 0);
#pragma unroll
    for (int j = 0; j < 6; ++j) {
        if (j < 5) {
#pragma unroll
            for (int i = 0; i < 4; ++i) acc[i][j] = MFMA16(wl[j], af[4][i], acc[i][j]);
        }
        if (j > 0) {
#pragma unroll
            for (int i = 0; i < 4; ++i) *(f32x4*)(lds + wbo + (i * 5 + j - 1) * 1024) = acc[i][j - 1];
        }
        __builtin_amdgcn_sched_barrier(0);
    }
    STAMP(6);
    {
        __syncthreads();
        f32x4 t[3][8];
#pragma unroll
        for (int u = 0; u < 8; ++u)
#pragma unroll
            for (int p = 0; p < 3; ++p) t[p][u] = *(const f32x4*)(lds + rbo + u * 20480 + (p < 2 ? p * 2048 : 4096 - h * 1024));
        __builtin_amdgcn_sched_barrier(0);
#pragma unroll
        for (int p = 0; p < 3; ++p) {
            const f32x4 z = ((t[p][0] + t[p][1]) + (t[p][2] + t[p][3])) + ((t[p][4] + t[p][5]) + (t[p][6] + t[p][7]));
            const u32x2 v = {pk_bf16(z[0], z[1]), pk_bf16(z[2], z[3])};
            if (p < 2 || h == 0) asm volatile("global_store_dwordx2 %0, %1, %2 offset:%3 " OUT_FLAGS :: "v"(opo), "v"(v), "s"(op), "i"(p * 64) : "memory");
        }
    }
    STAMP(7); STAMP_FLUSH(ws, 1);
}

extern "C" void kernel_launch(void* const* d_in, const int* in_sizes, int n_in, void* d_out, int out_size, void* d_ws, size_t ws_size,
                              hipStream_t stream) {
    const char* x = (const char*)d_in[0];
    const char* wq = (const char*)d_in[1];
    const char* wk = (const char*)d_in[2];
    const char* wv = (const char*)d_in[3];
    const char* woa = (const char*)d_in[4];
    const char* wqkv = (const char*)d_in[6];
    const char* wob = (const char*)d_in[7];
    const char* gw = (const char*)d_in[8];
    char* ws = (char*)d_ws;
    k_proj<<<256, 768, 0, stream>>>(x, wq, wqkv, wk, wv, gw, woa, wob, ws);
    k_attn<<<256, 512, 0, stream>>>(ws, (char*)d_out);
}
```

```cpp
#include <hip/hip_runtime.h>
#include <hip/hip_bf16.h>
#include <stdint.h>

typedef __attribute__((ext_vector_type(8))) short bf16x8;
typedef __attribute__((ext_vector_type(4))) short s16x4;
typedef __attribute__((ext_vector_type(4))) float f32x4;
typedef __attribute__((ext_vector_type(16))) float f32x16;
typedef __attribute__((ext_vector_type(32))) float f32x32;
typedef __attribute__((ext_vector_type(2))) unsigned u32x2;
typedef __attribute__((ext_vector_type(4))) unsigned u32x4;
typedef __attribute__((ext_vector_type(2))) __bf16 bf16x2_t;
typedef __attribute__((ext_vector_type(2))) float f32x2;
#define LDSP __attribute__((address_space(3)))
#define DI __device__ __forceinline__

#define STAMP_PARAMS
#define STAMP_ARGS
#define STAMP_DECL
#define STAMP(slot)
#define STAMP_FLUSH(wsp, kid)
constexpr int SEQ = 2048, DM = 640;
constexpr size_t Q_HEAD = 32 * 2560;
constexpr size_t KV_HEAD = 32 * 5120;
constexpr size_t WS_Q = 0;
constexpr size_t WS_KV = WS_Q + 64 * Q_HEAD;
constexpr size_t WS_G = WS_KV + 33 * KV_HEAD;
constexpr size_t WS_O1 = WS_G + 2048 * 4;
constexpr size_t WS_O2 = WS_O1 + 64 * 65536;
constexpr size_t WS_WO = WS_O2 + 32 * 2048 * 16;
#define OUT_FLAGS ""
#define XT_WIN 3
#define CNT_STRIDE 128
#define POLL_SLEEP 4
constexpr size_t WS_CNT = WS_WO + 1600 * 1024;

DI unsigned pk_bf16(float a, float b) {
    f32x2 v = {a, b};
    return __builtin_bit_cast(unsigned, __builtin_convertvector(v, bf16x2_t));
}
#define ST_FLAGS "sc1"
DI void st16(char* p, u32x4 v) { asm volatile("global_store_dwordx4 %0, %1, off " ST_FLAGS "\n\ts_nop 1" :: "v"(p), "v"(v) : "memory"); }
DI void st8(char* p, u32x2 v) { asm volatile("global_store_dwordx2 %0, %1, off " ST_FLAGS :: "v"(p), "v"(v) : "memory"); }
DI s16x4 tr_read(const LDSP char* p) {
    typedef short v4i16_t __attribute__((ext_vector_type(4)));
    return __builtin_amdgcn_ds_read_tr16_b64_v4i16((LDSP v4i16_t*)p);
}
DI bf16x8 cat8(s16x4 lo, s16x4 hi) { return __builtin_shufflevector(lo, hi, 0, 1, 2, 3, 4, 5, 6, 7); }
#define MFMA16(a, b, c) __builtin_amdgcn_mfma_f32_16x16x32_bf16((a), (b), (c), 0, 0, 0)
#define MFMA32(a, b, c) __builtin_amdgcn_mfma_f32_32x32x16_bf16((a), (b), (c), 0, 0, 0)

constexpr int P_SLOT = 40960;

DI void proj_loader(char* lds, const char* xs, const char* ws, const char* es, int sw, int se, unsigned long long m5, int st, int ekind, int ehalf, int mb,
                    const char* woa, const char* wob, char* wsp, const unsigned (&xo)[2], const unsigned (&wo)[3]) {
    const int l = threadIdx.x & 63, w = __builtin_amdgcn_readfirstlane(threadIdx.x >> 6), r = l & 15, g = l >> 4;
    const unsigned dw = __builtin_amdgcn_readfirstlane((unsigned)(uintptr_t)lds + w * 1024);
    auto issue = [&](int slot) {
        const unsigned d0 = dw + slot * P_SLOT;
        unsigned sv;
        asm volatile(
            "s_mov_b32 %[sv], m0\n\ts_mov_b32 m0, %[d0]\n\ts_nop 0\n\t"
            "global_load_lds_dwordx4 %[x0], %[xs]\n\ts_add_u32 m0, m0, 8192\n\ts_nop 0\n\t"
            "global_load_lds_dwordx4 %[x1], %[xs]\n\ts_add_u32 m0, m0, 8192\n\ts_nop 0\n\t"
            "global_load_lds_dwordx4 %[w0], %[ws]\n\ts_add_u32 m0, m0, 8192\n\ts_nop 0\n\t"
            "global_load_lds_dwordx4 %[w1], %[ws]\n\ts_add_u32 m0, m0, 8192\n\ts_mov_b64 exec, %[m5]\n\t"
            "global_load_lds_dwordx4 %[w2], %[es]\n\ts_mov_b64 exec, -1\n\ts_mov_b32 m0, %[sv]"
            : [sv] "=&s"(sv)
            : [d0] "s"(d0), [m5] "s"(m5), [xs] "s"(xs), [ws] "s"(ws), [es] "s"(es),
              [x0] "v"(xo[0]), [x1] "v"(xo[1]), [w0] "v"(wo[0]), [w1] "v"(wo[1]), [w2] "v"(wo[2])
            : "memory", "scc");
        const int d = st == 9 ? -9 : 1;
        st += d; xs += d * 128; ws += d * sw; es += d * se;
    };
    issue(0); issue(1);
    __builtin_amdgcn_s_setprio(3);
    f32x4 eacc = {0.f, 0.f, 0.f, 0.f};
    const int ets = 4 * ehalf + w;
    if (w >= 4) ekind = 5;
    const int exr = (16 * ets + r) * 128 + ((g ^ ((r >> 1) & 7)) << 4);
    const int ewr = ekind < 4 ? 36864 + (8 * g + (r >> 2)) * 40 + ((ekind & 1) ? 32 : 8 * (r & 3)) : 36864 + g * 16;
    auto extra = [&](int k) {
        if (ekind < 5) {
            const LDSP char* sb = (const LDSP char*)lds + (k & 3) * P_SLOT;
            const bf16x8 x0 = *(const LDSP bf16x8*)(sb + exr), x1 = *(const LDSP bf16x8*)(sb + (exr ^ 64));
            bf16x8 w0, w1;
            if (ekind < 4) { w0 = cat8(tr_read(sb + ewr), tr_read(sb + ewr + 160)); w1 = cat8(tr_read(sb + ewr + 1280), tr_read(sb + ewr + 1440)); }
            else { w0 = *(const LDSP bf16x8*)(sb + ewr); w1 = *(const LDSP bf16x8*)(sb + ewr + 64); }
            if ((ekind >> 1) == 1) { eacc = MFMA16(x0, w0, eacc); eacc = MFMA16(x1, w1, eacc); }
            else { eacc = MFMA16(w0, x0, eacc); eacc = MFMA16(w1, x1, eacc); }
            asm volatile("" : "+v"(eacc));
        }
    };
#pragma unroll 1
    for (int k = 0; k < 8; ++k) {
        asm volatile("s_waitcnt vmcnt(5)\n\ts_barrier" ::: "memory");
        issue((k + 2) & 3);
        if (k < 7) extra(k);
    }
    const int gw = blockIdx.x * 8 + w, fid = gw < 1600 ? gw : 1599;
    const int fj = fid % 5, fs = (fid / 5) % 5, fg = (fid / 25) & 7, fn = fid / 200;
    const int br = fs < 4 ? fs >> 1 : g >> 1;
    const int row = fs < 4 ? (4 * fg + 2 * (fs & 1) + (g >> 1)) * 20 + 4 * (g & 1) : (4 * fg + 2 * (g & 1)) * 20 + 16;
    const char* p0 = (br ? wob : woa) + row * 1280 + (80 * fn + 16 * fj + r) * 2;
    const char* p1 = p0 + (fs < 4 ? 8 : 20) * 1280;
    unsigned e0, e1, e2, e3, e4, e5, e6, e7;
    asm volatile(
        "global_load_ushort %0, %8, off\n\tglobal_load_ushort %1, %8, off offset:1280\n\tglobal_load_ushort %2, %8, off offset:2560\n\tglobal_load_ushort %3, %8, off offset:3840\n\t"
        "global_load_ushort %4, %9, off\n\tglobal_load_ushort %5, %9, off offset:1280\n\tglobal_load_ushort %6, %9, off offset:2560\n\tglobal_load_ushort %7, %9, off offset:3840"
        : "=&v"(e0), "=&v"(e1), "=&v"(e2), "=&v"(e3), "=&v"(e4), "=&v"(e5), "=&v"(e6), "=&v"(e7) : "v"(p0), "v"(p1) : "memory");
    extra(7);
    asm volatile("s_waitcnt vmcnt(13)\n\ts_barrier" ::: "memory");
    extra(8);
    asm volatile("s_waitcnt vmcnt(8)\n\ts_barrier" ::: "memory");
    extra(9);
    asm volatile("s_waitcnt vmcnt(0)" : "+v"(e0), "+v"(e1), "+v"(e2), "+v"(e3), "+v"(e4), "+v"(e5), "+v"(e6), "+v"(e7) :: "memory");
    {
        char* kva = wsp + WS_KV + 32 * KV_HEAD + (mb * 2 + ehalf) * 5120;
        const u32x2 v = {pk_bf16(eacc[0], eacc[1]), pk_bf16(eacc[2], eacc[3])};
        const int t = (w >> 1) & 1, erow = 16 * (r >> 3) + 8 * (w & 1) + (r & 7);
        if (ekind == 0) st8(kva + t * 1024 + (g >> 1) * 512 + erow * 16 + (g & 1) * 8, v);
        else if (ekind == 1) { if (g == 0) st8(kva + 2048 + erow * 16 + t * 8, v); }
        else if (ekind < 4) { if (ekind == 2 || r < 4) st8(kva + 2560 + t * 1280 + g * 320 + ((ekind & 1) * 16 + r) * 16 + (w & 1) * 8, v); }
        else if (ekind == 4) { if (g == 0) ((float*)(wsp + WS_G))[mb * 128 + 16 * ets + r] = 1.0f / (1.0f + __expf(-eacc[0])); }
    }
    if (gw < 1600) st16(wsp + WS_WO + (size_t)fid * 1024 + l * 16, u32x4{e0 | (e1 << 16), e2 | (e3 << 16), e4 | (e5 << 16), e6 | (e7 << 16)});
    if (w == 0 && l == 0) *(unsigned*)(wsp + WS_CNT + blockIdx.x * CNT_STRIDE) = 0;
}

#define STR_(x) #x
#define STR(x) STR_(x)
#define PR4(b) "v[" STR(b) ":" STR(b) "+3]"
#define PM_F(j, i, X) "v_mfma_f32_16x16x32_bf16 " PR4(20 + 16 * j + 4 * i) ", " PR4(136 + 4 * j) ", " PR4(X + 4 * i) ", " PR4(20 + 16 * j + 4 * i) "\n\t"
#define PM_T(j, i, X) "v_mfma_f32_16x16x32_bf16 " PR4(20 + 16 * j + 4 * i) ", " PR4(X + 4 * i) ", " PR4(136 + 4 * j) ", " PR4(20 + 16 * j + 4 * i) "\n\t"
#define PTL(j, sl, ks) "ds_read_b64_tr_b16 v[136+4*" #j ":136+4*" #j "+1], %[aw" #j "] offset:" #sl "*40960+" #ks "*10240\n\t"
#define PTH(j, sl, ks) "ds_read_b64_tr_b16 v[136+4*" #j "+2:136+4*" #j "+3], %[aw" #j "] offset:" #sl "*40960+" #ks "*10240+1280\n\t"
#define PBX(X, i, sl, ks) "ds_read_b128 " PR4(X + 4 * i) ", %[ax" #ks "] offset:" #sl "*40960+" #i "*2048\n\t"
#define PTG(a) "v_add_u32 %[" #a "], %[tg], %[" #a "]\n\t"
#define PSTEP(M, X, Y, cs, ck, ns, nk, BAR, F0, F1, F2, F3) \
    M(0, 0, X) PTL(4, cs, ck) M(0, 1, X) PTH(4, cs, ck) M(0, 2, X) BAR PBX(Y, 0, ns, nk) M(0, 3, X) PBX(Y, 1, ns, nk) \
    M(1, 0, X) PTL(0, ns, nk) M(1, 1, X) PTH(0, ns, nk) M(1, 2, X) PBX(Y, 2, ns, nk) M(1, 3, X) "s_waitcnt lgkmcnt(5)\n\t" \
    M(2, 0, X) PTL(1, ns, nk) M(2, 1, X) PTH(1, ns, nk) M(2, 2, X) PBX(Y, 3, ns, nk) M(2, 3, X) F0 \
    M(3, 0, X) PTL(2, ns, nk) M(3, 1, X) PTH(2, ns, nk) M(3, 2, X) F1 M(3, 3, X) F2 \
    M(4, 0, X) PTL(3, ns, nk) M(4, 1, X) PTH(3, ns, nk) M(4, 2, X) F3 M(4, 3, X) "s_waitcnt lgkmcnt(4)\n\t"
#define CL10(a) "v" #a "0", "v" #a "1", "v" #a "2", "v" #a "3", "v" #a "4", "v" #a "5", "v" #a "6", "v" #a "7", "v" #a "8", "v" #a "9"
#define PLOOP(M) \
    asm volatile( \
        "s_mov_b32 %[cnt], 0\n\ts_mov_b32 %[tg], 81920\n\t" \
        "s_barrier\n\t" \
        PBX(104, 0, 0, 0) PBX(104, 1, 0, 0) PTL(0, 0, 0) PTH(0, 0, 0) PBX(104, 2, 0, 0) PTL(1, 0, 0) PTH(1, 0, 0) PBX(104, 3, 0, 0) \
        PTL(2, 0, 0) PTH(2, 0, 0) PTL(3, 0, 0) PTH(3, 0, 0) "s_waitcnt lgkmcnt(4)\n\t" \
        "1:\n\t" \
        PSTEP(M, 104, 120, 0, 0, 0, 1, "", "", "", "", "") \
        PSTEP(M, 120, 104, 0, 1, 1, 0, "s_barrier\n\t", PTG(ax0), "s_add_u32 %[cnt], %[cnt], 1\n\t", "", "") \
        PSTEP(M, 104, 120, 1, 0, 1, 1, "", PTG(aw0), PTG(aw1), PTG(aw2), PTG(ax1)) \
        PSTEP(M, 120, 104, 1, 1, 0, 0, "s_cmp_eq_u32 %[cnt], 5\n\ts_cbranch_scc1 2f\n\ts_barrier\n\t2:\n\t", PTG(aw3), PTG(aw4), "s_sub_u32 %[tg], 0, %[tg]\n\t", \
              "s_cmp_lg_u32 %[cnt], 5\n\t") \
        "s_cbranch_scc1 1b\n\t" \
        "s_waitcnt lgkmcnt(0)\n\ts_nop 7" \
        : "+{v[20:35]}"(acc[0]), "+{v[36:51]}"(acc[1]), "+{v[52:67]}"(acc[2]), "+{v[68:83]}"(acc[3]), "+{v[84:99]}"(acc[4]), \
          [ax0] "+v"(ax0), [ax1] "+v"(ax1), [aw0] "+v"(aw[0]), [aw1] "+v"(aw[1]), [aw2] "+v"(aw[2]), [aw3] "+v"(aw[3]), [aw4] "+v"(aw[4]), \
          [cnt] "=&s"(cnt), [tg] "=&s"(tg) \
        : \
        : "memory", "scc", "v104", "v105", "v106", "v107", "v108", "v109", CL10(11), CL10(12), CL10(13), CL10(14), "v150", "v151", "v152", "v153", "v154", "v155")

template <bool VT>
DI void proj_mma(unsigned lds0, f32x16 (&acc)[5]) {
    const int tid = threadIdx.x, l = tid & 63, w = __builtin_amdgcn_readfirstlane(tid >> 6) - 8;
    const int wm = w >> 1, wn = w & 1, r = l & 15, g = l >> 4;
    const int xsw = (g ^ ((r >> 1) & 7)) << 4;
    unsigned ax0 = lds0 + (64 * wm + r) * 128 + xsw, ax1 = lds0 + (64 * wm + r) * 128 + (xsw ^ 64);
    unsigned aw[5];
    const int q = r >> 2, p = r & 3;
#pragma unroll
    for (int j = 0; j < 5; ++j) {
        int col;
        if (j == 4) col = (4 * wn + p) * 20 + 16;
        else if (VT) col = (4 * wn + j) * 20 + 4 * p;
        else col = (4 * wn + (j & 2) + (p >> 1)) * 20 + 8 * (p & 1) + 4 * (j & 1);
        aw[j] = lds0 + 16384 + (8 * g + q) * 320 + ((col * 2) ^ ((g & 1) << 5));
    }
    unsigned cnt, tg;
    if constexpr (VT) PLOOP(PM_T); else PLOOP(PM_F);
}
#define ACC(i, j) f32x4{acc[j][4 * (i)], acc[j][4 * (i) + 1], acc[j][4 * (i) + 2], acc[j][4 * (i) + 3]}

DI u32x4 pk8(const f32x4& a, const f32x4& b, float sc) {
    return u32x4{pk_bf16(a[0] * sc, a[1] * sc), pk_bf16(a[2] * sc, a[3] * sc), pk_bf16(b[0] * sc, b[1] * sc), pk_bf16(b[2] * sc, b[3] * sc)};
}

#define CODE_PROJ "_Z6k_projPKcS0_S0_S0_S0_S0_S0_S0_PcS0_"
#define CODE_ATTN "_Z6k_attnPcS_"
#define CODE_BYTES 8192
__global__ __launch_bounds__(768, 3) void k_proj(const char* __restrict__ x, const char* __restrict__ wq, const char* __restrict__ wqkv,
                                                 const char* __restrict__ wk, const char* __restrict__ wv, const char* __restrict__ gw,
                                                 const char* __restrict__ woa, const char* __restrict__ wob, char* __restrict__ ws, const char* out) {
    __shared__ __attribute__((aligned(16))) char lds[4 * P_SLOT];
    STAMP_DECL; STAMP(0);
    const int b = blockIdx.x, xcd = b & 7, ii = b >> 3;
    const int mb = (xcd & 1) * 8 + (ii & 7), type = xcd >> 1, nq = ii >> 3;
    const int tid = threadIdx.x, l = tid & 63, wv_ = __builtin_amdgcn_readfirstlane(tid >> 6);
    const int ldw2 = type == 0 ? 1280 : 3840;
    const int eidx = ((nq >> 1) | ((nq & 1) << 1)) * 4 + type, ehalf = eidx >= 5, ekind = eidx < 10 ? eidx - 5 * ehalf : 5;
    int rot = (ii & 7) + nq + type; rot = 2 * (rot >= 10 ? rot - 10 : (rot >= 5 ? rot - 5 : rot));
    const int ebytes = ekind < 4 ? 2560 : 128, nbytes = ebytes - (wv_ - 4) * 1024;
    const unsigned long long m5 = ~0ull >> (nbytes >= 1024 ? 0 : (nbytes > 0 ? 64 - (nbytes >> 4) : 63));
    const int sw = 64 * ldw2, se = wv_ < 4 ? sw : ebytes, wofs = (type > 1 ? (type - 1) * 1280 : 0) + nq * 320 + rot * sw;
    unsigned xo[2], wo[3];
    xo[0] = (mb * 128 + (tid >> 3)) * (DM * 2) + (((tid ^ (tid >> 4)) & 7) << 4) + rot * 128; xo[1] = xo[0] + 64 * DM * 2;
#pragma unroll
    for (int n = 0; n < 3; ++n) { const int cid = tid + 512 * n, k = (cid * 3277) >> 16, c = cid - 20 * k; wo[n] = k * ldw2 + ((c ^ (((k >> 3) & 1) << 1)) << 4) + wofs; }
    if (tid >= 256) wo[2] = l * 16 + (nbytes > 0 ? (wv_ - 4) * 1024 : 0) + rot * ebytes;
    asm volatile("" : "+v"(xo[0]), "+v"(xo[1]), "+v"(wo[0]), "+v"(wo[1]), "+v"(wo[2])
                 : "s"(x), "s"(wq), "s"(wqkv), "s"(wk), "s"(wv), "s"(gw), "s"(woa), "s"(wob), "s"(ws), "s"(m5), "s"(se), "s"(ekind));
    if (__builtin_expect(wv_ < 8, 1)) {
        const char* wsel = type == 0 ? wq : wqkv;
        proj_loader(lds, x, wsel, wv_ < 4 ? wsel : (ekind < 2 ? wk : (ekind < 4 ? wv : gw)), sw, se, m5, rot, ekind, ehalf, mb, woa, wob, ws, xo, wo);
        return;
    }
    const int w = wv_ - 8;
    if (w < 2) {
        const unsigned dp = (unsigned)(uintptr_t)lds + P_SLOT - 512, off = (ii * 3 + l) * 128; unsigned sv; unsigned long long pc;
        if (w == 0) asm volatile("s_getpc_b64 s[40:41]\n\ts_add_u32 s40, s40, " CODE_PROJ "@rel32@lo+4\n\ts_addc_u32 s41, s41, " CODE_PROJ "@rel32@hi+12" : "={s[40:41]}"(pc) :: "scc");
        else asm volatile("s_getpc_b64 s[40:41]\n\ts_add_u32 s40, s40, " CODE_ATTN "@rel32@lo+4\n\ts_addc_u32 s41, s41, " CODE_ATTN "@rel32@hi+12" : "={s[40:41]}"(pc) :: "scc");
        if (l < 3 && (w == 0 || off < CODE_BYTES))
            asm volatile("s_mov_b32 %0, m0\n\ts_mov_b32 m0, %1\n\ts_nop 0\n\tglobal_load_lds_dword %2, %3\n\ts_mov_b32 m0, %0" : "=&s"(sv) : "s"(dp), "v"(off), "s"(pc) : "memory");
    }
    const int wm = w >> 1, wn = w & 1, r = l & 15, g = l >> 4;
    f32x16 acc[5];
#pragma unroll
    for (int j = 0; j < 5; ++j)
#pragma unroll
        for (int i = 0; i < 16; ++i) acc[j][i] = 0.f;
    const unsigned lds0 = (unsigned)(uintptr_t)lds;
    const int chunk = mb * 2 + wm;
    if (type == 3) {
        proj_mma<true>(lds0, acc);
        STAMP(3);
        char* base = ws + WS_KV + (size_t)(nq * 8 + 4 * wn) * KV_HEAD + chunk * 5120 + 2560 + g * 320;
#pragma unroll
        for (int t = 0; t < 2; ++t) {
#pragma unroll
            for (int j = 0; j < 4; ++j) st16(base + (size_t)j * KV_HEAD + t * 1280 + r * 16, pk8(ACC(2 * t, j), ACC(2 * t + 1, j), 1.0f));
            st16(base + (size_t)(r >> 2) * KV_HEAD + t * 1280 + (16 + (r & 3)) * 16, pk8(ACC(2 * t, 4), ACC(2 * t + 1, 4), 1.0f));
        }
    } else {
        proj_mma<false>(lds0, acc);
        STAMP(3);
        const float sc = type == 2 ? 1.0f : 0.32259784f;
        const size_t hstride = type == 2 ? KV_HEAD : Q_HEAD;
        const int row0 = type == 2 ? 16 * (r >> 3) + (r & 7) : r, ustep = type == 2 ? 128 : 256;
        char* base = (type == 2 ? ws + WS_KV + chunk * 5120 : ws + WS_Q + (size_t)type * 32 * Q_HEAD + chunk * 2560) + (size_t)(nq * 8 + 4 * wn) * hstride + row0 * 16;
#pragma unroll
        for (int i = 0; i < 4; ++i)
#pragma unroll
            for (int jj = 0; jj < 2; ++jj)
                st16(base + (2 * jj + (g >> 1)) * hstride + (i >> 1) * 1024 + (g & 1) * 512 + (i & 1) * ustep, pk8(ACC(i, 2 * jj), ACC(i, 2 * jj + 1), sc));
#pragma unroll
        for (int u = 0; u < 2; ++u) st16(base + g * hstride + 2048 + u * ustep, pk8(ACC(u, 4), ACC(u + 2, 4), sc));
    }
    STAMP(4); STAMP_FLUSH(ws, 0);
}

#define S0R 56
#define S1R 88
#define NMR 120
#define VAR 152
#define VBR 160
#define KAR 168
#define KBR 172
#define WFR 176
#define E_(X, i) "v_exp_f32 v[" STR(X) "+" #i "], v[" STR(X) "+" #i "]\n\t"
#define C_(X, d, a, b) "v_cvt_pk_bf16_f32 v[" STR(X) "+" #d "], v[" STR(X) "+" #a "], v[" STR(X) "+" #b "]\n\t"
#define PV_(O, V, vo, X, po) "v_mfma_f32_32x32x16_bf16 %[" #O "], v[" STR(V) "+" #vo ":" STR(V) "+" #vo "+3], v[" STR(X) "+" #po ":" STR(X) "+" #po "+3], %[" #O "]\n\t"
#define M1_(Y, KB) "v_mfma_f32_32x32x4_2b_bf16 v[" STR(Y) ":" STR(Y) "+31], " KB ", %[q2], v[" STR(NMR) ":" STR(NMR) "+31]\n\t"
#define M2_(Y, o, Q) "v_mfma_f32_32x32x16_bf16 v[" STR(Y) "+" #o ":" STR(Y) "+" #o "+15], " KA_ ", %[" #Q "], v[" STR(Y) "+" #o ":" STR(Y) "+" #o "+15]\n\t"
#define KA_ "v[" STR(KAR) ":" STR(KAR) "+3]"
#define KB0_ "v[" STR(KBR) ":" STR(KBR) "+1]"
#define KB1_ "v[" STR(KBR) "+2:" STR(KBR) "+3]"
#define RDKA(off) "ds_read_b128 " KA_ ", %[ak1] offset:" STR(off) "\n\t"
#define RDKB(off) "ds_read_b128 v[" STR(KBR) ":" STR(KBR) "+3], %[ak2] offset:" STR(off) "\n\t"
#define RDV(V, off) "s_mov_b64 exec, %[vmask]\n\tds_read_b128 v[" STR(V) ":" STR(V) "+3], %[av] offset:" STR(off) "\n\t" \
                    "ds_read_b128 v[" STR(V) "+4:" STR(V) "+7], %[av] offset:" STR(off) "+640\n\ts_mov_b64 exec, -1\n\t"
#define MX3(d, a, i) "v_max3_f32 v[" STR(NMR) "+" #d "], v[" STR(a) "], v[" STR(S0R) "+" #i "], v[" STR(S0R) "+" #i "+1]\n\t"
#define PRIO(n) "s_setprio " #n "\n\t"
#define DMA1 "s_mov_b32 m0, %[dm]\n\ts_add_u32 %[dm], %[dm], 8192\n\tglobal_load_lds_dwordx4 %[dv], %[kvp]\n\tv_add_u32 %[dv], 0x2000, %[dv]\n\t"
#define WLD1(s, j) "global_load_dwordx4 v[" STR(WFR) "+20*" #s "+4*" #j ":" STR(WFR) "+20*" #s "+4*" #j "+3], %[wv], %[wp] offset:" #j "*1024-2048\n\t"
#define WLD(s) WLD1(s, 0) WLD1(s, 1) WLD1(s, 2) WLD1(s, 3) WLD1(s, 4) "v_add_u32 %[wv], 0x1400, %[wv]\n\ts_branch 1b\n\t"
#define TILE(X, Y, VC, VP, KAOFF, KB, KBRD, VOFF, PRE) \
    PRE E_(X, 0) E_(X, 1) \
    PV_(ob, VP, 4, Y, 24) \
    E_(X, 2) E_(X, 3) C_(X, 0, 0, 1) E_(X, 4) E_(X, 5) C_(X, 1, 2, 3) \
    "s_waitcnt lgkmcnt(0)\n\t" M1_(Y, KB) KBRD \
    E_(X, 6) E_(X, 7) C_(X, 2, 4, 5) E_(X, 8) C_(X, 3, 6, 7) E_(X, 9) E_(X, 10) E_(X, 11) C_(X, 8, 8, 9) \
    PV_(oa, VC, 0, X, 0) RDV(VP, VOFF) \
    E_(X, 12) C_(X, 9, 10, 11) E_(X, 13) E_(X, 14) E_(X, 15) C_(X, 10, 12, 13) E_(X, 16) C_(X, 11, 14, 15) E_(X, 17) \
    M2_(Y, 0, q1a) \
    E_(X, 18) E_(X, 19) C_(X, 16, 16, 17) E_(X, 20) E_(X, 21) C_(X, 17, 18, 19) \
    PV_(oa, VC, 4, X, 8) \
    E_(X, 22) E_(X, 23) C_(X, 18, 20, 21) E_(X, 24) C_(X, 19, 22, 23) E_(X, 25) \
    M2_(Y, 16, q1b) RDKA(KAOFF) \
    E_(X, 26) E_(X, 27) C_(X, 24, 24, 25) E_(X, 28) E_(X, 29) C_(X, 25, 26, 27) \
    PV_(ob, VC, 0, X, 16) \
    E_(X, 30) E_(X, 31) C_(X, 26, 28, 29) C_(X, 27, 30, 31)

DI float max_halves(float m) {
    auto rr = __builtin_amdgcn_permlane32_swap(__float_as_uint(m), __float_as_uint(m), false, false);
    return fmaxf(__uint_as_float(rr[0]), __uint_as_float(rr[1]));
}
DI void attn_fallback(const char* lds, int l, const char* qp, int tile, f32x16& o) {
    const int r32 = l & 31, h = l >> 5;
    const s16x4 z4 = {0, 0, 0, 0};
    const bf16x8 q1 = *(const bf16x8*)(qp + tile * 1024 + l * 16);
    const s16x4 q2 = *(const s16x4*)(qp + 2048 + r32 * 16 + tile * 8);
    const bf16x8 q2f = cat8(h == 0 ? q2 : z4, z4);
    const bf16x8 ones = {0x3f80, 0x3f80, 0x3f80, 0x3f80, 0x3f80, 0x3f80, 0x3f80, 0x3f80};
    float m = -INFINITY;
#pragma unroll
    for (int i = 0; i < 16; ++i) o[i] = 0.f;
#pragma unroll 1
    for (int t = 0; t < 64; ++t) {
        const LDSP char* cb = (const LDSP char*)lds + (t >> 1) * 5120;
        const bf16x8 k1 = *(const LDSP bf16x8*)(cb + (t & 1) * 1024 + l * 16);
        const s16x4 k2 = *(const LDSP s16x4*)(cb + 2048 + r32 * 16 + (t & 1) * 8);
        f32x16 s;
#pragma unroll
        for (int i = 0; i < 16; ++i) s[i] = 0.f;
        s = MFMA32(k1, q1, s);
        s = MFMA32(cat8(k2, z4), q2f, s);
        float tm = s[0];
#pragma unroll
        for (int i = 1; i < 16; ++i) tm = fmaxf(tm, s[i]);
        tm = max_halves(tm);
        const float mn = fmaxf(m, tm), alpha = __builtin_amdgcn_exp2f(m - mn);
        m = mn;
        u32x4 p0, p1;
#pragma unroll
        for (int i = 0; i < 4; ++i) {
            p0[i] = pk_bf16(__builtin_amdgcn_exp2f(s[2 * i] - m), __builtin_amdgcn_exp2f(s[2 * i + 1] - m));
            p1[i] = pk_bf16(__builtin_amdgcn_exp2f(s[8 + 2 * i] - m), __builtin_amdgcn_exp2f(s[9 + 2 * i] - m));
        }
#pragma unroll
        for (int i = 0; i < 16; ++i) o[i] *= alpha;
        const int dv = r32 < 20 ? r32 : 0;
        bf16x8 v0 = *(const LDSP bf16x8*)(cb + 2560 + (t & 1) * 1280 + h * 320 + dv * 16);
        bf16x8 v1 = *(const LDSP bf16x8*)(cb + 2560 + (t & 1) * 1280 + 640 + h * 320 + dv * 16);
        if (r32 >= 20) { v0 = ones; v1 = ones; }
        o = MFMA32(v0, __builtin_bit_cast(bf16x8, p0), o);
        o = MFMA32(v1, __builtin_bit_cast(bf16x8, p1), o);
    }
}

DI bool attn_ok(const f32x16& a, const f32x16& b) {
    f32x2 s = {0.f, 0.f};
#pragma unroll
    for (int i = 0; i < 12; i += 2) s += f32x2{a[i], a[i + 1]} + f32x2{b[i], b[i + 1]};
    return (fmaxf(a[12], b[12]) < 0x1p80f) & (fabsf(s[0] + s[1] + a[12] + b[12]) < 0x1p126f);
}
DI void attn_pack(const f32x16& oa, const f32x16& ob, float ga, float gb, u32x4& va, u32x4& vb, u32x2& vt) {
    const float fa = ga * __builtin_amdgcn_rcpf(oa[12]), fb = gb * __builtin_amdgcn_rcpf(ob[12]);
    va = u32x4{pk_bf16(oa[0] * fa, oa[1] * fa), pk_bf16(oa[2] * fa, oa[3] * fa), pk_bf16(oa[4] * fa, oa[5] * fa), pk_bf16(oa[6] * fa, oa[7] * fa)};
    vb = u32x4{pk_bf16(ob[0] * fb, ob[1] * fb), pk_bf16(ob[2] * fb, ob[3] * fb), pk_bf16(ob[4] * fb, ob[5] * fb), pk_bf16(ob[6] * fb, ob[7] * fb)};
    auto s0 = __builtin_amdgcn_permlane32_swap(pk_bf16(oa[8] * fa, oa[9] * fa), pk_bf16(ob[8] * fb, ob[9] * fb), false, false);
    auto s1 = __builtin_amdgcn_permlane32_swap(pk_bf16(oa[10] * fa, oa[11] * fa), pk_bf16(ob[10] * fb, ob[11] * fb), false, false);
    vt = u32x2{s0[0], s1[0]};
}

typedef __attribute__((ext_vector_type(16))) unsigned u32x16;
#define WSUB(v, i) __builtin_bit_cast(bf16x8, __builtin_shufflevector(v, v, 4 * (i), 4 * (i) + 1, 4 * (i) + 2, 4 * (i) + 3))

__global__ __launch_bounds__(512, 2) void k_attn(char* __restrict__ ws, char* __restrict__ out) {
    __shared__ __attribute__((aligned(16))) char lds[163840];
    STAMP_DECL; STAMP(0);
    const int b = blockIdx.x, xcd = b & 7, ii = b >> 3;
    const int pos = ii >> 2, head = (pos >> 2) * 32 + xcd * 4 + (pos & 3), slot = xcd * 8 + pos, qb = ii & 3;
    const int w = __builtin_amdgcn_readfirstlane(threadIdx.x >> 6);
    int l = threadIdx.x & 63, r32 = l & 31, h = l >> 5;
    const int qc = qb * 8 + w;
    const unsigned lds0 = (unsigned)(uintptr_t)lds;
    const unsigned qo = (unsigned)WS_Q + head * (unsigned)Q_HEAD + qc * 2560, kvo = (unsigned)WS_KV + (head < 32 ? 32 : head - 32) * (unsigned)KV_HEAD;
    bf16x8 q1a, q1b, k1f, k2f; s16x4 q2; float ga, gb;
    unsigned dv = kvo + w * 1024 + l * 16;
    {
        unsigned sv, dm = __builtin_amdgcn_readfirstlane(lds0 + w * 1024);
        asm volatile(
            "global_load_dwordx4 v[4:7], %[a0], %[kvp]\n\tglobal_load_dwordx4 v[8:11], %[a0], %[kvp] offset:1024\n\tglobal_load_dwordx2 v[12:13], %[a1], %[kvp] offset:2048\n\t"
            "global_load_dwordx4 " KA_ ", %[a2], %[kvp]\n\tglobal_load_dwordx4 v[" STR(KBR) ":" STR(KBR) "+3], %[a3], %[kvp] offset:2048\n\t"
            "global_load_dword v14, %[a4], %[kvp]\n\tglobal_load_dword v15, %[a4], %[kvp] offset:128\n\t"
            "s_mov_b32 %[sv], m0\n\t" DMA1 DMA1 DMA1 DMA1 "s_mov_b32 m0, %[sv]"
            : "=&{v[4:7]}"(q1a), "=&{v[8:11]}"(q1b), "=&{v[12:13]}"(q2), "=&{v14}"(ga), "=&{v15}"(gb), "=&{v[168:171]}"(k1f), "=&{v[172:175]}"(k2f),
              [sv] "=&s"(sv), [dv] "+v"(dv), [dm] "+s"(dm)
            : [kvp] "s"(ws), [a0] "v"(qo + l * 16), [a1] "v"(qo + r32 * 16 + h * 8), [a2] "v"(kvo + l * 16), [a3] "v"(kvo + r32 * 16), [a4] "v"((unsigned)WS_G + qc * 256 + r32 * 4)
            : "memory", "scc");
    }
    const int mb = xcd * 4 + (ii & 3), nb = ii >> 2;
    const char* wp = ws + WS_WO + (size_t)(nb * 8 + w) * 25600 + 2048;
    const char* cp = ws + WS_CNT + (((mb >> 3) * 8 + w) * 8 + nb) * CNT_STRIDE;
    const char* sgp = ws + WS_CNT + (qb * 8 + xcd) * 8 * CNT_STRIDE;
    const char* a1 = ws + WS_O1 + (size_t)w * 8 * 65536 + mb * 2048;
    const char* a2 = ws + WS_O2 + (size_t)w * 4 * 32768 + mb * 1024;
    const char* op = out + (size_t)mb * 64 * 1280 + nb * 160;
    const char* po1 = ws + WS_O1 + (size_t)slot * 65536 + qc * 2048;
    const char* po2 = ws + WS_O2 + (size_t)(slot >> 1) * 32768 + qc * 1024 + (slot & 1) * 8;
    f32x16 oa, ob; u32x16 vini;
#pragma unroll
    for (int i = 0; i < 16; ++i) { oa[i] = 0.f; ob[i] = 0.f; vini[i] = (r32 & 27) == 24 ? 0x3f803f80u : 0u; }
    unsigned ak1 = lds0 + l * 16, ak2 = lds0 + 2048 + r32 * 16, av = lds0 + 2560 + h * 320 + r32 * 16, wv = l * 16;
    unsigned cnt, inc, svm, dm = __builtin_amdgcn_readfirstlane(lds0 + w * 1024 + 32768);
    u32x16 wt0, wt1, wt2, wt3, wt4;
    STAMP(2);
#define LSTAMP ""
    asm volatile(
        "s_mov_b32 %[sv], m0\n\t"
        ".irp n,24,25,26,27\n\tv_mov_b32 v[" STR(S1R) "+\\n], 0\n\t.endr\n\t"
        "s_mov_b32 %[cnt], 0\n\ts_mov_b32 %[inc], 20480\n\ts_mov_b64 vcc, -1\n\t"
        "s_waitcnt vmcnt(6)\n\t"
        "v_mfma_f32_32x32x4_2b_bf16 v[" STR(S0R) ":" STR(S0R) "+31], " KB0_ ", %[q2], 0\n\ts_nop 15\n\ts_nop 1\n\t"
        M2_(S0R, 0, q1a) M2_(S0R, 16, q1b)
        "s_nop 10\n\t"
        MX3(0, S0R + 0, 1) MX3(16, S0R + 16, 17) MX3(0, NMR, 3) MX3(16, NMR + 16, 19) MX3(0, NMR, 5) MX3(16, NMR + 16, 21) MX3(0, NMR, 7) MX3(16, NMR + 16, 23)
        MX3(0, NMR, 9) MX3(16, NMR + 16, 25) MX3(0, NMR, 11) MX3(16, NMR + 16, 27) MX3(0, NMR, 13) MX3(16, NMR + 16, 29) MX3(0, NMR, 14) MX3(16, NMR + 16, 30)
        "v_mov_b32 v[" STR(NMR) "+1], v[" STR(NMR) "]\n\tv_mov_b32 v[" STR(NMR) "+17], v[" STR(NMR) "+16]\n\ts_nop 1\n\t"
        "v_permlane32_swap_b32 v[" STR(NMR) "], v[" STR(NMR) "+1]\n\tv_permlane32_swap_b32 v[" STR(NMR) "+16], v[" STR(NMR) "+17]\n\ts_nop 1\n\t"
        "v_min_f32 v[" STR(NMR) "], -v[" STR(NMR) "], -v[" STR(NMR) "+1]\n\tv_min_f32 v[" STR(NMR) "+16], -v[" STR(NMR) "+16], -v[" STR(NMR) "+17]\n\t"
        ".irp n,1,2,3,4,5,6,7,8,9,10,11,12,13,14,15\n\tv_mov_b32 v[" STR(NMR) "+\\n], v[" STR(NMR) "]\n\tv_mov_b32 v[" STR(NMR) "+16+\\n], v[" STR(NMR) "+16]\n\t.endr\n\t"
        ".irp n,0,2,4,6,8,10,12,14,16,18,20,22,24,26,28,30\n\tv_pk_add_f32 v[" STR(S0R) "+\\n:" STR(S0R) "+\\n+1], v[" STR(S0R) "+\\n:" STR(S0R) "+\\n+1], v[" STR(NMR) "+\\n:" STR(NMR) "+\\n+1]\n\t.endr\n\t"
        "s_waitcnt vmcnt(0)\n\ts_barrier\n\t"
        RDKA(1024) RDV(VAR, 0)
        "1:\n\t"
        LSTAMP
        TILE(S0R, S1R, VAR, VBR, 5120, KB1_, RDKB(5120), 1280, PRIO(3) "s_cbranch_vccnz 11f\n\t21:\n\t")
        TILE(S1R, S0R, VBR, VAR, 5120 + 1024, KB0_, "", 5120, "s_cbranch_vccnz 12f\n\t22:\n\t")
        TILE(S0R, S1R, VAR, VBR, 10240, KB1_, RDKB(10240), 5120 + 1280, "s_cbranch_vccnz 13f\n\t23:\n\t")
        TILE(S1R, S0R, VBR, VAR, 10240 + 1024, KB0_, "", 10240, "s_cbranch_vccnz 14f\n\t24:\n\t")
        TILE(S0R, S1R, VAR, VBR, 15360, KB1_, RDKB(15360), 10240 + 1280, PRIO(2))
        TILE(S1R, S0R, VBR, VAR, 15360 + 1024, KB0_, "", 15360, "s_cmp_eq_u32 %[cnt], 7\n\ts_cselect_b32 %[inc], -143360, %[inc]\n\t")
        TILE(S0R, S1R, VAR, VBR, 0, KB1_, RDKB(0), 15360 + 1280, PRIO(1) "v_add_u32 %[ak1], %[inc], %[ak1]\n\tv_add_u32 %[ak2], %[inc], %[ak2]\n\t")
        TILE(S1R, S0R, VBR, VAR, 1024, KB0_, "", 0, PRIO(0) "v_add_u32 %[av], %[inc], %[av]\n\t")
        "s_add_u32 %[cnt], %[cnt], 1\n\t"
        "s_cmp_eq_u32 %[cnt], 1\n\ts_cbranch_scc1 2f\n\t"
        "s_cmp_eq_u32 %[cnt], 3\n\ts_cbranch_scc1 3f\n\t"
        "s_cmp_eq_u32 %[cnt], 4\n\ts_cbranch_scc1 5f\n\t"
        "s_cmp_eq_u32 %[cnt], 5\n\ts_cbranch_scc1 6f\n\t"
        "s_cmp_eq_u32 %[cnt], 6\n\ts_cbranch_scc1 8f\n\t"
        "s_cmp_lt_u32 %[cnt], 8\n\ts_cbranch_scc1 1b\n\t"
        "s_branch 4f\n\t"
        "11:\n\t" DMA1 DMA1 DMA1 DMA1 "s_branch 21b\n\t"
        "12:\n\t" DMA1 DMA1 DMA1 DMA1 "s_branch 22b\n\t"
        "13:\n\t" DMA1 DMA1 DMA1 DMA1 "s_branch 23b\n\t"
        "14:\n\t" DMA1 DMA1 DMA1 DMA1 "s_branch 24b\n\t"
        "2:\n\ts_mov_b64 vcc, 0\n\ts_waitcnt vmcnt(0)\n\ts_barrier\n\ts_branch 1b\n\t"
        "3:\n\t" WLD(0)
        "5:\n\t" WLD(1)
        "6:\n\t" WLD(2)
        "8:\n\tv_and_b32 %[dv], 0x3ff, %[wv]\n\tv_lshlrev_b32 %[dv], 3, %[dv]\n\tv_min_u32 %[dv], 0x1380, %[dv]\n\tv_add_u32 %[dv], 0x5000, %[dv]\n\t"
        "global_load_dword %[dv], %[dv], %[wp] offset:-2048\n\t" WLD(3)
        "4:\n\ts_nop 1\n\t"
        LSTAMP
        PV_(ob, VBR, 4, S1R, 24)
        "s_mov_b32 m0, %[sv]\n\ts_waitcnt vmcnt(0) lgkmcnt(0)\n\ts_nop 15\n\ts_nop 7"
        : [oa] "+{v[24:39]}"(oa), [ob] "+{v[40:55]}"(ob), [ak1] "+v"(ak1), [ak2] "+v"(ak2), [av] "+v"(av), [dv] "+v"(dv), [wv] "+v"(wv),
          "+{v[152:167]}"(vini), "+{v[168:171]}"(k1f), "+{v[172:175]}"(k2f),
          [q1a] "+{v[4:7]}"(q1a), [q1b] "+{v[8:11]}"(q1b), [q2] "+{v[12:13]}"(q2), "+{v14}"(ga), "+{v15}"(gb),
          "=&{v[176:191]}"(wt0), "=&{v[192:207]}"(wt1), "=&{v[208:223]}"(wt2), "=&{v[224:239]}"(wt3), "=&{v[240:255]}"(wt4),
          [cnt] "=&s"(cnt), [inc] "=&s"(inc), [sv] "=&s"(svm), [dm] "+s"(dm)
        : [vmask] "s"(0x000fffff000fffffull), [kvp] "s"(ws), [wp] "s"(wp),
          "s"(cp), "s"(sgp), "s"(a1), "s"(a2), "s"(op), "s"(po1), "s"(po2)
        : "memory", "scc", "vcc", "v56", "v57", "v58", "v59", CL10(6), CL10(7), CL10(8), CL10(9), CL10(10), CL10(11), CL10(12), CL10(13), CL10(14), "v150", "v151"
          );
    asm volatile("v_mbcnt_lo_u32_b32 %0, -1, 0\n\tv_mbcnt_hi_u32_b32 %0, -1, %0" : "=v"(l));
    r32 = l & 31; h = l >> 5;
    STAMP(3);
    bf16x8 wl[5], af[5][4]; unsigned seen;
    {
        if (head >= 32) { ga = 1.0f - ga; gb = 1.0f - gb; }
        u32x4 va, vb; u32x2 vt;
        attn_pack(oa, ob, ga, gb, va, vb, vt);
        if (__builtin_expect(!__all(attn_ok(oa, ob)), 0)) {
#pragma unroll 1
            for (int t = 0; t < 2; ++t) {
                f32x16 ro; u32x4 v0, v1; u32x2 v2;
                attn_fallback(lds, l, ws + qo, t, ro);
                attn_pack(ro, ro, t ? gb : ga, t ? gb : ga, v0, v1, v2);
                if (t == 0) va = v0; else vb = v0;
                if (h == t) vt = v2;
            }
        }
        asm volatile(
            "global_store_dwordx4 %[lo], %[va], %[o1] sc1\n\tglobal_store_dwordx4 %[lo], %[vb], %[o1] offset:1024 sc1\n\tglobal_store_dwordx2 %[lo], %[vt], %[o2] sc1\n\t"
            "s_nop 0"
            :: [lo] "v"(l * 16), [va] "v"(va), [vb] "v"(vb), [vt] "v"(vt), [o1] "s"(po1), [o2] "s"(po2)
            : "memory");
    }
    const int r = l & 15, g = l >> 4, r8 = l & 7, g2 = (l >> 3) & 3;
    unsigned o1 = (g >> 1) * 65536 + (g & 1) * 512 + r * 16, o2 = g * 32768 + r * 16;
    unsigned wbo = w * 20480 + (g * 16 + (r ^ ((g >> 1) << 3))) * 16;
    unsigned rbo = ((w >> 1) * 5 + h) * 1024 + (g2 * 16 + ((8 * (w & 1) + r8) ^ ((g2 >> 1) << 3))) * 16;
    unsigned opo = (8 * w + r8) * 1280 + h * 32 + g2 * 8, sgo = l * CNT_STRIDE;
    asm volatile("s_waitcnt vmcnt(0)\n\ts_barrier\n\t"
                 "s_cmp_lg_u32 %[w], %[sw]\n\ts_cbranch_scc1 1f\n\ts_mov_b64 exec, 0xff\n\tglobal_atomic_add %[so], %[one], %[sp]\n\ts_mov_b64 exec, -1\n\t1:"
                 : "+v"(o1), "+v"(o2), "+v"(wbo), "+v"(rbo), "+v"(opo)
                 : [w] "s"(w), [sw] "s"(xcd ^ 1), [so] "v"(sgo), [one] "v"(1), [sp] "s"(sgp) : "memory", "scc");
    STAMP(4);
    asm volatile(
        "global_load_dwordx4 %0, %6, %7 offset:-2048\n\tglobal_load_dwordx4 %1, %6, %7 offset:-1024\n\tglobal_load_dwordx4 %2, %6, %7\n\t"
        "global_load_dwordx4 %3, %6, %7 offset:1024\n\tglobal_load_dwordx4 %4, %6, %7 offset:2048\n\tglobal_load_dword %5, %8, %9 sc1\n\ts_waitcnt vmcnt(0)"
        : "=&v"(wl[0]), "=&v"(wl[1]), "=&v"(wl[2]), "=&v"(wl[3]), "=&v"(wl[4]), "=&v"(seen) : "v"(wv), "s"(wp), "v"(0), "s"(cp) : "memory");
    const unsigned late = __builtin_amdgcn_readfirstlane(seen);
#pragma unroll 1
    for (int spin = 0; spin < 400000 && __builtin_amdgcn_readfirstlane(seen) < 8; ++spin) {
        __builtin_amdgcn_s_sleep(POLL_SLEEP);
        asm volatile("global_load_dword %0, %1, %2 sc1\n\ts_waitcnt vmcnt(0)" : "=v"(seen) : "v"(0), "s"(cp) : "memory");
    }
    STAMP(5);
#define ALD(s, base, off, d1, d2) asm volatile( \
        "global_load_dwordx4 %0, %4, %5 sc1\n\tglobal_load_dwordx4 %1, %4, %5 offset:" #d1 " sc1\n\t" \
        "global_load_dwordx4 %2, %4, %5 offset:" #d2 " sc1\n\tglobal_load_dwordx4 %3, %4, %5 offset:" #d1 "+" #d2 " sc1" \
        : "=&v"(af[s][0]), "=&v"(af[s][1]), "=&v"(af[s][2]), "=&v"(af[s][3]) : "v"(off), "s"(base) : "memory")
#define AWAIT(s, N) asm volatile("s_waitcnt vmcnt(" STR(N) ")" : "+v"(af[s][0]), "+v"(af[s][1]), "+v"(af[s][2]), "+v"(af[s][3]) :: "memory")
#define OMM(s, w0, w1, w2, w3, w4) do { const bf16x8 wf_[5] = {w0, w1, w2, w3, w4}; \
        _Pragma("unroll") for (int j = 0; j < 5; ++j) _Pragma("unroll") for (int i = 0; i < 4; ++i) acc[i][j] = MFMA16(wf_[j], af[s][i], acc[i][j]); \
        __builtin_amdgcn_sched_barrier(0); } while (0)
    f32x4 acc[4][5];
#pragma unroll
    for (int i = 0; i < 4; ++i)
#pragma unroll
        for (int j = 0; j < 5; ++j) acc[i][j] = f32x4{0.f, 0.f, 0.f, 0.f};
    ALD(0, a1, o1, 256, 1024); ALD(1, a1 + 131072, o1, 256, 1024); ALD(2, a1 + 2 * 131072, o1, 256, 1024);
    AWAIT(0, 8); ALD(3, a1 + 3 * 131072, o1, 256, 1024); OMM(0, WSUB(wt0, 0), WSUB(wt0, 1), WSUB(wt0, 2), WSUB(wt0, 3), WSUB(wt1, 0));
    AWAIT(1, 8); ALD(4, a2, o2, 256, 512); OMM(1, WSUB(wt1, 1), WSUB(wt1, 2), WSUB(wt1, 3), WSUB(wt2, 0), WSUB(wt2, 1));
    AWAIT(2, 8); OMM(2, WSUB(wt2, 2), WSUB(wt2, 3), WSUB(wt3, 0), WSUB(wt3, 1), WSUB(wt3, 2));
    AWAIT(3, 4); OMM(3, WSUB(wt3, 3), WSUB(wt4, 0), WSUB(wt4, 1), WSUB(wt4, 2), WSUB(wt4, 3));
    AWAIT(4, 0);
#pragma unroll
    for (int j = 0; j < 6; ++j) {
        if (j < 5) {
#pragma unroll
            for (int i = 0; i < 4; ++i) acc[i][j] = MFMA16(wl[j], af[4][i], acc[i][j]);
        }
        if (j > 0) {
#pragma unroll
            for (int i = 0; i < 4; ++i) *(f32x4*)(lds + wbo + (i * 5 + j - 1) * 1024) = acc[i][j - 1];
        }
        __builtin_amdgcn_sched_barrier(0);
    }
    STAMP(6);
    {
        __syncthreads();
        f32x4 t[3][8];
#pragma unroll
        for (int u = 0; u < 8; ++u)
#pragma unroll
            for (int p = 0; p < 3; ++p) t[p][u] = *(const f32x4*)(lds + rbo + u * 20480 + (p < 2 ? p * 2048 : 4096 - h * 1024));
        __builtin_amdgcn_sched_barrier(0);
#pragma unroll
        for (int p = 0; p < 3; ++p) {
            const f32x4 z = ((t[p][0] + t[p][1]) + (t[p][2] + t[p][3])) + ((t[p][4] + t[p][5]) + (t[p][6] + t[p][7]));
            const u32x2 v = {pk_bf16(z[0], z[1]), pk_bf16(z[2], z[3])};
            if (p < 2 || h == 0) asm volatile("global_store_dwordx2 %0, %1, %2 offset:%3 " OUT_FLAGS :: "v"(opo), "v"(v), "s"(op), "i"(p * 64) : "memory");
        }
    }
    STAMP(7); STAMP_FLUSH(ws, 1);
}

extern "C" void kernel_launch(void* const* d_in, const int* in_sizes, int n_in, void* d_out, int out_size, void* d_ws, size_t ws_size,
                              hipStream_t stream) {
    const char* x = (const char*)d_in[0];
    const char* wq = (const char*)d_in[1];
    const char* wk = (const char*)d_in[2];
    const char* wv = (const char*)d_in[3];
    const char* woa = (const char*)d_in[4];
    const char* wqkv = (const char*)d_in[6];
    const char* wob = (const char*)d_in[7];
    const char* gw = (const char*)d_in[8];
    char* ws = (char*)d_ws;
    k_proj<<<256, 768, 0, stream>>>(x, wq, wqkv, wk, wv, gw, woa, wob, ws, (const char*)d_out);
    k_attn<<<256, 512, 0, stream>>>(ws, (char*)d_out);
}
```

```cpp
#define PROBE 0
#define PROBE_B 77
#include <hip/hip_runtime.h>
#include <hip/hip_bf16.h>
#include <stdint.h>

typedef __attribute__((ext_vector_type(8))) short bf16x8;
typedef __attribute__((ext_vector_type(4))) short s16x4;
typedef __attribute__((ext_vector_type(4))) float f32x4;
typedef __attribute__((ext_vector_type(16))) float f32x16;
typedef __attribute__((ext_vector_type(32))) float f32x32;
typedef __attribute__((ext_vector_type(2))) unsigned u32x2;
typedef __attribute__((ext_vector_type(4))) unsigned u32x4;
typedef __attribute__((ext_vector_type(2))) __bf16 bf16x2_t;
typedef __attribute__((ext_vector_type(2))) float f32x2;
#define LDSP __attribute__((address_space(3)))
#define DI __device__ __forceinline__

#define STAMP_PARAMS
#define STAMP_ARGS
#define STAMP_DECL
#define STAMP(slot)
#define STAMP_FLUSH(wsp, kid)
constexpr int SEQ = 2048, DM = 640;
constexpr size_t Q_HEAD = 32 * 2560;
constexpr size_t KV_HEAD = 32 * 5120;
constexpr size_t WS_Q = 0;
constexpr size_t WS_KV = WS_Q + 64 * Q_HEAD;
constexpr size_t WS_G = WS_KV + 33 * KV_HEAD;
constexpr size_t WS_O1 = WS_G + 2048 * 4;
constexpr size_t WS_O2 = WS_O1 + 64 * 65536;
constexpr size_t WS_WO = WS_O2 + 32 * 2048 * 16;
#define OUT_FLAGS ""
#define XT_WIN 3
#define CNT_STRIDE 128
#define POLL_SLEEP 4
constexpr size_t WS_CNT = WS_WO + 1600 * 1024;

DI unsigned pk_bf16(float a, float b) {
    f32x2 v = {a, b};
    return __builtin_bit_cast(unsigned, __builtin_convertvector(v, bf16x2_t));
}
#define ST_FLAGS "sc1"
DI void st16(char* p, u32x4 v) { asm volatile("global_store_dwordx4 %0, %1, off " ST_FLAGS "\n\ts_nop 1" :: "v"(p), "v"(v) : "memory"); }
DI void st8(char* p, u32x2 v) { asm volatile("global_store_dwordx2 %0, %1, off " ST_FLAGS :: "v"(p), "v"(v) : "memory"); }
DI s16x4 tr_read(const LDSP char* p) {
    typedef short v4i16_t __attribute__((ext_vector_type(4)));
    return __builtin_amdgcn_ds_read_tr16_b64_v4i16((LDSP v4i16_t*)p);
}
DI bf16x8 cat8(s16x4 lo, s16x4 hi) { return __builtin_shufflevector(lo, hi, 0, 1, 2, 3, 4, 5, 6, 7); }
#define MFMA16(a, b, c) __builtin_amdgcn_mfma_f32_16x16x32_bf16((a), (b), (c), 0, 0, 0)
#define MFMA32(a, b, c) __builtin_amdgcn_mfma_f32_32x32x16_bf16((a), (b), (c), 0, 0, 0)

constexpr int P_SLOT = 40960;

DI void proj_loader(char* lds, const char* xs, const char* ws, const char* es, int sw, int se, unsigned long long m5, int st, int ekind, int ehalf, int mb,
                    const char* woa, const char* wob, char* wsp, const unsigned (&xo)[2], const unsigned (&wo)[3]) {
    const int l = threadIdx.x & 63, w = __builtin_amdgcn_readfirstlane(threadIdx.x >> 6), r = l & 15, g = l >> 4;
    const unsigned dw = __builtin_amdgcn_readfirstlane((unsigned)(uintptr_t)lds + w * 1024);
    auto issue = [&](int slot) {
        const unsigned d0 = dw + slot * P_SLOT;
        unsigned sv;
        asm volatile(
            "s_mov_b32 %[sv], m0\n\ts_mov_b32 m0, %[d0]\n\ts_nop 0\n\t"
            "global_load_lds_dwordx4 %[x0], %[xs]\n\ts_add_u32 m0, m0, 8192\n\ts_nop 0\n\t"
            "global_load_lds_dwordx4 %[x1], %[xs]\n\ts_add_u32 m0, m0, 8192\n\ts_nop 0\n\t"
            "global_load_lds_dwordx4 %[w0], %[ws]\n\ts_add_u32 m0, m0, 8192\n\ts_nop 0\n\t"
            "global_load_lds_dwordx4 %[w1], %[ws]\n\ts_add_u32 m0, m0, 8192\n\ts_mov_b64 exec, %[m5]\n\t"
            "global_load_lds_dwordx4 %[w2], %[es]\n\ts_mov_b64 exec, -1\n\ts_mov_b32 m0, %[sv]"
            : [sv] "=&s"(sv)
            : [d0] "s"(d0), [m5] "s"(m5), [xs] "s"(xs), [ws] "s"(ws), [es] "s"(es),
              [x0] "v"(xo[0]), [x1] "v"(xo[1]), [w0] "v"(wo[0]), [w1] "v"(wo[1]), [w2] "v"(wo[2])
            : "memory", "scc");
        const int d = st == 9 ? -9 : 1;
        st += d; xs += d * 128; ws += d * sw; es += d * se;
    };
    issue(0); issue(1);
    __builtin_amdgcn_s_setprio(3);
    f32x4 eacc = {0.f, 0.f, 0.f, 0.f};
    const int ets = 4 * ehalf + w;
    if (w >= 4) ekind = 5;
    const int exr = (16 * ets + r) * 128 + ((g ^ ((r >> 1) & 7)) << 4);
    const int ewr = ekind < 4 ? 36864 + (8 * g + (r >> 2)) * 40 + ((ekind & 1) ? 32 : 8 * (r & 3)) : 36864 + g * 16;
    auto extra = [&](int k) {
        if (ekind < 5) {
            const LDSP char* sb = (const LDSP char*)lds + (k & 3) * P_SLOT;
            const bf16x8 x0 = *(const LDSP bf16x8*)(sb + exr), x1 = *(const LDSP bf16x8*)(sb + (exr ^ 64));
            bf16x8 w0, w1;
            if (ekind < 4) { w0 = cat8(tr_read(sb + ewr), tr_read(sb + ewr + 160)); w1 = cat8(tr_read(sb + ewr + 1280), tr_read(sb + ewr + 1440)); }
            else { w0 = *(const LDSP bf16x8*)(sb + ewr); w1 = *(const LDSP bf16x8*)(sb + ewr + 64); }
            if ((ekind >> 1) == 1) { eacc = MFMA16(x0, w0, eacc); eacc = MFMA16(x1, w1, eacc); }
            else { eacc = MFMA16(w0, x0, eacc); eacc = MFMA16(w1, x1, eacc); }
            asm volatile("" : "+v"(eacc));
        }
    };
#pragma unroll 1
    for (int k = 0; k < 8; ++k) {
        asm volatile("s_waitcnt vmcnt(5)\n\ts_barrier" ::: "memory");
        issue((k + 2) & 3);
        if (k < 7) extra(k);
    }
    const int gw = blockIdx.x * 8 + w, fid = gw < 1600 ? gw : 1599;
    const int fj = fid % 5, fs = (fid / 5) % 5, fg = (fid / 25) & 7, fn = fid / 200;
    const int br = fs < 4 ? fs >> 1 : g >> 1;
    const int row = fs < 4 ? (4 * fg + 2 * (fs & 1) + (g >> 1)) * 20 + 4 * (g & 1) : (4 * fg + 2 * (g & 1)) * 20 + 16;
    const char* p0 = (br ? wob : woa) + row * 1280 + (80 * fn + 16 * fj + r) * 2;
    const char* p1 = p0 + (fs < 4 ? 8 : 20) * 1280;
    unsigned e0, e1, e2, e3, e4, e5, e6, e7;
    asm volatile(
        "global_load_ushort %0, %8, off\n\tglobal_load_ushort %1, %8, off offset:1280\n\tglobal_load_ushort %2, %8, off offset:2560\n\tglobal_load_ushort %3, %8, off offset:3840\n\t"
        "global_load_ushort %4, %9, off\n\tglobal_load_ushort %5, %9, off offset:1280\n\tglobal_load_ushort %6, %9, off offset:2560\n\tglobal_load_ushort %7, %9, off offset:3840"
        : "=&v"(e0), "=&v"(e1), "=&v"(e2), "=&v"(e3), "=&v"(e4), "=&v"(e5), "=&v"(e6), "=&v"(e7) : "v"(p0), "v"(p1) : "memory");
    extra(7);
    asm volatile("s_waitcnt vmcnt(13)\n\ts_barrier" ::: "memory");
    extra(8);
    asm volatile("s_waitcnt vmcnt(8)\n\ts_barrier" ::: "memory");
    extra(9);
    asm volatile("s_waitcnt vmcnt(0)" : "+v"(e0), "+v"(e1), "+v"(e2), "+v"(e3), "+v"(e4), "+v"(e5), "+v"(e6), "+v"(e7) :: "memory");
    {
        char* kva = wsp + WS_KV + 32 * KV_HEAD + (mb * 2 + ehalf) * 5120;
        const u32x2 v = {pk_bf16(eacc[0], eacc[1]), pk_bf16(eacc[2], eacc[3])};
        const int t = (w >> 1) & 1, erow = 16 * (r >> 3) + 8 * (w & 1) + (r & 7);
        if (ekind == 0) st8(kva + t * 1024 + (g >> 1) * 512 + erow * 16 + (g & 1) * 8, v);
        else if (ekind == 1) { if (g == 0) st8(kva + 2048 + erow * 16 + t * 8, v); }
        else if (ekind < 4) { if (ekind == 2 || r < 4) st8(kva + 2560 + t * 1280 + g * 320 + ((ekind & 1) * 16 + r) * 16 + (w & 1) * 8, v); }
        else if (ekind == 4) { if (g == 0) ((float*)(wsp + WS_G))[mb * 128 + 16 * ets + r] = 1.0f / (1.0f + __expf(-eacc[0])); }
    }
    if (gw < 1600) st16(wsp + WS_WO + (size_t)fid * 1024 + l * 16, u32x4{e0 | (e1 << 16), e2 | (e3 << 16), e4 | (e5 << 16), e6 | (e7 << 16)});
    if (w == 0 && l == 0) *(unsigned*)(wsp + WS_CNT + blockIdx.x * CNT_STRIDE) = 0;
}

#define STR_(x) #x
#define STR(x) STR_(x)
#define PR4(b) "v[" STR(b) ":" STR(b) "+3]"
#define PM_F(j, i, X) "v_mfma_f32_16x16x32_bf16 " PR4(20 + 16 * j + 4 * i) ", " PR4(136 + 4 * j) ", " PR4(X + 4 * i) ", " PR4(20 + 16 * j + 4 * i) "\n\t"
#define PM_T(j, i, X) "v_mfma_f32_16x16x32_bf16 " PR4(20 + 16 * j + 4 * i) ", " PR4(X + 4 * i) ", " PR4(136 + 4 * j) ", " PR4(20 + 16 * j + 4 * i) "\n\t"
#define PTL(j, sl, ks) "ds_read_b64_tr_b16 v[136+4*" #j ":136+4*" #j "+1], %[aw" #j "] offset:" #sl "*40960+" #ks "*10240\n\t"
#define PTH(j, sl, ks) "ds_read_b64_tr_b16 v[136+4*" #j "+2:136+4*" #j "+3], %[aw" #j "] offset:" #sl "*40960+" #ks "*10240+1280\n\t"
#define PBX(X, i, sl, ks) "ds_read_b128 " PR4(X + 4 * i) ", %[ax" #ks "] offset:" #sl "*40960+" #i "*2048\n\t"
#define PTG(a) "v_add_u32 %[" #a "], %[tg], %[" #a "]\n\t"
#define PSTEP(M, X, Y, cs, ck, ns, nk, BAR, F0, F1, F2, F3) \
    M(0, 0, X) PTL(4, cs, ck) M(0, 1, X) PTH(4, cs, ck) M(0, 2, X) BAR PBX(Y, 0, ns, nk) M(0, 3, X) PBX(Y, 1, ns, nk) \
    M(1, 0, X) PTL(0, ns, nk) M(1, 1, X) PTH(0, ns, nk) M(1, 2, X) PBX(Y, 2, ns, nk) M(1, 3, X) "s_waitcnt lgkmcnt(5)\n\t" \
    M(2, 0, X) PTL(1, ns, nk) M(2, 1, X) PTH(1, ns, nk) M(2, 2, X) PBX(Y, 3, ns, nk) M(2, 3, X) F0 \
    M(3, 0, X) PTL(2, ns, nk) M(3, 1, X) PTH(2, ns, nk) M(3, 2, X) F1 M(3, 3, X) F2 \
    M(4, 0, X) PTL(3, ns, nk) M(4, 1, X) PTH(3, ns, nk) M(4, 2, X) F3 M(4, 3, X) "s_waitcnt lgkmcnt(4)\n\t"
#define CL10(a) "v" #a "0", "v" #a "1", "v" #a "2", "v" #a "3", "v" #a "4", "v" #a "5", "v" #a "6", "v" #a "7", "v" #a "8", "v" #a "9"
#define PLOOP(M) \
    asm volatile( \
        "s_mov_b32 %[cnt], 0\n\ts_mov_b32 %[tg], 81920\n\t" \
        "s_barrier\n\t" \
        PBX(104, 0, 0, 0) PBX(104, 1, 0, 0) PTL(0, 0, 0) PTH(0, 0, 0) PBX(104, 2, 0, 0) PTL(1, 0, 0) PTH(1, 0, 0) PBX(104, 3, 0, 0) \
        PTL(2, 0, 0) PTH(2, 0, 0) PTL(3, 0, 0) PTH(3, 0, 0) "s_waitcnt lgkmcnt(4)\n\t" \
        "1:\n\t" \
        PSTEP(M, 104, 120, 0, 0, 0, 1, "", "", "", "", "") \
        PSTEP(M, 120, 104, 0, 1, 1, 0, "s_barrier\n\t", PTG(ax0), "s_add_u32 %[cnt], %[cnt], 1\n\t", "", "") \
        PSTEP(M, 104, 120, 1, 0, 1, 1, "", PTG(aw0), PTG(aw1), PTG(aw2), PTG(ax1)) \
        PSTEP(M, 120, 104, 1, 1, 0, 0, "s_cmp_eq_u32 %[cnt], 5\n\ts_cbranch_scc1 2f\n\ts_barrier\n\t2:\n\t", PTG(aw3), PTG(aw4), "s_sub_u32 %[tg], 0, %[tg]\n\t", \
              "s_cmp_lg_u32 %[cnt], 5\n\t") \
        "s_cbranch_scc1 1b\n\t" \
        "s_waitcnt lgkmcnt(0)\n\ts_nop 7" \
        : "+{v[20:35]}"(acc[0]), "+{v[36:51]}"(acc[1]), "+{v[52:67]}"(acc[2]), "+{v[68:83]}"(acc[3]), "+{v[84:99]}"(acc[4]), \
          [ax0] "+v"(ax0), [ax1] "+v"(ax1), [aw0] "+v"(aw[0]), [aw1] "+v"(aw[1]), [aw2] "+v"(aw[2]), [aw3] "+v"(aw[3]), [aw4] "+v"(aw[4]), \
          [cnt] "=&s"(cnt), [tg] "=&s"(tg) \
        : \
        : "memory", "scc", "v104", "v105", "v106", "v107", "v108", "v109", CL10(11), CL10(12), CL10(13), CL10(14), "v150", "v151", "v152", "v153", "v154", "v155")

template <bool VT>
DI void proj_mma(unsigned lds0, f32x16 (&acc)[5]) {
    const int tid = threadIdx.x, l = tid & 63, w = __builtin_amdgcn_readfirstlane(tid >> 6) - 8;
    const int wm = w >> 1, wn = w & 1, r = l & 15, g = l >> 4;
    const int xsw = (g ^ ((r >> 1) & 7)) << 4;
    unsigned ax0 = lds0 + (64 * wm + r) * 128 + xsw, ax1 = lds0 + (64 * wm + r) * 128 + (xsw ^ 64);
    unsigned aw[5];
    const int q = r >> 2, p = r & 3;
#pragma unroll
    for (int j = 0; j < 5; ++j) {
        int col;
        if (j == 4) col = (4 * wn + p) * 20 + 16;
        else if (VT) col = (4 * wn + j) * 20 + 4 * p;
        else col = (4 * wn + (j & 2) + (p >> 1)) * 20 + 8 * (p & 1) + 4 * (j & 1);
        aw[j] = lds0 + 16384 + (8 * g + q) * 320 + ((col * 2) ^ ((g & 1) << 5));
    }
    unsigned cnt, tg;
    if constexpr (VT) PLOOP(PM_T); else PLOOP(PM_F);
}
#define ACC(i, j) f32x4{acc[j][4 * (i)], acc[j][4 * (i) + 1], acc[j][4 * (i) + 2], acc[j][4 * (i) + 3]}

DI u32x4 pk8(const f32x4& a, const f32x4& b, float sc) {
    return u32x4{pk_bf16(a[0] * sc, a[1] * sc), pk_bf16(a[2] * sc, a[3] * sc), pk_bf16(b[0] * sc, b[1] * sc), pk_bf16(b[2] * sc, b[3] * sc)};
}

#define CODE_PROJ "_Z6k_projPKcS0_S0_S0_S0_S0_S0_S0_Pc"
#define CODE_ATTN "_Z6k_attnPcS_"
#define CODE_BYTES 8192
__global__ __launch_bounds__(768, 3) void k_proj(const char* __restrict__ x, const char* __restrict__ wq, const char* __restrict__ wqkv,
                                                 const char* __restrict__ wk, const char* __restrict__ wv, const char* __restrict__ gw,
                                                 const char* __restrict__ woa, const char* __restrict__ wob, char* __restrict__ ws) {
    __shared__ __attribute__((aligned(16))) char lds[4 * P_SLOT];
    const unsigned long long pr0 = __builtin_amdgcn_s_memrealtime();
    STAMP_DECL; STAMP(0);
    const int b = blockIdx.x, xcd = b & 7, ii = b >> 3;
    const int mb = (xcd & 1) * 8 + (ii & 7), type = xcd >> 1, nq = ii >> 3;
    const int tid = threadIdx.x, l = tid & 63, wv_ = __builtin_amdgcn_readfirstlane(tid >> 6);
    const int ldw2 = type == 0 ? 1280 : 3840;
    const int eidx = ((nq >> 1) | ((nq & 1) << 1)) * 4 + type, ehalf = eidx >= 5, ekind = eidx < 10 ? eidx - 5 * ehalf : 5;
    int rot = (ii & 7) + nq + type; rot = 2 * (rot >= 10 ? rot - 10 : (rot >= 5 ? rot - 5 : rot));
    const int ebytes = ekind < 4 ? 2560 : 128, nbytes = ebytes - (wv_ - 4) * 1024;
    const unsigned long long m5 = ~0ull >> (nbytes >= 1024 ? 0 : (nbytes > 0 ? 64 - (nbytes >> 4) : 63));
    const int sw = 64 * ldw2, se = wv_ < 4 ? sw : ebytes, wofs = (type > 1 ? (type - 1) * 1280 : 0) + nq * 320 + rot * sw;
    unsigned xo[2], wo[3];
    xo[0] = (mb * 128 + (tid >> 3)) * (DM * 2) + (((tid ^ (tid >> 4)) & 7) << 4) + rot * 128; xo[1] = xo[0] + 64 * DM * 2;
#pragma unroll
    for (int n = 0; n < 3; ++n) { const int cid = tid + 512 * n, k = (cid * 3277) >> 16, c = cid - 20 * k; wo[n] = k * ldw2 + ((c ^ (((k >> 3) & 1) << 1)) << 4) + wofs; }
    if (tid >= 256) wo[2] = l * 16 + (nbytes > 0 ? (wv_ - 4) * 1024 : 0) + rot * ebytes;
    asm volatile("" : "+v"(xo[0]), "+v"(xo[1]), "+v"(wo[0]), "+v"(wo[1]), "+v"(wo[2])
                 : "s"(x), "s"(wq), "s"(wqkv), "s"(wk), "s"(wv), "s"(gw), "s"(woa), "s"(wob), "s"(ws), "s"(m5), "s"(se), "s"(ekind));
    if (__builtin_expect(wv_ < 8, 1)) {
        const char* wsel = type == 0 ? wq : wqkv;
        proj_loader(lds, x, wsel, wv_ < 4 ? wsel : (ekind < 2 ? wk : (ekind < 4 ? wv : gw)), sw, se, m5, rot, ekind, ehalf, mb, woa, wob, ws, xo, wo);
        return;
    }
    const int w = wv_ - 8;
    if (w < 2) {
        const unsigned dp = (unsigned)(uintptr_t)lds + P_SLOT - 512, off = (ii * 3 + l) * 128; unsigned sv; unsigned long long pc;
        if (w == 0) asm volatile("s_getpc_b64 s[40:41]\n\ts_add_u32 s40, s40, " CODE_PROJ "@rel32@lo+4\n\ts_addc_u32 s41, s41, " CODE_PROJ "@rel32@hi+12" : "={s[40:41]}"(pc) :: "scc");
        else asm volatile("s_getpc_b64 s[40:41]\n\ts_add_u32 s40, s40, " CODE_ATTN "@rel32@lo+4\n\ts_addc_u32 s41, s41, " CODE_ATTN "@rel32@hi+12" : "={s[40:41]}"(pc) :: "scc");
        if (l < 3 && (w == 0 || off < CODE_BYTES))
            asm volatile("s_mov_b32 %0, m0\n\ts_mov_b32 m0, %1\n\ts_nop 0\n\tglobal_load_lds_dword %2, %3\n\ts_mov_b32 m0, %0" : "=&s"(sv) : "s"(dp), "v"(off), "s"(pc) : "memory");
    }
    const int wm = w >> 1, wn = w & 1, r = l & 15, g = l >> 4;
    f32x16 acc[5];
#pragma unroll
    for (int j = 0; j < 5; ++j)
#pragma unroll
        for (int i = 0; i < 16; ++i) acc[j][i] = 0.f;
    const unsigned lds0 = (unsigned)(uintptr_t)lds;
    const int chunk = mb * 2 + wm;
    if (type == 3) {
        proj_mma<true>(lds0, acc);
        STAMP(3);
        char* base = ws + WS_KV + (size_t)(nq * 8 + 4 * wn) * KV_HEAD + chunk * 5120 + 2560 + g * 320;
#pragma unroll
        for (int t = 0; t < 2; ++t) {
#pragma unroll
            for (int j = 0; j < 4; ++j) st16(base + (size_t)j * KV_HEAD + t * 1280 + r * 16, pk8(ACC(2 * t, j), ACC(2 * t + 1, j), 1.0f));
            st16(base + (size_t)(r >> 2) * KV_HEAD + t * 1280 + (16 + (r & 3)) * 16, pk8(ACC(2 * t, 4), ACC(2 * t + 1, 4), 1.0f));
        }
    } else {
        proj_mma<false>(lds0, acc);
        STAMP(3);
        const float sc = type == 2 ? 1.0f : 0.32259784f;
        const size_t hstride = type == 2 ? KV_HEAD : Q_HEAD;
        const int row0 = type == 2 ? 16 * (r >> 3) + (r & 7) : r, ustep = type == 2 ? 128 : 256;
        char* base = (type == 2 ? ws + WS_KV + chunk * 5120 : ws + WS_Q + (size_t)type * 32 * Q_HEAD + chunk * 2560) + (size_t)(nq * 8 + 4 * wn) * hstride + row0 * 16;
#pragma unroll
        for (int i = 0; i < 4; ++i)
#pragma unroll
            for (int jj = 0; jj < 2; ++jj)
                st16(base + (2 * jj + (g >> 1)) * hstride + (i >> 1) * 1024 + (g & 1) * 512 + (i & 1) * ustep, pk8(ACC(i, 2 * jj), ACC(i, 2 * jj + 1), sc));
#pragma unroll
        for (int u = 0; u < 2; ++u) st16(base + g * hstride + 2048 + u * ustep, pk8(ACC(u, 4), ACC(u + 2, 4), sc));
    }
    if (threadIdx.x == 512) { ((unsigned long long*)(ws + (64u << 20)))[blockIdx.x * 2] = pr0; ((unsigned long long*)(ws + (64u << 20)))[blockIdx.x * 2 + 1] = __builtin_amdgcn_s_memrealtime(); }
}

#define S0R 56
#define S1R 88
#define NMR 120
#define VAR 152
#define VBR 160
#define KAR 168
#define KBR 172
#define WFR 176
#define E_(X, i) "v_exp_f32 v[" STR(X) "+" #i "], v[" STR(X) "+" #i "]\n\t"
#define C_(X, d, a, b) "v_cvt_pk_bf16_f32 v[" STR(X) "+" #d "], v[" STR(X) "+" #a "], v[" STR(X) "+" #b "]\n\t"
#define PV_(O, V, vo, X, po) "v_mfma_f32_32x32x16_bf16 %[" #O "], v[" STR(V) "+" #vo ":" STR(V) "+" #vo "+3], v[" STR(X) "+" #po ":" STR(X) "+" #po "+3], %[" #O "]\n\t"
#define M1_(Y, KB) "v_mfma_f32_32x32x4_2b_bf16 v[" STR(Y) ":" STR(Y) "+31], " KB ", %[q2], v[" STR(NMR) ":" STR(NMR) "+31]\n\t"
#define M2_(Y, o, Q) "v_mfma_f32_32x32x16_bf16 v[" STR(Y) "+" #o ":" STR(Y) "+" #o "+15], " KA_ ", %[" #Q "], v[" STR(Y) "+" #o ":" STR(Y) "+" #o "+15]\n\t"
#define KA_ "v[" STR(KAR) ":" STR(KAR) "+3]"
#define KB0_ "v[" STR(KBR) ":" STR(KBR) "+1]"
#define KB1_ "v[" STR(KBR) "+2:" STR(KBR) "+3]"
#define RDKA(off) "ds_read_b128 " KA_ ", %[ak1] offset:" STR(off) "\n\t"
#define RDKB(off) "ds_read_b128 v[" STR(KBR) ":" STR(KBR) "+3], %[ak2] offset:" STR(off) "\n\t"
#define RDV(V, off) "s_mov_b64 exec, %[vmask]\n\tds_read_b128 v[" STR(V) ":" STR(V) "+3], %[av] offset:" STR(off) "\n\t" \
                    "ds_read_b128 v[" STR(V) "+4:" STR(V) "+7], %[av] offset:" STR(off) "+640\n\ts_mov_b64 exec, -1\n\t"
#define MX3(d, a, i) "v_max3_f32 v[" STR(NMR) "+" #d "], v[" STR(a) "], v[" STR(S0R) "+" #i "], v[" STR(S0R) "+" #i "+1]\n\t"
#define PRIO(n) "s_setprio " #n "\n\t"
#define DMA1 "s_mov_b32 m0, %[dm]\n\ts_add_u32 %[dm], %[dm], 8192\n\tglobal_load_lds_dwordx4 %[dv], %[kvp]\n\tv_add_u32 %[dv], 0x2000, %[dv]\n\t"
#define WLD1(s, j) "global_load_dwordx4 v[" STR(WFR) "+20*" #s "+4*" #j ":" STR(WFR) "+20*" #s "+4*" #j "+3], %[wv], %[wp] offset:" #j "*1024-2048\n\t"
#define WLD(s) WLD1(s, 0) WLD1(s, 1) WLD1(s, 2) WLD1(s, 3) WLD1(s, 4) "v_add_u32 %[wv], 0x1400, %[wv]\n\ts_branch 1b\n\t"
#define TILE(X, Y, VC, VP, KAOFF, KB, KBRD, VOFF, PRE) \
    PRE E_(X, 0) E_(X, 1) \
    PV_(ob, VP, 4, Y, 24) \
    E_(X, 2) E_(X, 3) C_(X, 0, 0, 1) E_(X, 4) E_(X, 5) C_(X, 1, 2, 3) \
    "s_waitcnt lgkmcnt(0)\n\t" M1_(Y, KB) KBRD \
    E_(X, 6) E_(X, 7) C_(X, 2, 4, 5) E_(X, 8) C_(X, 3, 6, 7) E_(X, 9) E_(X, 10) E_(X, 11) C_(X, 8, 8, 9) \
    PV_(oa, VC, 0, X, 0) RDV(VP, VOFF) \
    E_(X, 12) C_(X, 9, 10, 11) E_(X, 13) E_(X, 14) E_(X, 15) C_(X, 10, 12, 13) E_(X, 16) C_(X, 11, 14, 15) E_(X, 17) \
    M2_(Y, 0, q1a) \
    E_(X, 18) E_(X, 19) C_(X, 16, 16, 17) E_(X, 20) E_(X, 21) C_(X, 17, 18, 19) \
    PV_(oa, VC, 4, X, 8) \
    E_(X, 22) E_(X, 23) C_(X, 18, 20, 21) E_(X, 24) C_(X, 19, 22, 23) E_(X, 25) \
    M2_(Y, 16, q1b) RDKA(KAOFF) \
    E_(X, 26) E_(X, 27) C_(X, 24, 24, 25) E_(X, 28) E_(X, 29) C_(X, 25, 26, 27) \
    PV_(ob, VC, 0, X, 16) \
    E_(X, 30) E_(X, 31) C_(X, 26, 28, 29) C_(X, 27, 30, 31)

DI float max_halves(float m) {
    auto rr = __builtin_amdgcn_permlane32_swap(__float_as_uint(m), __float_as_uint(m), false, false);
    return fmaxf(__uint_as_float(rr[0]), __uint_as_float(rr[1]));
}
DI void attn_fallback(const char* lds, int l, const char* qp, int tile, f32x16& o) {
    const int r32 = l & 31, h = l >> 5;
    const s16x4 z4 = {0, 0, 0, 0};
    const bf16x8 q1 = *(const bf16x8*)(qp + tile * 1024 + l * 16);
    const s16x4 q2 = *(const s16x4*)(qp + 2048 + r32 * 16 + tile * 8);
    const bf16x8 q2f = cat8(h == 0 ? q2 : z4, z4);
    const bf16x8 ones = {0x3f80, 0x3f80, 0x3f80, 0x3f80, 0x3f80, 0x3f80, 0x3f80, 0x3f80};
    float m = -INFINITY;
#pragma unroll
    for (int i = 0; i < 16; ++i) o[i] = 0.f;
#pragma unroll 1
    for (int t = 0; t < 64; ++t) {
        const LDSP char* cb = (const LDSP char*)lds + (t >> 1) * 5120;
        const bf16x8 k1 = *(const LDSP bf16x8*)(cb + (t & 1) * 1024 + l * 16);
        const s16x4 k2 = *(const LDSP s16x4*)(cb + 2048 + r32 * 16 + (t & 1) * 8);
        f32x16 s;
#pragma unroll
        for (int i = 0; i < 16; ++i) s[i] = 0.f;
        s = MFMA32(k1, q1, s);
        s = MFMA32(cat8(k2, z4), q2f, s);
        float tm = s[0];
#pragma unroll
        for (int i = 1; i < 16; ++i) tm = fmaxf(tm, s[i]);
        tm = max_halves(tm);
        const float mn = fmaxf(m, tm), alpha = __builtin_amdgcn_exp2f(m - mn);
        m = mn;
        u32x4 p0, p1;
#pragma unroll
        for (int i = 0; i < 4; ++i) {
            p0[i] = pk_bf16(__builtin_amdgcn_exp2f(s[2 * i] - m), __builtin_amdgcn_exp2f(s[2 * i + 1] - m));
            p1[i] = pk_bf16(__builtin_amdgcn_exp2f(s[8 + 2 * i] - m), __builtin_amdgcn_exp2f(s[9 + 2 * i] - m));
        }
#pragma unroll
        for (int i = 0; i < 16; ++i) o[i] *= alpha;
        const int dv = r32 < 20 ? r32 : 0;
        bf16x8 v0 = *(const LDSP bf16x8*)(cb + 2560 + (t & 1) * 1280 + h * 320 + dv * 16);
        bf16x8 v1 = *(const LDSP bf16x8*)(cb + 2560 + (t & 1) * 1280 + 640 + h * 320 + dv * 16);
        if (r32 >= 20) { v0 = ones; v1 = ones; }
        o = MFMA32(v0, __builtin_bit_cast(bf16x8, p0), o);
        o = MFMA32(v1, __builtin_bit_cast(bf16x8, p1), o);
    }
}

DI bool attn_ok(const f32x16& a, const f32x16& b) {
    f32x2 s = {0.f, 0.f};
#pragma unroll
    for (int i = 0; i < 12; i += 2) s += f32x2{a[i], a[i + 1]} + f32x2{b[i], b[i + 1]};
    return (fmaxf(a[12], b[12]) < 0x1p80f) & (fabsf(s[0] + s[1] + a[12] + b[12]) < 0x1p126f);
}
DI void attn_pack(const f32x16& oa, const f32x16& ob, float ga, float gb, u32x4& va, u32x4& vb, u32x2& vt) {
    const float fa = ga * __builtin_amdgcn_rcpf(oa[12]), fb = gb * __builtin_amdgcn_rcpf(ob[12]);
    va = u32x4{pk_bf16(oa[0] * fa, oa[1] * fa), pk_bf16(oa[2] * fa, oa[3] * fa), pk_bf16(oa[4] * fa, oa[5] * fa), pk_bf16(oa[6] * fa, oa[7] * fa)};
    vb = u32x4{pk_bf16(ob[0] * fb, ob[1] * fb), pk_bf16(ob[2] * fb, ob[3] * fb), pk_bf16(ob[4] * fb, ob[5] * fb), pk_bf16(ob[6] * fb, ob[7] * fb)};
    auto s0 = __builtin_amdgcn_permlane32_swap(pk_bf16(oa[8] * fa, oa[9] * fa), pk_bf16(ob[8] * fb, ob[9] * fb), false, false);
    auto s1 = __builtin_amdgcn_permlane32_swap(pk_bf16(oa[10] * fa, oa[11] * fa), pk_bf16(ob[10] * fb, ob[11] * fb), false, false);
    vt = u32x2{s0[0], s1[0]};
}

typedef __attribute__((ext_vector_type(16))) unsigned u32x16;
#define WSUB(v, i) __builtin_bit_cast(bf16x8, __builtin_shufflevector(v, v, 4 * (i), 4 * (i) + 1, 4 * (i) + 2, 4 * (i) + 3))

__global__ __launch_bounds__(512, 2) void k_attn(char* __restrict__ ws, char* __restrict__ out) {
    __shared__ __attribute__((aligned(16))) char lds[163840];
    const unsigned long long r0 = __builtin_amdgcn_s_memrealtime(), c0 = __builtin_amdgcn_s_memtime();
    STAMP_DECL; STAMP(0);
    const int b = blockIdx.x, xcd = b & 7, ii = b >> 3;
    const int pos = ii >> 2, head = (pos >> 2) * 32 + xcd * 4 + (pos & 3), slot = xcd * 8 + pos, qb = ii & 3;
    const int w = __builtin_amdgcn_readfirstlane(threadIdx.x >> 6);
    int l = threadIdx.x & 63, r32 = l & 31, h = l >> 5;
    const int qc = qb * 8 + w;
    const unsigned lds0 = (unsigned)(uintptr_t)lds;
    const unsigned qo = (unsigned)WS_Q + head * (unsigned)Q_HEAD + qc * 2560, kvo = (unsigned)WS_KV + (head < 32 ? 32 : head - 32) * (unsigned)KV_HEAD;
    bf16x8 q1a, q1b, k1f, k2f; s16x4 q2; float ga, gb;
    unsigned dv = kvo + w * 1024 + l * 16;
    {
        unsigned sv, dm = __builtin_amdgcn_readfirstlane(lds0 + w * 1024);
        asm volatile(
            "global_load_dwordx4 v[4:7], %[a0], %[kvp]\n\tglobal_load_dwordx4 v[8:11], %[a0], %[kvp] offset:1024\n\tglobal_load_dwordx2 v[12:13], %[a1], %[kvp] offset:2048\n\t"
            "global_load_dwordx4 " KA_ ", %[a2], %[kvp]\n\tglobal_load_dwordx4 v[" STR(KBR) ":" STR(KBR) "+3], %[a3], %[kvp] offset:2048\n\t"
            "global_load_dword v14, %[a4], %[kvp]\n\tglobal_load_dword v15, %[a4], %[kvp] offset:128\n\t"
            "s_mov_b32 %[sv], m0\n\t" DMA1 DMA1 DMA1 DMA1 "s_mov_b32 m0, %[sv]"
            : "=&{v[4:7]}"(q1a), "=&{v[8:11]}"(q1b), "=&{v[12:13]}"(q2), "=&{v14}"(ga), "=&{v15}"(gb), "=&{v[168:171]}"(k1f), "=&{v[172:175]}"(k2f),
              [sv] "=&s"(sv), [dv] "+v"(dv), [dm] "+s"(dm)
            : [kvp] "s"(ws), [a0] "v"(qo + l * 16), [a1] "v"(qo + r32 * 16 + h * 8), [a2] "v"(kvo + l * 16), [a3] "v"(kvo + r32 * 16), [a4] "v"((unsigned)WS_G + qc * 256 + r32 * 4)
            : "memory", "scc");
    }
    const int mb = xcd * 4 + (ii & 3), nb = ii >> 2;
    const char* wp = ws + WS_WO + (size_t)(nb * 8 + w) * 25600 + 2048;
    const char* cp = ws + WS_CNT + (((mb >> 3) * 8 + w) * 8 + nb) * CNT_STRIDE;
    const char* sgp = ws + WS_CNT + (qb * 8 + xcd) * 8 * CNT_STRIDE;
    const char* a1 = ws + WS_O1 + (size_t)w * 8 * 65536 + mb * 2048;
    const char* a2 = ws + WS_O2 + (size_t)w * 4 * 32768 + mb * 1024;
    const char* op = out + (size_t)mb * 64 * 1280 + nb * 160;
    const char* po1 = ws + WS_O1 + (size_t)slot * 65536 + qc * 2048;
    const char* po2 = ws + WS_O2 + (size_t)(slot >> 1) * 32768 + qc * 1024 + (slot & 1) * 8;
    f32x16 oa, ob; u32x16 vini;
#pragma unroll
    for (int i = 0; i < 16; ++i) { oa[i] = 0.f; ob[i] = 0.f; vini[i] = (r32 & 27) == 24 ? 0x3f803f80u : 0u; }
    unsigned ak1 = lds0 + l * 16, ak2 = lds0 + 2048 + r32 * 16, av = lds0 + 2560 + h * 320 + r32 * 16, wv = l * 16;
    unsigned cnt, inc, svm, dm = __builtin_amdgcn_readfirstlane(lds0 + w * 1024 + 32768);
    u32x16 wt0, wt1, wt2, wt3, wt4;
    STAMP(2);
#define LSTAMP ""
    asm volatile(
        "s_mov_b32 %[sv], m0\n\t"
        ".irp n,24,25,26,27\n\tv_mov_b32 v[" STR(S1R) "+\\n], 0\n\t.endr\n\t"
        "s_mov_b32 %[cnt], 0\n\ts_mov_b32 %[inc], 20480\n\ts_mov_b64 vcc, -1\n\t"
        "s_waitcnt vmcnt(6)\n\t"
        "v_mfma_f32_32x32x4_2b_bf16 v[" STR(S0R) ":" STR(S0R) "+31], " KB0_ ", %[q2], 0\n\ts_nop 15\n\ts_nop 1\n\t"
        M2_(S0R, 0, q1a) M2_(S0R, 16, q1b)
        "s_nop 10\n\t"
        MX3(0, S0R + 0, 1) MX3(16, S0R + 16, 17) MX3(0, NMR, 3) MX3(16, NMR + 16, 19) MX3(0, NMR, 5) MX3(16, NMR + 16, 21) MX3(0, NMR, 7) MX3(16, NMR + 16, 23)
        MX3(0, NMR, 9) MX3(16, NMR + 16, 25) MX3(0, NMR, 11) MX3(16, NMR + 16, 27) MX3(0, NMR, 13) MX3(16, NMR + 16, 29) MX3(0, NMR, 14) MX3(16, NMR + 16, 30)
        "v_mov_b32 v[" STR(NMR) "+1], v[" STR(NMR) "]\n\tv_mov_b32 v[" STR(NMR) "+17], v[" STR(NMR) "+16]\n\ts_nop 1\n\t"
        "v_permlane32_swap_b32 v[" STR(NMR) "], v[" STR(NMR) "+1]\n\tv_permlane32_swap_b32 v[" STR(NMR) "+16], v[" STR(NMR) "+17]\n\ts_nop 1\n\t"
        "v_min_f32 v[" STR(NMR) "], -v[" STR(NMR) "], -v[" STR(NMR) "+1]\n\tv_min_f32 v[" STR(NMR) "+16], -v[" STR(NMR) "+16], -v[" STR(NMR) "+17]\n\t"
        ".irp n,1,2,3,4,5,6,7,8,9,10,11,12,13,14,15\n\tv_mov_b32 v[" STR(NMR) "+\\n], v[" STR(NMR) "]\n\tv_mov_b32 v[" STR(NMR) "+16+\\n], v[" STR(NMR) "+16]\n\t.endr\n\t"
        ".irp n,0,2,4,6,8,10,12,14,16,18,20,22,24,26,28,30\n\tv_pk_add_f32 v[" STR(S0R) "+\\n:" STR(S0R) "+\\n+1], v[" STR(S0R) "+\\n:" STR(S0R) "+\\n+1], v[" STR(NMR) "+\\n:" STR(NMR) "+\\n+1]\n\t.endr\n\t"
        "s_waitcnt vmcnt(0)\n\ts_barrier\n\t"
        RDKA(1024) RDV(VAR, 0)
        "1:\n\t"
        LSTAMP
        TILE(S0R, S1R, VAR, VBR, 5120, KB1_, RDKB(5120), 1280, PRIO(3) "s_cbranch_vccnz 11f\n\t21:\n\t")
        TILE(S1R, S0R, VBR, VAR, 5120 + 1024, KB0_, "", 5120, "s_cbranch_vccnz 12f\n\t22:\n\t")
        TILE(S0R, S1R, VAR, VBR, 10240, KB1_, RDKB(10240), 5120 + 1280, "s_cbranch_vccnz 13f\n\t23:\n\t")
        TILE(S1R, S0R, VBR, VAR, 10240 + 1024, KB0_, "", 10240, "s_cbranch_vccnz 14f\n\t24:\n\t")
        TILE(S0R, S1R, VAR, VBR, 15360, KB1_, RDKB(15360), 10240 + 1280, PRIO(2))
        TILE(S1R, S0R, VBR, VAR, 15360 + 1024, KB0_, "", 15360, "s_cmp_eq_u32 %[cnt], 7\n\ts_cselect_b32 %[inc], -143360, %[inc]\n\t")
        TILE(S0R, S1R, VAR, VBR, 0, KB1_, RDKB(0), 15360 + 1280, PRIO(1) "v_add_u32 %[ak1], %[inc], %[ak1]\n\tv_add_u32 %[ak2], %[inc], %[ak2]\n\t")
        TILE(S1R, S0R, VBR, VAR, 1024, KB0_, "", 0, PRIO(0) "v_add_u32 %[av], %[inc], %[av]\n\t")
        "s_add_u32 %[cnt], %[cnt], 1\n\t"
        "s_cmp_eq_u32 %[cnt], 1\n\ts_cbranch_scc1 2f\n\t"
        "s_cmp_eq_u32 %[cnt], 3\n\ts_cbranch_scc1 3f\n\t"
        "s_cmp_eq_u32 %[cnt], 4\n\ts_cbranch_scc1 5f\n\t"
        "s_cmp_eq_u32 %[cnt], 5\n\ts_cbranch_scc1 6f\n\t"
        "s_cmp_eq_u32 %[cnt], 6\n\ts_cbranch_scc1 8f\n\t"
        "s_cmp_lt_u32 %[cnt], 8\n\ts_cbranch_scc1 1b\n\t"
        "s_branch 4f\n\t"
        "11:\n\t" DMA1 DMA1 DMA1 DMA1 "s_branch 21b\n\t"
        "12:\n\t" DMA1 DMA1 DMA1 DMA1 "s_branch 22b\n\t"
        "13:\n\t" DMA1 DMA1 DMA1 DMA1 "s_branch 23b\n\t"
        "14:\n\t" DMA1 DMA1 DMA1 DMA1 "s_branch 24b\n\t"
        "2:\n\ts_mov_b64 vcc, 0\n\ts_waitcnt vmcnt(0)\n\ts_barrier\n\ts_branch 1b\n\t"
        "3:\n\t" WLD(0)
        "5:\n\t" WLD(1)
        "6:\n\t" WLD(2)
        "8:\n\tv_and_b32 %[dv], 0x3ff, %[wv]\n\tv_lshlrev_b32 %[dv], 3, %[dv]\n\tv_min_u32 %[dv], 0x1380, %[dv]\n\tv_add_u32 %[dv], 0x5000, %[dv]\n\t"
        "global_load_dword %[dv], %[dv], %[wp] offset:-2048\n\t" WLD(3)
        "4:\n\ts_nop 1\n\t"
        LSTAMP
        PV_(ob, VBR, 4, S1R, 24)
        "s_mov_b32 m0, %[sv]\n\ts_waitcnt vmcnt(0) lgkmcnt(0)\n\ts_nop 15\n\ts_nop 7"
        : [oa] "+{v[24:39]}"(oa), [ob] "+{v[40:55]}"(ob), [ak1] "+v"(ak1), [ak2] "+v"(ak2), [av] "+v"(av), [dv] "+v"(dv), [wv] "+v"(wv),
          "+{v[152:167]}"(vini), "+{v[168:171]}"(k1f), "+{v[172:175]}"(k2f),
          [q1a] "+{v[4:7]}"(q1a), [q1b] "+{v[8:11]}"(q1b), [q2] "+{v[12:13]}"(q2), "+{v14}"(ga), "+{v15}"(gb),
          "=&{v[176:191]}"(wt0), "=&{v[192:207]}"(wt1), "=&{v[208:223]}"(wt2), "=&{v[224:239]}"(wt3), "=&{v[240:255]}"(wt4),
          [cnt] "=&s"(cnt), [inc] "=&s"(inc), [sv] "=&s"(svm), [dm] "+s"(dm)
        : [vmask] "s"(0x000fffff000fffffull), [kvp] "s"(ws), [wp] "s"(wp),
          "s"(cp), "s"(sgp), "s"(a1), "s"(a2), "s"(op), "s"(po1), "s"(po2)
        : "memory", "scc", "vcc", "v56", "v57", "v58", "v59", CL10(6), CL10(7), CL10(8), CL10(9), CL10(10), CL10(11), CL10(12), CL10(13), CL10(14), "v150", "v151"
          );
    asm volatile("v_mbcnt_lo_u32_b32 %0, -1, 0\n\tv_mbcnt_hi_u32_b32 %0, -1, %0" : "=v"(l));
    r32 = l & 31; h = l >> 5;
    const unsigned long long r3 = __builtin_amdgcn_s_memrealtime(), c3 = __builtin_amdgcn_s_memtime();
    STAMP(3);
    bf16x8 wl[5], af[5][4]; unsigned seen;
    {
        if (head >= 32) { ga = 1.0f - ga; gb = 1.0f - gb; }
        u32x4 va, vb; u32x2 vt;
        attn_pack(oa, ob, ga, gb, va, vb, vt);
        if (__builtin_expect(!__all(attn_ok(oa, ob)), 0)) {
#pragma unroll 1
            for (int t = 0; t < 2; ++t) {
                f32x16 ro; u32x4 v0, v1; u32x2 v2;
                attn_fallback(lds, l, ws + qo, t, ro);
                attn_pack(ro, ro, t ? gb : ga, t ? gb : ga, v0, v1, v2);
                if (t == 0) va = v0; else vb = v0;
                if (h == t) vt = v2;
            }
        }
        asm volatile(
            "global_store_dwordx4 %[lo], %[va], %[o1] sc1\n\tglobal_store_dwordx4 %[lo], %[vb], %[o1] offset:1024 sc1\n\tglobal_store_dwordx2 %[lo], %[vt], %[o2] sc1\n\t"
            "s_nop 0"
            :: [lo] "v"(l * 16), [va] "v"(va), [vb] "v"(vb), [vt] "v"(vt), [o1] "s"(po1), [o2] "s"(po2)
            : "memory");
    }
    const int r = l & 15, g = l >> 4, r8 = l & 7, g2 = (l >> 3) & 3;
    unsigned o1 = (g >> 1) * 65536 + (g & 1) * 512 + r * 16, o2 = g * 32768 + r * 16;
    unsigned wbo = w * 20480 + (g * 16 + (r ^ ((g >> 1) << 3))) * 16;
    unsigned rbo = ((w >> 1) * 5 + h) * 1024 + (g2 * 16 + ((8 * (w & 1) + r8) ^ ((g2 >> 1) << 3))) * 16;
    unsigned opo = (8 * w + r8) * 1280 + h * 32 + g2 * 8, sgo = l * CNT_STRIDE;
    asm volatile("s_waitcnt vmcnt(0)\n\ts_barrier\n\t"
                 "s_cmp_lg_u32 %[w], %[sw]\n\ts_cbranch_scc1 1f\n\ts_mov_b64 exec, 0xff\n\tglobal_atomic_add %[so], %[one], %[sp]\n\ts_mov_b64 exec, -1\n\t1:"
                 : "+v"(o1), "+v"(o2), "+v"(wbo), "+v"(rbo), "+v"(opo)
                 : [w] "s"(w), [sw] "s"(xcd ^ 1), [so] "v"(sgo), [one] "v"(1), [sp] "s"(sgp) : "memory", "scc");
    STAMP(4);
    asm volatile(
        "global_load_dwordx4 %0, %6, %7 offset:-2048\n\tglobal_load_dwordx4 %1, %6, %7 offset:-1024\n\tglobal_load_dwordx4 %2, %6, %7\n\t"
        "global_load_dwordx4 %3, %6, %7 offset:1024\n\tglobal_load_dwordx4 %4, %6, %7 offset:2048\n\tglobal_load_dword %5, %8, %9 sc1\n\ts_waitcnt vmcnt(0)"
        : "=&v"(wl[0]), "=&v"(wl[1]), "=&v"(wl[2]), "=&v"(wl[3]), "=&v"(wl[4]), "=&v"(seen) : "v"(wv), "s"(wp), "v"(0), "s"(cp) : "memory");
    const unsigned late = __builtin_amdgcn_readfirstlane(seen);
#pragma unroll 1
    for (int spin = 0; spin < 400000 && __builtin_amdgcn_readfirstlane(seen) < 8; ++spin) {
        __builtin_amdgcn_s_sleep(POLL_SLEEP);
        asm volatile("global_load_dword %0, %1, %2 sc1\n\ts_waitcnt vmcnt(0)" : "=v"(seen) : "v"(0), "s"(cp) : "memory");
    }
    STAMP(5);
#define ALD(s, base, off, d1, d2) asm volatile( \
        "global_load_dwordx4 %0, %4, %5 sc1\n\tglobal_load_dwordx4 %1, %4, %5 offset:" #d1 " sc1\n\t" \
        "global_load_dwordx4 %2, %4, %5 offset:" #d2 " sc1\n\tglobal_load_dwordx4 %3, %4, %5 offset:" #d1 "+" #d2 " sc1" \
        : "=&v"(af[s][0]), "=&v"(af[s][1]), "=&v"(af[s][2]), "=&v"(af[s][3]) : "v"(off), "s"(base) : "memory")
#define AWAIT(s, N) asm volatile("s_waitcnt vmcnt(" STR(N) ")" : "+v"(af[s][0]), "+v"(af[s][1]), "+v"(af[s][2]), "+v"(af[s][3]) :: "memory")
#define OMM(s, w0, w1, w2, w3, w4) do { const bf16x8 wf_[5] = {w0, w1, w2, w3, w4}; \
        _Pragma("unroll") for (int j = 0; j < 5; ++j) _Pragma("unroll") for (int i = 0; i < 4; ++i) acc[i][j] = MFMA16(wf_[j], af[s][i], acc[i][j]); \
        __builtin_amdgcn_sched_barrier(0); } while (0)
    f32x4 acc[4][5];
#pragma unroll
    for (int i = 0; i < 4; ++i)
#pragma unroll
        for (int j = 0; j < 5; ++j) acc[i][j] = f32x4{0.f, 0.f, 0.f, 0.f};
    ALD(0, a1, o1, 256, 1024); ALD(1, a1 + 131072, o1, 256, 1024); ALD(2, a1 + 2 * 131072, o1, 256, 1024);
    AWAIT(0, 8); ALD(3, a1 + 3 * 131072, o1, 256, 1024); OMM(0, WSUB(wt0, 0), WSUB(wt0, 1), WSUB(wt0, 2), WSUB(wt0, 3), WSUB(wt1, 0));
    AWAIT(1, 8); ALD(4, a2, o2, 256, 512); OMM(1, WSUB(wt1, 1), WSUB(wt1, 2), WSUB(wt1, 3), WSUB(wt2, 0), WSUB(wt2, 1));
    AWAIT(2, 8); OMM(2, WSUB(wt2, 2), WSUB(wt2, 3), WSUB(wt3, 0), WSUB(wt3, 1), WSUB(wt3, 2));
    AWAIT(3, 4); OMM(3, WSUB(wt3, 3), WSUB(wt4, 0), WSUB(wt4, 1), WSUB(wt4, 2), WSUB(wt4, 3));
    AWAIT(4, 0);
#pragma unroll
    for (int j = 0; j < 6; ++j) {
        if (j < 5) {
#pragma unroll
            for (int i = 0; i < 4; ++i) acc[i][j] = MFMA16(wl[j], af[4][i], acc[i][j]);
        }
        if (j > 0) {
#pragma unroll
            for (int i = 0; i < 4; ++i) *(f32x4*)(lds + wbo + (i * 5 + j - 1) * 1024) = acc[i][j - 1];
        }
        __builtin_amdgcn_sched_barrier(0);
    }
    STAMP(6);
    {
        __syncthreads();
        f32x4 t[3][8];
#pragma unroll
        for (int u = 0; u < 8; ++u)
#pragma unroll
            for (int p = 0; p < 3; ++p) t[p][u] = *(const f32x4*)(lds + rbo + u * 20480 + (p < 2 ? p * 2048 : 4096 - h * 1024));
        __builtin_amdgcn_sched_barrier(0);
#pragma unroll
        for (int p = 0; p < 3; ++p) {
            const f32x4 z = ((t[p][0] + t[p][1]) + (t[p][2] + t[p][3])) + ((t[p][4] + t[p][5]) + (t[p][6] + t[p][7]));
            const u32x2 v = {pk_bf16(z[0], z[1]), pk_bf16(z[2], z[3])};
            if (p < 2 || h == 0) asm volatile("global_store_dwordx2 %0, %1, %2 offset:%3 " OUT_FLAGS :: "v"(opo), "v"(v), "s"(op), "i"(p * 64) : "memory");
        }
    }
    { const unsigned long long r7 = __builtin_amdgcn_s_memrealtime(); const unsigned long long p0 = ((const unsigned long long*)(ws + (64u << 20)))[blockIdx.x * 2], p1 = ((const unsigned long long*)(ws + (64u << 20)))[blockIdx.x * 2 + 1];
      unsigned long long iv = PROBE == 0 ? 0 : PROBE == 1 ? r0 - p0 : PROBE == 2 ? r3 - r0 : PROBE == 3 ? r7 - r3 : PROBE == 4 ? p1 - p0 : (c3 - c0) / 20;
      if (iv > 20000) iv = 20000;
      if (blockIdx.x == PROBE_B) for (int i = 0; i < 4000000 && __builtin_amdgcn_s_memrealtime() < r7 + 4 * iv; ++i) __builtin_amdgcn_s_sleep(1); }
}

extern "C" void kernel_launch(void* const* d_in, const int* in_sizes, int n_in, void* d_out, int out_size, void* d_ws, size_t ws_size,
                              hipStream_t stream) {
    const char* x = (const char*)d_in[0];
    const char* wq = (const char*)d_in[1];
    const char* wk = (const char*)d_in[2];
    const char* wv = (const char*)d_in[3];
    const char* woa = (const char*)d_in[4];
    const char* wqkv = (const char*)d_in[6];
    const char* wob = (const char*)d_in[7];
    const char* gw = (const char*)d_in[8];
    char* ws = (char*)d_ws;
    k_proj<<<256, 768, 0, stream>>>(x, wq, wqkv, wk, wv, gw, woa, wob, ws);
    k_attn<<<256, 512, 0, stream>>>(ws, (char*)d_out);
}
```

```cpp
#include <hip/hip_runtime.h>
#include <hip/hip_bf16.h>
#include <stdint.h>

typedef __attribute__((ext_vector_type(8))) short bf16x8;
typedef __attribute__((ext_vector_type(4))) short s16x4;
typedef __attribute__((ext_vector_type(4))) float f32x4;
typedef __attribute__((ext_vector_type(16))) float f32x16;
typedef __attribute__((ext_vector_type(32))) float f32x32;
typedef __attribute__((ext_vector_type(2))) unsigned u32x2;
typedef __attribute__((ext_vector_type(4))) unsigned u32x4;
typedef __attribute__((ext_vector_type(2))) __bf16 bf16x2_t;
typedef __attribute__((ext_vector_type(2))) float f32x2;
#define LDSP __attribute__((address_space(3)))
#define DI __device__ __forceinline__

#define STAMP_PARAMS
#define STAMP_ARGS
#define STAMP_DECL
#define STAMP(slot)
#define STAMP_FLUSH(wsp, kid)
constexpr int SEQ = 2048, DM = 640;
constexpr size_t Q_HEAD = 32 * 2560;
constexpr size_t KV_HEAD = 32 * 5120;
constexpr size_t WS_Q = 0;
constexpr size_t WS_KV = WS_Q + 64 * Q_HEAD;
constexpr size_t WS_G = WS_KV + 33 * KV_HEAD;
constexpr size_t WS_O1 = WS_G + 2048 * 4;
constexpr size_t WS_O2 = WS_O1 + 64 * 65536;
constexpr size_t WS_WO = WS_O2 + 32 * 2048 * 16;
#define OUT_FLAGS ""
#define CNT_STRIDE 128
#define POLL_SLEEP 4
constexpr size_t WS_CNT = WS_WO + 1600 * 1024;

DI unsigned pk_bf16(float a, float b) {
    f32x2 v = {a, b};
    return __builtin_bit_cast(unsigned, __builtin_convertvector(v, bf16x2_t));
}
#define ST_FLAGS "sc1"
DI void st16(char* p, u32x4 v) { asm volatile("global_store_dwordx4 %0, %1, off " ST_FLAGS "\n\ts_nop 1" :: "v"(p), "v"(v) : "memory"); }
DI void st8(char* p, u32x2 v) { asm volatile("global_store_dwordx2 %0, %1, off " ST_FLAGS :: "v"(p), "v"(v) : "memory"); }
DI s16x4 tr_read(const LDSP char* p) {
    typedef short v4i16_t __attribute__((ext_vector_type(4)));
    return __builtin_amdgcn_ds_read_tr16_b64_v4i16((LDSP v4i16_t*)p);
}
DI bf16x8 cat8(s16x4 lo, s16x4 hi) { return __builtin_shufflevector(lo, hi, 0, 1, 2, 3, 4, 5, 6, 7); }
#define MFMA16(a, b, c) __builtin_amdgcn_mfma_f32_16x16x32_bf16((a), (b), (c), 0, 0, 0)
#define MFMA32(a, b, c) __builtin_amdgcn_mfma_f32_32x32x16_bf16((a), (b), (c), 0, 0, 0)

constexpr int P_SLOT = 40960;

DI void proj_loader(char* lds, const char* xs, const char* ws, const char* es, int sw, int se, unsigned long long m5, int st, int ekind, int ehalf, int mb,
                    const char* woa, const char* wob, char* wsp, const unsigned (&xo)[2], const unsigned (&wo)[3]) {
    const int l = threadIdx.x & 63, w = __builtin_amdgcn_readfirstlane(threadIdx.x >> 6), r = l & 15, g = l >> 4;
    const unsigned dw = __builtin_amdgcn_readfirstlane((unsigned)(uintptr_t)lds + w * 1024);
    auto issue = [&](int slot) {
        const unsigned d0 = dw + slot * P_SLOT;
        unsigned sv;
        asm volatile(
            "s_mov_b32 %[sv], m0\n\ts_mov_b32 m0, %[d0]\n\ts_nop 0\n\t"
            "global_load_lds_dwordx4 %[x0], %[xs]\n\ts_add_u32 m0, m0, 8192\n\ts_nop 0\n\t"
            "global_load_lds_dwordx4 %[x1], %[xs]\n\ts_add_u32 m0, m0, 8192\n\ts_nop 0\n\t"
            "global_load_lds_dwordx4 %[w0], %[ws]\n\ts_add_u32 m0, m0, 8192\n\ts_nop 0\n\t"
            "global_load_lds_dwordx4 %[w1], %[ws]\n\ts_add_u32 m0, m0, 8192\n\ts_mov_b64 exec, %[m5]\n\t"
            "global_load_lds_dwordx4 %[w2], %[es]\n\ts_mov_b64 exec, -1\n\ts_mov_b32 m0, %[sv]"
            : [sv] "=&s"(sv)
            : [d0] "s"(d0), [m5] "s"(m5), [xs] "s"(xs), [ws] "s"(ws), [es] "s"(es),
              [x0] "v"(xo[0]), [x1] "v"(xo[1]), [w0] "v"(wo[0]), [w1] "v"(wo[1]), [w2] "v"(wo[2])
            : "memory", "scc");
        const int d = st == 9 ? -9 : 1;
        st += d; xs += d * 128; ws += d * sw; es += d * se;
    };
    issue(0); issue(1);
    __builtin_amdgcn_s_setprio(3);
    f32x4 eacc = {0.f, 0.f, 0.f, 0.f};
    const int ets = 4 * ehalf + w;
    if (w >= 4) ekind = 5;
    const int exr = (16 * ets + r) * 128 + ((g ^ ((r >> 1) & 7)) << 4);
    const int ewr = ekind < 4 ? 36864 + (8 * g + (r >> 2)) * 40 + ((ekind & 1) ? 32 : 8 * (r & 3)) : 36864 + g * 16;
    auto extra = [&](int k) {
        if (ekind < 5) {
            const LDSP char* sb = (const LDSP char*)lds + (k & 3) * P_SLOT;
            const bf16x8 x0 = *(const LDSP bf16x8*)(sb + exr), x1 = *(const LDSP bf16x8*)(sb + (exr ^ 64));
            bf16x8 w0, w1;
            if (ekind < 4) { w0 = cat8(tr_read(sb + ewr), tr_read(sb + ewr + 160)); w1 = cat8(tr_read(sb + ewr + 1280), tr_read(sb + ewr + 1440)); }
            else { w0 = *(const LDSP bf16x8*)(sb + ewr); w1 = *(const LDSP bf16x8*)(sb + ewr + 64); }
            if ((ekind >> 1) == 1) { eacc = MFMA16(x0, w0, eacc); eacc = MFMA16(x1, w1, eacc); }
            else { eacc = MFMA16(w0, x0, eacc); eacc = MFMA16(w1, x1, eacc); }
            asm volatile("" : "+v"(eacc));
        }
    };
#pragma unroll 1
    for (int k = 0; k < 8; ++k) {
        asm volatile("s_waitcnt vmcnt(5)\n\ts_barrier" ::: "memory");
        issue((k + 2) & 3);
        if (k < 7) extra(k);
    }
    const int gw = blockIdx.x * 8 + w, fid = gw < 1600 ? gw : 1599;
    const int fj = fid % 5, fs = (fid / 5) % 5, fg = (fid / 25) & 7, fn = fid / 200;
    const int br = fs < 4 ? fs >> 1 : g >> 1;
    const int row = fs < 4 ? (4 * fg + 2 * (fs & 1) + (g >> 1)) * 20 + 4 * (g & 1) : (4 * fg + 2 * (g & 1)) * 20 + 16;
    const char* p0 = (br ? wob : woa) + row * 1280 + (80 * fn + 16 * fj + r) * 2;
    const char* p1 = p0 + (fs < 4 ? 8 : 20) * 1280;
    unsigned e0, e1, e2, e3, e4, e5, e6, e7;
    asm volatile(
        "global_load_ushort %0, %8, off\n\tglobal_load_ushort %1, %8, off offset:1280\n\tglobal_load_ushort %2, %8, off offset:2560\n\tglobal_load_ushort %3, %8, off offset:3840\n\t"
        "global_load_ushort %4, %9, off\n\tglobal_load_ushort %5, %9, off offset:1280\n\tglobal_load_ushort %6, %9, off offset:2560\n\tglobal_load_ushort %7, %9, off offset:3840"
        : "=&v"(e0), "=&v"(e1), "=&v"(e2), "=&v"(e3), "=&v"(e4), "=&v"(e5), "=&v"(e6), "=&v"(e7) : "v"(p0), "v"(p1) : "memory");
    extra(7);
    asm volatile("s_waitcnt vmcnt(13)\n\ts_barrier" ::: "memory");
    extra(8);
    asm volatile("s_waitcnt vmcnt(8)\n\ts_barrier" ::: "memory");
    extra(9);
    asm volatile("s_waitcnt vmcnt(0)" : "+v"(e0), "+v"(e1), "+v"(e2), "+v"(e3), "+v"(e4), "+v"(e5), "+v"(e6), "+v"(e7) :: "memory");
    {
        char* kva = wsp + WS_KV + 32 * KV_HEAD + (mb * 2 + ehalf) * 5120;
        const u32x2 v = {pk_bf16(eacc[0], eacc[1]), pk_bf16(eacc[2], eacc[3])};
        const int t = (w >> 1) & 1, erow = 16 * (r >> 3) + 8 * (w & 1) + (r & 7);
        if (ekind == 0) st8(kva + t * 1024 + (g >> 1) * 512 + erow * 16 + (g & 1) * 8, v);
        else if (ekind == 1) { if (g == 0) st8(kva + 2048 + erow * 16 + t * 8, v); }
        else if (ekind < 4) { if (ekind == 2 || r < 4) st8(kva + 2560 + t * 1280 + g * 320 + ((ekind & 1) * 16 + r) * 16 + (w & 1) * 8, v); }
        else if (ekind == 4) { if (g == 0) ((float*)(wsp + WS_G))[mb * 128 + 16 * ets + r] = 1.0f / (1.0f + __expf(-eacc[0])); }
    }
    if (gw < 1600)
    st16(wsp + WS_WO + (size_t)fid * 1024 + l * 16, u32x4{e0 | (e1 << 16), e2 | (e3 << 16), e4 | (e5 << 16), e6 | (e7 << 16)});
    if (w == 0 && l == 0) *(unsigned*)(wsp + WS_CNT + blockIdx.x * CNT_STRIDE) = 0;
}

#define STR_(x) #x
#define STR(x) STR_(x)
#define PR4(b) "v[" STR(b) ":" STR(b) "+3]"
#define PM_F(j, i, X) "v_mfma_f32_16x16x32_bf16 " PR4(20 + 16 * j + 4 * i) ", " PR4(136 + 4 * j) ", " PR4(X + 4 * i) ", " PR4(20 + 16 * j + 4 * i) "\n\t"
#define PM_T(j, i, X) "v_mfma_f32_16x16x32_bf16 " PR4(20 + 16 * j + 4 * i) ", " PR4(X + 4 * i) ", " PR4(136 + 4 * j) ", " PR4(20 + 16 * j + 4 * i) "\n\t"
#define PTL(j, sl, ks) "ds_read_b64_tr_b16 v[136+4*" #j ":136+4*" #j "+1], %[aw" #j "] offset:" #sl "*40960+" #ks "*10240\n\t"
#define PTH(j, sl, ks) "ds_read_b64_tr_b16 v[136+4*" #j "+2:136+4*" #j "+3], %[aw" #j "] offset:" #sl "*40960+" #ks "*10240+1280\n\t"
#define PBX(X, i, sl, ks) "ds_read_b128 " PR4(X + 4 * i) ", %[ax" #ks "] offset:" #sl "*40960+" #i "*2048\n\t"
#define PTG(a) "v_add_u32 %[" #a "], %[tg], %[" #a "]\n\t"
#define PSTEP(M, X, Y, cs, ck, ns, nk, BAR, F0, F1, F2, F3) \
    M(0, 0, X) PTL(4, cs, ck) M(0, 1, X) PTH(4, cs, ck) M(0, 2, X) BAR PBX(Y, 0, ns, nk) M(0, 3, X) PBX(Y, 1, ns, nk) \
    M(1, 0, X) PTL(0, ns, nk) M(1, 1, X) PTH(0, ns, nk) M(1, 2, X) PBX(Y, 2, ns, nk) M(1, 3, X) "s_waitcnt lgkmcnt(5)\n\t" \
    M(2, 0, X) PTL(1, ns, nk) M(2, 1, X) PTH(1, ns, nk) M(2, 2, X) PBX(Y, 3, ns, nk) M(2, 3, X) F0 \
    M(3, 0, X) PTL(2, ns, nk) M(3, 1, X) PTH(2, ns, nk) M(3, 2, X) F1 M(3, 3, X) F2 \
    M(4, 0, X) PTL(3, ns, nk) M(4, 1, X) PTH(3, ns, nk) M(4, 2, X) F3 M(4, 3, X) "s_waitcnt lgkmcnt(4)\n\t"
#define CL10(a) "v" #a "0", "v" #a "1", "v" #a "2", "v" #a "3", "v" #a "4", "v" #a "5", "v" #a "6", "v" #a "7", "v" #a "8", "v" #a "9"
#define PLOOP(M) \
    asm volatile( \
        "s_mov_b32 %[cnt], 0\n\ts_mov_b32 %[tg], 81920\n\t" \
        "s_barrier\n\t" \
        PBX(104, 0, 0, 0) PBX(104, 1, 0, 0) PTL(0, 0, 0) PTH(0, 0, 0) PBX(104, 2, 0, 0) PTL(1, 0, 0) PTH(1, 0, 0) PBX(104, 3, 0, 0) \
        PTL(2, 0, 0) PTH(2, 0, 0) PTL(3, 0, 0) PTH(3, 0, 0) "s_waitcnt lgkmcnt(4)\n\t" \
        "1:\n\t" \
        PSTEP(M, 104, 120, 0, 0, 0, 1, "", "", "", "", "") \
        PSTEP(M, 120, 104, 0, 1, 1, 0, "s_barrier\n\t", PTG(ax0), "s_add_u32 %[cnt], %[cnt], 1\n\t", "", "") \
        PSTEP(M, 104, 120, 1, 0, 1, 1, "", PTG(aw0), PTG(aw1), PTG(aw2), PTG(ax1)) \
        PSTEP(M, 120, 104, 1, 1, 0, 0, "s_cmp_eq_u32 %[cnt], 5\n\ts_cbranch_scc1 2f\n\ts_barrier\n\t2:\n\t", PTG(aw3), PTG(aw4), "s_sub_u32 %[tg], 0, %[tg]\n\t", \
              "s_cmp_lg_u32 %[cnt], 5\n\t") \
        "s_cbranch_scc1 1b\n\t" \
        "s_waitcnt lgkmcnt(0)\n\ts_nop 7" \
        : "+{v[20:35]}"(acc[0]), "+{v[36:51]}"(acc[1]), "+{v[52:67]}"(acc[2]), "+{v[68:83]}"(acc[3]), "+{v[84:99]}"(acc[4]), \
          [ax0] "+v"(ax0), [ax1] "+v"(ax1), [aw0] "+v"(aw[0]), [aw1] "+v"(aw[1]), [aw2] "+v"(aw[2]), [aw3] "+v"(aw[3]), [aw4] "+v"(aw[4]), \
          [cnt] "=&s"(cnt), [tg] "=&s"(tg) \
        : \
        : "memory", "scc", "v104", "v105", "v106", "v107", "v108", "v109", CL10(11), CL10(12), CL10(13), CL10(14), "v150", "v151", "v152", "v153", "v154", "v155")

template <bool VT>
DI void proj_mma(unsigned lds0, f32x16 (&acc)[5]) {
    const int tid = threadIdx.x, l = tid & 63, w = __builtin_amdgcn_readfirstlane(tid >> 6) - 8;
    const int wm = w >> 1, wn = w & 1, r = l & 15, g = l >> 4;
    const int xsw = (g ^ ((r >> 1) & 7)) << 4;
    unsigned ax0 = lds0 + (64 * wm + r) * 128 + xsw, ax1 = lds0 + (64 * wm + r) * 128 + (xsw ^ 64);
    unsigned aw[5];
    const int q = r >> 2, p = r & 3;
#pragma unroll
    for (int j = 0; j < 5; ++j) {
        int col;
        if (j == 4) col = (4 * wn + p) * 20 + 16;
        else if (VT) col = (4 * wn + j) * 20 + 4 * p;
        else col = (4 * wn + (j & 2) + (p >> 1)) * 20 + 8 * (p & 1) + 4 * (j & 1);
        aw[j] = lds0 + 16384 + (8 * g + q) * 320 + ((col * 2) ^ ((g & 1) << 5));
    }
    unsigned cnt, tg;
    if constexpr (VT) PLOOP(PM_T); else PLOOP(PM_F);
}
#define ACC(i, j) f32x4{acc[j][4 * (i)], acc[j][4 * (i) + 1], acc[j][4 * (i) + 2], acc[j][4 * (i) + 3]}

DI u32x4 pk8(const f32x4& a, const f32x4& b, float sc) {
    return u32x4{pk_bf16(a[0] * sc, a[1] * sc), pk_bf16(a[2] * sc, a[3] * sc), pk_bf16(b[0] * sc, b[1] * sc), pk_bf16(b[2] * sc, b[3] * sc)};
}

#define CODE_PROJ "_Z6k_projPKcS0_S0_S0_S0_S0_S0_S0_Pc"
#define CODE_ATTN "_Z6k_attnPcS_"
#define CODE_BYTES 8192
__global__ __launch_bounds__(768, 3) void k_proj(const char* __restrict__ x, const char* __restrict__ wq, const char* __restrict__ wqkv,
                                                 const char* __restrict__ wk, const char* __restrict__ wv, const char* __restrict__ gw,
                                                 const char* __restrict__ woa, const char* __restrict__ wob, char* __restrict__ ws) {
    __shared__ __attribute__((aligned(16))) char lds[4 * P_SLOT];
    STAMP_DECL; STAMP(0);
    const int b = blockIdx.x, xcd = b & 7, ii = b >> 3;
    const int mb = (xcd & 1) * 8 + (ii & 7), type = xcd >> 1, nq = ii >> 3;
    const int tid = threadIdx.x, l = tid & 63, wv_ = __builtin_amdgcn_readfirstlane(tid >> 6);
    const int ldw2 = type == 0 ? 1280 : 3840;
    const int eidx = ((nq >> 1) | ((nq & 1) << 1)) * 4 + type, ehalf = eidx >= 5, ekind = eidx < 10 ? eidx - 5 * ehalf : 5;
    int rot = (ii & 7) + nq + type; rot = 2 * (rot >= 10 ? rot - 10 : (rot >= 5 ? rot - 5 : rot));
    const int ebytes = ekind < 4 ? 2560 : 128, nbytes = ebytes - (wv_ - 4) * 1024;
    const unsigned long long m5 = ~0ull >> (nbytes >= 1024 ? 0 : (nbytes > 0 ? 64 - (nbytes >> 4) : 63));
    const int sw = 64 * ldw2, se = wv_ < 4 ? sw : ebytes, wofs = (type > 1 ? (type - 1) * 1280 : 0) + nq * 320 + rot * sw;
    unsigned xo[2], wo[3];
    xo[0] = (mb * 128 + (tid >> 3)) * (DM * 2) + (((tid ^ (tid >> 4)) & 7) << 4) + rot * 128; xo[1] = xo[0] + 64 * DM * 2;
#pragma unroll
    for (int n = 0; n < 3; ++n) { const int cid = tid + 512 * n, k = (cid * 3277) >> 16, c = cid - 20 * k; wo[n] = k * ldw2 + ((c ^ (((k >> 3) & 1) << 1)) << 4) + wofs; }
    if (tid >= 256) wo[2] = l * 16 + (nbytes > 0 ? (wv_ - 4) * 1024 : 0) + rot * ebytes;
    asm volatile("" : "+v"(xo[0]), "+v"(xo[1]), "+v"(wo[0]), "+v"(wo[1]), "+v"(wo[2])
                 : "s"(x), "s"(wq), "s"(wqkv), "s"(wk), "s"(wv), "s"(gw), "s"(woa), "s"(wob), "s"(ws), "s"(m5), "s"(se), "s"(ekind));
    if (__builtin_expect(wv_ < 8, 1)) {
        const char* wsel = type == 0 ? wq : wqkv;
        proj_loader(lds, x, wsel, wv_ < 4 ? wsel : (ekind < 2 ? wk : (ekind < 4 ? wv : gw)), sw, se, m5, rot, ekind, ehalf, mb, woa, wob, ws, xo, wo);
        return;
    }
    const int w = wv_ - 8;
    if (w < 2) {
        const unsigned dp = (unsigned)(uintptr_t)lds + P_SLOT - 512, off = (ii * 3 + l) * 128; unsigned sv; unsigned long long pc;
        if (w == 0) asm volatile("s_getpc_b64 s[40:41]\n\ts_add_u32 s40, s40, " CODE_PROJ "@rel32@lo+4\n\ts_addc_u32 s41, s41, " CODE_PROJ "@rel32@hi+12" : "={s[40:41]}"(pc) :: "scc");
        else asm volatile("s_getpc_b64 s[40:41]\n\ts_add_u32 s40, s40, " CODE_ATTN "@rel32@lo+4\n\ts_addc_u32 s41, s41, " CODE_ATTN "@rel32@hi+12" : "={s[40:41]}"(pc) :: "scc");
        if (l < 3 && (w == 0 || off < CODE_BYTES))
            asm volatile("s_mov_b32 %0, m0\n\ts_mov_b32 m0, %1\n\ts_nop 0\n\tglobal_load_lds_dword %2, %3\n\ts_mov_b32 m0, %0" : "=&s"(sv) : "s"(dp), "v"(off), "s"(pc) : "memory");
    }
    const int wm = w >> 1, wn = w & 1, r = l & 15, g = l >> 4;
    f32x16 acc[5];
#pragma unroll
    for (int j = 0; j < 5; ++j)
#pragma unroll
        for (int i = 0; i < 16; ++i) acc[j][i] = 0.f;
    const unsigned lds0 = (unsigned)(uintptr_t)lds;
    const int chunk = mb * 2 + wm;
    if (type == 3) {
        proj_mma<true>(lds0, acc);
        STAMP(3);
        char* base = ws + WS_KV + (size_t)(nq * 8 + 4 * wn) * KV_HEAD + chunk * 5120 + 2560 + g * 320;
#pragma unroll
        for (int t = 0; t < 2; ++t) {
#pragma unroll
            for (int j = 0; j < 4; ++j) st16(base + (size_t)j * KV_HEAD + t * 1280 + r * 16, pk8(ACC(2 * t, j), ACC(2 * t + 1, j), 1.0f));
            st16(base + (size_t)(r >> 2) * KV_HEAD + t * 1280 + (16 + (r & 3)) * 16, pk8(ACC(2 * t, 4), ACC(2 * t + 1, 4), 1.0f));
        }
    } else {
        proj_mma<false>(lds0, acc);
        STAMP(3);
        const float sc = type == 2 ? 1.0f : 0.32259784f;
        const size_t hstride = type == 2 ? KV_HEAD : Q_HEAD;
        const int row0 = type == 2 ? 16 * (r >> 3) + (r & 7) : r, ustep = type == 2 ? 128 : 256;
        char* base = (type == 2 ? ws + WS_KV + chunk * 5120 : ws + WS_Q + (size_t)type * 32 * Q_HEAD + chunk * 2560) + (size_t)(nq * 8 + 4 * wn) * hstride + row0 * 16;
#pragma unroll
        for (int i = 0; i < 4; ++i)
#pragma unroll
            for (int jj = 0; jj < 2; ++jj)
                st16(base + (2 * jj + (g >> 1)) * hstride + (i >> 1) * 1024 + (g & 1) * 512 + (i & 1) * ustep, pk8(ACC(i, 2 * jj), ACC(i, 2 * jj + 1), sc));
#pragma unroll
        for (int u = 0; u < 2; ++u) st16(base + g * hstride + 2048 + u * ustep, pk8(ACC(u, 4), ACC(u + 2, 4), sc));
    }
    STAMP(4); STAMP_FLUSH(ws, 0);
}

#define S0R 56
#define S1R 88
#define NMR 120
#define VAR 152
#define VBR 160
#define KAR 168
#define KBR 172
#define WFR 176
#define E_(X, i) "v_exp_f32 v[" STR(X) "+" #i "], v[" STR(X) "+" #i "]\n\t"
#define C_(X, d, a, b) "v_cvt_pk_bf16_f32 v[" STR(X) "+" #d "], v[" STR(X) "+" #a "], v[" STR(X) "+" #b "]\n\t"
#define PV_(O, V, vo, X, po) "v_mfma_f32_32x32x16_bf16 %[" #O "], v[" STR(V) "+" #vo ":" STR(V) "+" #vo "+3], v[" STR(X) "+" #po ":" STR(X) "+" #po "+3], %[" #O "]\n\t"
#define M1_(Y, KB) "v_mfma_f32_32x32x4_2b_bf16 v[" STR(Y) ":" STR(Y) "+31], " KB ", %[q2], v[" STR(NMR) ":" STR(NMR) "+31]\n\t"
#define M2_(Y, o, Q) "v_mfma_f32_32x32x16_bf16 v[" STR(Y) "+" #o ":" STR(Y) "+" #o "+15], " KA_ ", %[" #Q "], v[" STR(Y) "+" #o ":" STR(Y) "+" #o "+15]\n\t"
#define KA_ "v[" STR(KAR) ":" STR(KAR) "+3]"
#define KB0_ "v[" STR(KBR) ":" STR(KBR) "+1]"
#define KB1_ "v[" STR(KBR) "+2:" STR(KBR) "+3]"
#define RDKA(off) "ds_read_b128 " KA_ ", %[ak1] offset:" STR(off) "\n\t"
#define RDKB(off) "ds_read_b128 v[" STR(KBR) ":" STR(KBR) "+3], %[ak2] offset:" STR(off) "\n\t"
#define RDV(V, off) "s_mov_b64 exec, %[vmask]\n\tds_read_b128 v[" STR(V) ":" STR(V) "+3], %[av] offset:" STR(off) "\n\t" \
                    "ds_read_b128 v[" STR(V) "+4:" STR(V) "+7], %[av] offset:" STR(off) "+640\n\ts_mov_b64 exec, -1\n\t"
#define MX3(d, a, i) "v_max3_f32 v[" STR(NMR) "+" #d "], v[" STR(a) "], v[" STR(S0R) "+" #i "], v[" STR(S0R) "+" #i "+1]\n\t"
#define PRIO(n) "s_setprio " #n "\n\t"
#define DMA4 "s_mov_b32 m0, %[dm]\n\ts_add_u32 %[dm], %[dm], 0x8000\n\tglobal_load_lds_dwordx4 %[dv], %[kvp]\n\tglobal_load_lds_dwordx4 %[dv], %[kvp] offset:1024\n\t" \
             "global_load_lds_dwordx4 %[dv], %[kvp] offset:2048\n\tglobal_load_lds_dwordx4 %[dv], %[kvp] offset:3072\n\tv_add_u32 %[dv], 0x8000, %[dv]\n\t"
#define WLD1(s, j) "global_load_dwordx4 v[" STR(WFR) "+20*" #s "+4*" #j ":" STR(WFR) "+20*" #s "+4*" #j "+3], %[wv], %[wp] offset:" #j "*1024-2048\n\t"
#define WLD(s) WLD1(s, 0) WLD1(s, 1) WLD1(s, 2) WLD1(s, 3) WLD1(s, 4) "v_add_u32 %[wv], 0x1400, %[wv]\n\ts_branch 1b\n\t"
#define TILE(X, Y, VC, VP, KAOFF, KB, KBRD, VOFF, PRE) \
    PRE E_(X, 0) E_(X, 1) \
    PV_(ob, VP, 4, Y, 24) \
    E_(X, 2) E_(X, 3) C_(X, 0, 0, 1) E_(X, 4) E_(X, 5) C_(X, 1, 2, 3) \
    "s_waitcnt lgkmcnt(0)\n\t" M1_(Y, KB) KBRD \
    E_(X, 6) E_(X, 7) C_(X, 2, 4, 5) E_(X, 8) C_(X, 3, 6, 7) E_(X, 9) E_(X, 10) E_(X, 11) C_(X, 8, 8, 9) \
    PV_(oa, VC, 0, X, 0) RDV(VP, VOFF) \
    E_(X, 12) C_(X, 9, 10, 11) E_(X, 13) E_(X, 14) E_(X, 15) C_(X, 10, 12, 13) E_(X, 16) C_(X, 11, 14, 15) E_(X, 17) \
    M2_(Y, 0, q1a) \
    E_(X, 18) E_(X, 19) C_(X, 16, 16, 17) E_(X, 20) E_(X, 21) C_(X, 17, 18, 19) \
    PV_(oa, VC, 4, X, 8) \
    E_(X, 22) E_(X, 23) C_(X, 18, 20, 21) E_(X, 24) C_(X, 19, 22, 23) E_(X, 25) \
    M2_(Y, 16, q1b) RDKA(KAOFF) \
    E_(X, 26) E_(X, 27) C_(X, 24, 24, 25) E_(X, 28) E_(X, 29) C_(X, 25, 26, 27) \
    PV_(ob, VC, 0, X, 16) \
    E_(X, 30) E_(X, 31) C_(X, 26, 28, 29) C_(X, 27, 30, 31)

DI float max_halves(float m) {
    auto rr = __builtin_amdgcn_permlane32_swap(__float_as_uint(m), __float_as_uint(m), false, false);
    return fmaxf(__uint_as_float(rr[0]), __uint_as_float(rr[1]));
}
DI void attn_fallback(const char* lds, int l, const char* qp, int tile, f32x16& o) {
    const int r32 = l & 31, h = l >> 5;
    const s16x4 z4 = {0, 0, 0, 0};
    const bf16x8 q1 = *(const bf16x8*)(qp + tile * 1024 + l * 16);
    const s16x4 q2 = *(const s16x4*)(qp + 2048 + r32 * 16 + tile * 8);
    const bf16x8 q2f = cat8(h == 0 ? q2 : z4, z4);
    const bf16x8 ones = {0x3f80, 0x3f80, 0x3f80, 0x3f80, 0x3f80, 0x3f80, 0x3f80, 0x3f80};
    float m = -INFINITY;
#pragma unroll
    for (int i = 0; i < 16; ++i) o[i] = 0.f;
#pragma unroll 1
    for (int t = 0; t < 64; ++t) {
        const LDSP char* cb = (const LDSP char*)lds + (t >> 1) * 5120;
        const bf16x8 k1 = *(const LDSP bf16x8*)(cb + (t & 1) * 1024 + l * 16);
        const s16x4 k2 = *(const LDSP s16x4*)(cb + 2048 + r32 * 16 + (t & 1) * 8);
        f32x16 s;
#pragma unroll
        for (int i = 0; i < 16; ++i) s[i] = 0.f;
        s = MFMA32(k1, q1, s);
        s = MFMA32(cat8(k2, z4), q2f, s);
        float tm = s[0];
#pragma unroll
        for (int i = 1; i < 16; ++i) tm = fmaxf(tm, s[i]);
        tm = max_halves(tm);
        const float mn = fmaxf(m, tm), alpha = __builtin_amdgcn_exp2f(m - mn);
        m = mn;
        u32x4 p0, p1;
#pragma unroll
        for (int i = 0; i < 4; ++i) {
            p0[i] = pk_bf16(__builtin_amdgcn_exp2f(s[2 * i] - m), __builtin_amdgcn_exp2f(s[2 * i + 1] - m));
            p1[i] = pk_bf16(__builtin_amdgcn_exp2f(s[8 + 2 * i] - m), __builtin_amdgcn_exp2f(s[9 + 2 * i] - m));
        }
#pragma unroll
        for (int i = 0; i < 16; ++i) o[i] *= alpha;
        const int dv = r32 < 20 ? r32 : 0;
        bf16x8 v0 = *(const LDSP bf16x8*)(cb + 2560 + (t & 1) * 1280 + h * 320 + dv * 16);
        bf16x8 v1 = *(const LDSP bf16x8*)(cb + 2560 + (t & 1) * 1280 + 640 + h * 320 + dv * 16);
        if (r32 >= 20) { v0 = ones; v1 = ones; }
        o = MFMA32(v0, __builtin_bit_cast(bf16x8, p0), o);
        o = MFMA32(v1, __builtin_bit_cast(bf16x8, p1), o);
    }
}

DI bool attn_ok(const f32x16& a, const f32x16& b) {
    f32x2 s = {0.f, 0.f};
#pragma unroll
    for (int i = 0; i < 12; i += 2) s += f32x2{a[i], a[i + 1]} + f32x2{b[i], b[i + 1]};
    return (fmaxf(a[12], b[12]) < 0x1p80f) & (fabsf(s[0] + s[1] + a[12] + b[12]) < 0x1p126f);
}
DI void attn_pack(const f32x16& oa, const f32x16& ob, float ga, float gb, u32x4& va, u32x4& vb, u32x2& vt) {
    const float fa = ga * __builtin_amdgcn_rcpf(oa[12]), fb = gb * __builtin_amdgcn_rcpf(ob[12]);
    va = u32x4{pk_bf16(oa[0] * fa, oa[1] * fa), pk_bf16(oa[2] * fa, oa[3] * fa), pk_bf16(oa[4] * fa, oa[5] * fa), pk_bf16(oa[6] * fa, oa[7] * fa)};
    vb = u32x4{pk_bf16(ob[0] * fb, ob[1] * fb), pk_bf16(ob[2] * fb, ob[3] * fb), pk_bf16(ob[4] * fb, ob[5] * fb), pk_bf16(ob[6] * fb, ob[7] * fb)};
    auto s0 = __builtin_amdgcn_permlane32_swap(pk_bf16(oa[8] * fa, oa[9] * fa), pk_bf16(ob[8] * fb, ob[9] * fb), false, false);
    auto s1 = __builtin_amdgcn_permlane32_swap(pk_bf16(oa[10] * fa, oa[11] * fa), pk_bf16(ob[10] * fb, ob[11] * fb), false, false);
    vt = u32x2{s0[0], s1[0]};
}

typedef __attribute__((ext_vector_type(16))) unsigned u32x16;
#define WSUB(v, i) __builtin_bit_cast(bf16x8, __builtin_shufflevector(v, v, 4 * (i), 4 * (i) + 1, 4 * (i) + 2, 4 * (i) + 3))

__global__ __launch_bounds__(512, 2) void k_attn(char* __restrict__ ws, char* __restrict__ out) {
    __shared__ __attribute__((aligned(16))) char lds[163840];
    STAMP_DECL; STAMP(0);
    const int b = blockIdx.x, xcd = b & 7, ii = b >> 3;
    const int pos = ii >> 2, head = (pos >> 2) * 32 + xcd * 4 + (pos & 3), slot = xcd * 8 + pos, qb = ii & 3;
    const int w = __builtin_amdgcn_readfirstlane(threadIdx.x >> 6);
    int l = threadIdx.x & 63, r32 = l & 31, h = l >> 5;
    const int qc = qb * 8 + w;
    const unsigned lds0 = (unsigned)(uintptr_t)lds;
    const unsigned qo = (unsigned)WS_Q + head * (unsigned)Q_HEAD + qc * 2560, kvo = (unsigned)WS_KV + (head < 32 ? 32 : head - 32) * (unsigned)KV_HEAD;
    bf16x8 q1a, q1b, k1f, k2f; s16x4 q2; float ga, gb;
    unsigned dv = kvo + w * 4096 + l * 16;
    {
        unsigned sv, dm = __builtin_amdgcn_readfirstlane(lds0 + w * 4096);
        asm volatile(
            "global_load_dwordx4 v[4:7], %[a0], %[kvp]\n\tglobal_load_dwordx4 v[8:11], %[a0], %[kvp] offset:1024\n\tglobal_load_dwordx2 v[12:13], %[a1], %[kvp] offset:2048\n\t"
            "global_load_dwordx4 " KA_ ", %[a2], %[kvp]\n\tglobal_load_dwordx4 v[" STR(KBR) ":" STR(KBR) "+3], %[a3], %[kvp] offset:2048\n\t"
            "global_load_dword v14, %[a4], %[kvp]\n\tglobal_load_dword v15, %[a4], %[kvp] offset:128\n\t"
            "s_mov_b32 %[sv], m0\n\t" DMA4 "s_mov_b32 m0, %[sv]"
            : "=&{v[4:7]}"(q1a), "=&{v[8:11]}"(q1b), "=&{v[12:13]}"(q2), "=&{v14}"(ga), "=&{v15}"(gb), "=&{v[168:171]}"(k1f), "=&{v[172:175]}"(k2f),
              [sv] "=&s"(sv), [dv] "+v"(dv), [dm] "+s"(dm)
            : [kvp] "s"(ws), [a0] "v"(qo + l * 16), [a1] "v"(qo + r32 * 16 + h * 8), [a2] "v"(kvo + l * 16), [a3] "v"(kvo + r32 * 16), [a4] "v"((unsigned)WS_G + qc * 256 + r32 * 4)
            : "memory", "scc");
    }
    const int mb = xcd * 4 + (ii & 3), nb = ii >> 2;
    const char* wp = ws + WS_WO + (size_t)(nb * 8 + w) * 25600 + 2048;
    const char* cp = ws + WS_CNT + (((mb >> 3) * 8 + w) * 8 + nb) * CNT_STRIDE;
    const char* sgp = ws + WS_CNT + (qb * 8 + xcd) * 8 * CNT_STRIDE;
    const char* a1 = ws + WS_O1 + (size_t)w * 8 * 65536 + mb * 2048;
    const char* a2 = ws + WS_O2 + (size_t)w * 4 * 32768 + mb * 1024;
    const char* op = out + (size_t)mb * 64 * 1280 + nb * 160;
    const char* po1 = ws + WS_O1 + (size_t)slot * 65536 + qc * 2048;
    const char* po2 = ws + WS_O2 + (size_t)(slot >> 1) * 32768 + qc * 1024 + (slot & 1) * 8;
    f32x16 oa, ob; u32x16 vini;
#pragma unroll
    for (int i = 0; i < 16; ++i) { oa[i] = 0.f; ob[i] = 0.f; vini[i] = (r32 & 27) == 24 ? 0x3f803f80u : 0u; }
    unsigned ak1 = lds0 + l * 16, ak2 = lds0 + 2048 + r32 * 16, av = lds0 + 2560 + h * 320 + r32 * 16, wv = l * 16;
    unsigned cnt, inc, svm, dm = __builtin_amdgcn_readfirstlane(lds0 + w * 4096 + 32768);
    u32x16 wt0, wt1, wt2, wt3, wt4;
    STAMP(2);
#define LSTAMP ""
    asm volatile(
        "s_mov_b32 %[sv], m0\n\t"
        ".irp n,24,25,26,27\n\tv_mov_b32 v[" STR(S1R) "+\\n], 0\n\t.endr\n\t"
        "s_mov_b32 %[cnt], 0\n\ts_mov_b32 %[inc], 20480\n\ts_mov_b64 vcc, -1\n\t"
        "s_waitcnt vmcnt(6)\n\t"
        "v_mfma_f32_32x32x4_2b_bf16 v[" STR(S0R) ":" STR(S0R) "+31], " KB0_ ", %[q2], 0\n\ts_nop 15\n\ts_nop 1\n\t"
        M2_(S0R, 0, q1a) M2_(S0R, 16, q1b)
        "s_nop 10\n\t"
        MX3(0, S0R + 0, 1) MX3(16, S0R + 16, 17) MX3(0, NMR, 3) MX3(16, NMR + 16, 19) MX3(0, NMR, 5) MX3(16, NMR + 16, 21) MX3(0, NMR, 7) MX3(16, NMR + 16, 23)
        MX3(0, NMR, 9) MX3(16, NMR + 16, 25) MX3(0, NMR, 11) MX3(16, NMR + 16, 27) MX3(0, NMR, 13) MX3(16, NMR + 16, 29) MX3(0, NMR, 14) MX3(16, NMR + 16, 30)
        "v_mov_b32 v[" STR(NMR) "+1], v[" STR(NMR) "]\n\tv_mov_b32 v[" STR(NMR) "+17], v[" STR(NMR) "+16]\n\ts_nop 1\n\t"
        "v_permlane32_swap_b32 v[" STR(NMR) "], v[" STR(NMR) "+1]\n\tv_permlane32_swap_b32 v[" STR(NMR) "+16], v[" STR(NMR) "+17]\n\ts_nop 1\n\t"
        "v_min_f32 v[" STR(NMR) "], -v[" STR(NMR) "], -v[" STR(NMR) "+1]\n\tv_min_f32 v[" STR(NMR) "+16], -v[" STR(NMR) "+16], -v[" STR(NMR) "+17]\n\t"
        "v_mov_b32 v[" STR(NMR) "+1], v[" STR(NMR) "]\n\tv_mov_b32 v[" STR(NMR) "+17], v[" STR(NMR) "+16]\n\t"
        ".irp n,0,2,4,6,8,10,12,14\n\tv_pk_add_f32 v[" STR(S0R) "+\\n:" STR(S0R) "+\\n+1], v[" STR(S0R) "+\\n:" STR(S0R) "+\\n+1], v[" STR(NMR) ":" STR(NMR) "+1]\n\t"
        "v_pk_add_f32 v[" STR(S0R) "+16+\\n:" STR(S0R) "+17+\\n], v[" STR(S0R) "+16+\\n:" STR(S0R) "+17+\\n], v[" STR(NMR) "+16:" STR(NMR) "+17]\n\t.endr\n\t"
        ".irp n,2,4,6,8,10,12,14\n\tv_mov_b64 v[" STR(NMR) "+\\n:" STR(NMR) "+\\n+1], v[" STR(NMR) ":" STR(NMR) "+1]\n\t"
        "v_mov_b64 v[" STR(NMR) "+16+\\n:" STR(NMR) "+17+\\n], v[" STR(NMR) "+16:" STR(NMR) "+17]\n\t.endr\n\t"
        "s_waitcnt vmcnt(0)\n\ts_barrier\n\t"
        RDKA(1024) RDV(VAR, 0)
        "1:\n\t"
        LSTAMP
        TILE(S0R, S1R, VAR, VBR, 5120, KB1_, RDKB(5120), 1280, PRIO(3) "s_cbranch_vccnz 11f\n\t21:\n\t")
        TILE(S1R, S0R, VBR, VAR, 5120 + 1024, KB0_, "", 5120, "s_cbranch_vccnz 12f\n\t22:\n\t")
        TILE(S0R, S1R, VAR, VBR, 10240, KB1_, RDKB(10240), 5120 + 1280, "s_cbranch_vccnz 13f\n\t23:\n\t")
        TILE(S1R, S0R, VBR, VAR, 10240 + 1024, KB0_, "", 10240, "s_cbranch_vccnz 14f\n\t24:\n\t")
        TILE(S0R, S1R, VAR, VBR, 15360, KB1_, RDKB(15360), 10240 + 1280, PRIO(2))
        TILE(S1R, S0R, VBR, VAR, 15360 + 1024, KB0_, "", 15360, "s_cmp_eq_u32 %[cnt], 7\n\ts_cselect_b32 %[inc], -143360, %[inc]\n\t")
        TILE(S0R, S1R, VAR, VBR, 0, KB1_, RDKB(0), 15360 + 1280, PRIO(1) "v_add_u32 %[ak1], %[inc], %[ak1]\n\tv_add_u32 %[ak2], %[inc], %[ak2]\n\t")
        TILE(S1R, S0R, VBR, VAR, 1024, KB0_, "", 0, PRIO(0) "v_add_u32 %[av], %[inc], %[av]\n\t")
        "s_add_u32 %[cnt], %[cnt], 1\n\t"
        "s_cmp_eq_u32 %[cnt], 1\n\ts_cbranch_scc1 2f\n\t"
        "s_cmp_eq_u32 %[cnt], 3\n\ts_cbranch_scc1 3f\n\t"
        "s_cmp_eq_u32 %[cnt], 4\n\ts_cbranch_scc1 5f\n\t"
        "s_cmp_eq_u32 %[cnt], 5\n\ts_cbranch_scc1 6f\n\t"
        "s_cmp_eq_u32 %[cnt], 6\n\ts_cbranch_scc1 8f\n\t"
        "s_cmp_lt_u32 %[cnt], 8\n\ts_cbranch_scc1 1b\n\t"
        "s_branch 4f\n\t"
        "11:\n\t" DMA4 "s_branch 21b\n\t"
        "12:\n\t" DMA4 "s_branch 22b\n\t"
        "13:\n\t" DMA4 "s_branch 23b\n\t"
        "14:\n\t" DMA4 "s_branch 24b\n\t"
        "2:\n\ts_mov_b64 vcc, 0\n\ts_waitcnt vmcnt(0)\n\ts_barrier\n\ts_branch 1b\n\t"
        "3:\n\t" WLD(0)
        "5:\n\t" WLD(1)
        "6:\n\t" WLD(2)
        "8:\n\tv_and_b32 %[dv], 0x3ff, %[wv]\n\tv_lshlrev_b32 %[dv], 3, %[dv]\n\tv_min_u32 %[dv], 0x1380, %[dv]\n\tv_add_u32 %[dv], 0x5000, %[dv]\n\t"
        "global_load_dword %[dv], %[dv], %[wp] offset:-2048\n\t" WLD(3)
        "4:\n\ts_nop 1\n\t"
        LSTAMP
        PV_(ob, VBR, 4, S1R, 24)
        "s_mov_b32 m0, %[sv]\n\ts_waitcnt vmcnt(0) lgkmcnt(0)\n\ts_nop 15\n\ts_nop 7"
        : [oa] "+{v[24:39]}"(oa), [ob] "+{v[40:55]}"(ob), [ak1] "+v"(ak1), [ak2] "+v"(ak2), [av] "+v"(av), [dv] "+v"(dv), [wv] "+v"(wv),
          "+{v[152:167]}"(vini), "+{v[168:171]}"(k1f), "+{v[172:175]}"(k2f),
          [q1a] "+{v[4:7]}"(q1a), [q1b] "+{v[8:11]}"(q1b), [q2] "+{v[12:13]}"(q2), "+{v14}"(ga), "+{v15}"(gb),
          "=&{v[176:191]}"(wt0), "=&{v[192:207]}"(wt1), "=&{v[208:223]}"(wt2), "=&{v[224:239]}"(wt3), "=&{v[240:255]}"(wt4),
          [cnt] "=&s"(cnt), [inc] "=&s"(inc), [sv] "=&s"(svm), [dm] "+s"(dm)
        : [vmask] "s"(0x000fffff000fffffull), [kvp] "s"(ws), [wp] "s"(wp),
          "s"(cp), "s"(sgp), "s"(a1), "s"(a2), "s"(op), "s"(po1), "s"(po2)
        : "memory", "scc", "vcc", "v56", "v57", "v58", "v59", CL10(6), CL10(7), CL10(8), CL10(9), CL10(10), CL10(11), CL10(12), CL10(13), CL10(14), "v150", "v151"
          );
    asm volatile("v_mbcnt_lo_u32_b32 %0, -1, 0\n\tv_mbcnt_hi_u32_b32 %0, -1, %0" : "=v"(l));
    r32 = l & 31; h = l >> 5;
    STAMP(3);
    bf16x8 wl[5], af[5][4]; unsigned seen;
    {
        if (head >= 32) { ga = 1.0f - ga; gb = 1.0f - gb; }
        u32x4 va, vb; u32x2 vt;
        attn_pack(oa, ob, ga, gb, va, vb, vt);
        if (__builtin_expect(!__all(attn_ok(oa, ob)), 0)) {
#pragma unroll 1
            for (int t = 0; t < 2; ++t) {
                f32x16 ro; u32x4 v0, v1; u32x2 v2;
                attn_fallback(lds, l, ws + qo, t, ro);
                attn_pack(ro, ro, t ? gb : ga, t ? gb : ga, v0, v1, v2);
                if (t == 0) va = v0; else vb = v0;
                if (h == t) vt = v2;
            }
        }
        asm volatile(
            "global_store_dwordx4 %[lo], %[va], %[o1] sc1\n\tglobal_store_dwordx4 %[lo], %[vb], %[o1] offset:1024 sc1\n\tglobal_store_dwordx2 %[lo], %[vt], %[o2] sc1\n\t"
            "s_nop 0"
            :: [lo] "v"(l * 16), [va] "v"(va), [vb] "v"(vb), [vt] "v"(vt), [o1] "s"(po1), [o2] "s"(po2)
            : "memory");
    }
    const int r = l & 15, g = l >> 4, r8 = l & 7, g2 = (l >> 3) & 3;
    unsigned o1 = (g >> 1) * 65536 + (g & 1) * 512 + r * 16, o2 = g * 32768 + r * 16;
    unsigned wbo = w * 20480 + (g * 16 + (r ^ ((g >> 1) << 3))) * 16;
    unsigned rbo = ((w >> 1) * 5 + h) * 1024 + (g2 * 16 + ((8 * (w & 1) + r8) ^ ((g2 >> 1) << 3))) * 16;
    unsigned opo = (8 * w + r8) * 1280 + h * 32 + g2 * 8, sgo = l * CNT_STRIDE;
    asm volatile("s_waitcnt vmcnt(0)\n\ts_barrier\n\t"
                 "s_cmp_lg_u32 %[w], %[sw]\n\ts_cbranch_scc1 1f\n\ts_mov_b64 exec, 0xff\n\tglobal_atomic_add %[so], %[one], %[sp]\n\ts_mov_b64 exec, -1\n\t1:"
                 : "+v"(o1), "+v"(o2), "+v"(wbo), "+v"(rbo), "+v"(opo)
                 : [w] "s"(w), [sw] "s"(xcd ^ 1), [so] "v"(sgo), [one] "v"(1), [sp] "s"(sgp) : "memory", "scc");
    STAMP(4);
    asm volatile(
        "global_load_dwordx4 %0, %6, %7 offset:-2048\n\tglobal_load_dwordx4 %1, %6, %7 offset:-1024\n\tglobal_load_dwordx4 %2, %6, %7\n\t"
        "global_load_dwordx4 %3, %6, %7 offset:1024\n\tglobal_load_dwordx4 %4, %6, %7 offset:2048\n\tglobal_load_dword %5, %8, %9 sc1\n\ts_waitcnt vmcnt(0)"
        : "=&v"(wl[0]), "=&v"(wl[1]), "=&v"(wl[2]), "=&v"(wl[3]), "=&v"(wl[4]), "=&v"(seen) : "v"(wv), "s"(wp), "v"(0), "s"(cp) : "memory");
#pragma unroll 1
    for (int spin = 0; spin < 400000 && __builtin_amdgcn_readfirstlane(seen) < 8; ++spin) {
        __builtin_amdgcn_s_sleep(POLL_SLEEP);
        asm volatile("global_load_dword %0, %1, %2 sc1\n\ts_waitcnt vmcnt(0)" : "=v"(seen) : "v"(0), "s"(cp) : "memory");
    }
    STAMP(5);
#define ALD(s, base, off, d1, d2) asm volatile( \
        "global_load_dwordx4 %0, %4, %5 sc1\n\tglobal_load_dwordx4 %1, %4, %5 offset:" #d1 " sc1\n\t" \
        "global_load_dwordx4 %2, %4, %5 offset:" #d2 " sc1\n\tglobal_load_dwordx4 %3, %4, %5 offset:" #d1 "+" #d2 " sc1" \
        : "=&v"(af[s][0]), "=&v"(af[s][1]), "=&v"(af[s][2]), "=&v"(af[s][3]) : "v"(off), "s"(base) : "memory")
#define AWAIT(s, N) asm volatile("s_waitcnt vmcnt(" STR(N) ")" : "+v"(af[s][0]), "+v"(af[s][1]), "+v"(af[s][2]), "+v"(af[s][3]) :: "memory")
#define OMM(s, w0, w1, w2, w3, w4) do { const bf16x8 wf_[5] = {w0, w1, w2, w3, w4}; \
        _Pragma("unroll") for (int j = 0; j < 5; ++j) _Pragma("unroll") for (int i = 0; i < 4; ++i) acc[i][j] = MFMA16(wf_[j], af[s][i], acc[i][j]); \
        __builtin_amdgcn_sched_barrier(0); } while (0)
    f32x4 acc[4][5];
#pragma unroll
    for (int i = 0; i < 4; ++i)
#pragma unroll
        for (int j = 0; j < 5; ++j) acc[i][j] = f32x4{0.f, 0.f, 0.f, 0.f};
    ALD(0, a1, o1, 256, 1024); ALD(1, a1 + 131072, o1, 256, 1024); ALD(2, a1 + 2 * 131072, o1, 256, 1024);
    AWAIT(0, 8); ALD(3, a1 + 3 * 131072, o1, 256, 1024); OMM(0, WSUB(wt0, 0), WSUB(wt0, 1), WSUB(wt0, 2), WSUB(wt0, 3), WSUB(wt1, 0));
    AWAIT(1, 8); ALD(4, a2, o2, 256, 512); OMM(1, WSUB(wt1, 1), WSUB(wt1, 2), WSUB(wt1, 3), WSUB(wt2, 0), WSUB(wt2, 1));
    AWAIT(2, 8); OMM(2, WSUB(wt2, 2), WSUB(wt2, 3), WSUB(wt3, 0), WSUB(wt3, 1), WSUB(wt3, 2));
    AWAIT(3, 4); OMM(3, WSUB(wt3, 3), WSUB(wt4, 0), WSUB(wt4, 1), WSUB(wt4, 2), WSUB(wt4, 3));
    AWAIT(4, 0);
#pragma unroll
    for (int j = 0; j < 6; ++j) {
        if (j < 5) {
#pragma unroll
            for (int i = 0; i < 4; ++i) acc[i][j] = MFMA16(wl[j], af[4][i], acc[i][j]);
        }
        if (j > 0) {
#pragma unroll
            for (int i = 0; i < 4; ++i) *(f32x4*)(lds + wbo + (i * 5 + j - 1) * 1024) = acc[i][j - 1];
        }
        __builtin_amdgcn_sched_barrier(0);
    }
    STAMP(6);
    {
        __syncthreads();
        f32x4 t[3][8];
#pragma unroll
        for (int u = 0; u < 8; ++u)
#pragma unroll
            for (int p = 0; p < 3; ++p) t[p][u] = *(const f32x4*)(lds + rbo + u * 20480 + (p < 2 ? p * 2048 : 4096 - h * 1024));
        __builtin_amdgcn_sched_barrier(0);
#pragma unroll
        for (int p = 0; p < 3; ++p) {
            const f32x4 z = ((t[p][0] + t[p][1]) + (t[p][2] + t[p][3])) + ((t[p][4] + t[p][5]) + (t[p][6] + t[p][7]));
            const u32x2 v = {pk_bf16(z[0], z[1]), pk_bf16(z[2], z[3])};
            if (p < 2 || h == 0) asm volatile("global_store_dwordx2 %0, %1, %2 offset:%3 " OUT_FLAGS :: "v"(opo), "v"(v), "s"(op), "i"(p * 64) : "memory");
        }
    }
    STAMP(7); STAMP_FLUSH(ws, 1);
}

extern "C" void kernel_launch(void* const* d_in, const int* in_sizes, int n_in, void* d_out, int out_size, void* d_ws, size_t ws_size,
                              hipStream_t stream) {
    const char* x = (const char*)d_in[0];
    const char* wq = (const char*)d_in[1];
    const char* wk = (const char*)d_in[2];
    const char* wv = (const char*)d_in[3];
    const char* woa = (const char*)d_in[4];
    const char* wqkv = (const char*)d_in[6];
    const char* wob = (const char*)d_in[7];
    const char* gw = (const char*)d_in[8];
    char* ws = (char*)d_ws;
    k_proj<<<256, 768, 0, stream>>>(x, wq, wqkv, wk, wv, gw, woa, wob, ws);
    k_attn<<<256, 512, 0, stream>>>(ws, (char*)d_out);
}
```

```cpp
#include <hip/hip_runtime.h>
#include <hip/hip_bf16.h>
#include <stdint.h>

typedef __attribute__((ext_vector_type(8))) short bf16x8;
typedef __attribute__((ext_vector_type(4))) short s16x4;
typedef __attribute__((ext_vector_type(4))) float f32x4;
typedef __attribute__((ext_vector_type(16))) float f32x16;
typedef __attribute__((ext_vector_type(2))) unsigned u32x2;
typedef __attribute__((ext_vector_type(4))) unsigned u32x4;
typedef __attribute__((ext_vector_type(2))) __bf16 bf16x2_t;
typedef __attribute__((ext_vector_type(2))) float f32x2;
#define LDSP __attribute__((address_space(3)))
#define DI __device__ __forceinline__

constexpr int DM = 640;
constexpr size_t Q_HEAD = 32 * 2560;
constexpr size_t KV_HEAD = 32 * 5120;
constexpr size_t WS_Q = 0;
constexpr size_t WS_KV = WS_Q + 64 * Q_HEAD;
constexpr size_t WS_G = WS_KV + 33 * KV_HEAD;
constexpr size_t WS_O1 = WS_G + 2048 * 4;
constexpr size_t WS_O2 = WS_O1 + 64 * 65536;
constexpr size_t WS_WO = WS_O2 + 32 * 2048 * 16;
constexpr size_t WS_CNT = WS_WO + 1600 * 1024;

DI unsigned pk_bf16(float a, float b) {
    f32x2 v = {a, b};
    return __builtin_bit_cast(unsigned, __builtin_convertvector(v, bf16x2_t));
}
DI void st16(char* p, u32x4 v) { asm volatile("global_store_dwordx4 %0, %1, off sc1\n\ts_nop 1" :: "v"(p), "v"(v) : "memory"); }
DI void st8(char* p, u32x2 v) { asm volatile("global_store_dwordx2 %0, %1, off sc1" :: "v"(p), "v"(v) : "memory"); }
DI s16x4 tr_read(const LDSP char* p) {
    typedef short v4i16_t __attribute__((ext_vector_type(4)));
    return __builtin_amdgcn_ds_read_tr16_b64_v4i16((LDSP v4i16_t*)p);
}
DI bf16x8 cat8(s16x4 lo, s16x4 hi) { return __builtin_shufflevector(lo, hi, 0, 1, 2, 3, 4, 5, 6, 7); }
#define MFMA16(a, b, c) __builtin_amdgcn_mfma_f32_16x16x32_bf16((a), (b), (c), 0, 0, 0)
#define MFMA32(a, b, c) __builtin_amdgcn_mfma_f32_32x32x16_bf16((a), (b), (c), 0, 0, 0)

constexpr int P_SLOT = 40960;

DI void proj_loader(char* lds, const char* xs, const char* ws, const char* es, int sw, int se, unsigned long long m5, int st, int ekind, int ehalf, int mb,
                    const char* woa, const char* wob, char* wsp, const unsigned (&xo)[2], const unsigned (&wo)[3]) {
    const int l = threadIdx.x & 63, w = __builtin_amdgcn_readfirstlane(threadIdx.x >> 6), r = l & 15, g = l >> 4;
    const unsigned dw = __builtin_amdgcn_readfirstlane((unsigned)(uintptr_t)lds + w * 1024);
    auto issue = [&](int slot) {
        const unsigned d0 = dw + slot * P_SLOT;
        unsigned sv;
        asm volatile(
            "s_mov_b32 %[sv], m0\n\ts_mov_b32 m0, %[d0]\n\ts_nop 0\n\t"
            "global_load_lds_dwordx4 %[x0], %[xs]\n\ts_add_u32 m0, m0, 8192\n\ts_nop 0\n\t"
            "global_load_lds_dwordx4 %[x1], %[xs]\n\ts_add_u32 m0, m0, 8192\n\ts_nop 0\n\t"
            "global_load_lds_dwordx4 %[w0], %[ws]\n\ts_add_u32 m0, m0, 8192\n\ts_nop 0\n\t"
            "global_load_lds_dwordx4 %[w1], %[ws]\n\ts_add_u32 m0, m0, 8192\n\ts_mov_b64 exec, %[m5]\n\t"
            "global_load_lds_dwordx4 %[w2], %[es]\n\ts_mov_b64 exec, -1\n\ts_mov_b32 m0, %[sv]"
            : [sv] "=&s"(sv)
            : [d0] "s"(d0), [m5] "s"(m5), [xs] "s"(xs), [ws] "s"(ws), [es] "s"(es),
              [x0] "v"(xo[0]), [x1] "v"(xo[1]), [w0] "v"(wo[0]), [w1] "v"(wo[1]), [w2] "v"(wo[2])
            : "memory", "scc");
        const int d = st == 9 ? -9 : 1;
        st += d; xs += d * 128; ws += d * sw; es += d * se;
    };
    issue(0); issue(1);
    __builtin_amdgcn_s_setprio(3);
    f32x4 eacc = {0.f, 0.f, 0.f, 0.f};
    const int ets = 4 * ehalf + w;
    if (w >= 4) ekind = 5;
    const int exr = (16 * ets + r) * 128 + ((g ^ ((r >> 1) & 7)) << 4);
    const int ewr = ekind < 4 ? 36864 + (8 * g + (r >> 2)) * 40 + ((ekind & 1) ? 32 : 8 * (r & 3)) : 36864 + g * 16;
    auto extra = [&](int k) {
        if (ekind < 5) {
            const LDSP char* sb = (const LDSP char*)lds + (k & 3) * P_SLOT;
            const bf16x8 x0 = *(const LDSP bf16x8*)(sb + exr), x1 = *(const LDSP bf16x8*)(sb + (exr ^ 64));
            bf16x8 w0, w1;
            if (ekind < 4) { w0 = cat8(tr_read(sb + ewr), tr_read(sb + ewr + 160)); w1 = cat8(tr_read(sb + ewr + 1280), tr_read(sb + ewr + 1440)); }
            else { w0 = *(const LDSP bf16x8*)(sb + ewr); w1 = *(const LDSP bf16x8*)(sb + ewr + 64); }
            if ((ekind >> 1) == 1) { eacc = MFMA16(x0, w0, eacc); eacc = MFMA16(x1, w1, eacc); }
            else { eacc = MFMA16(w0, x0, eacc); eacc = MFMA16(w1, x1, eacc); }
            asm volatile("" : "+v"(eacc));
        }
    };
    const int gw = blockIdx.x * 8 + w, fid = gw < 1600 ? gw : 1599;
    const int fj = fid % 5, fs = (fid / 5) % 5, fg = (fid / 25) & 7, fn = fid / 200;
    const int br = fs < 4 ? fs >> 1 : g >> 1;
    const int row = fs < 4 ? (4 * fg + 2 * (fs & 1) + (g >> 1)) * 20 + 4 * (g & 1) : (4 * fg + 2 * (g & 1)) * 20 + 16;
    const char* p0 = (br ? wob : woa) + row * 1280 + (80 * fn + 16 * fj + r) * 2;
    const char* p1 = p0 + (fs < 4 ? 8 : 20) * 1280;
    asm volatile("" : "+v"(p0), "+v"(p1));
#pragma unroll 1
    for (int k = 0; k < 8; ++k) {
        asm volatile("s_waitcnt vmcnt(5)\n\ts_barrier" ::: "memory");
        issue((k + 2) & 3);
        if (k < 7) extra(k);
    }
    unsigned e0, e1, e2, e3, e4, e5, e6, e7;
    asm volatile(
        "global_load_ushort %0, %8, off\n\tglobal_load_ushort %1, %8, off offset:1280\n\tglobal_load_ushort %2, %8, off offset:2560\n\tglobal_load_ushort %3, %8, off offset:3840\n\t"
        "global_load_ushort %4, %9, off\n\tglobal_load_ushort %5, %9, off offset:1280\n\tglobal_load_ushort %6, %9, off offset:2560\n\tglobal_load_ushort %7, %9, off offset:3840"
        : "=&v"(e0), "=&v"(e1), "=&v"(e2), "=&v"(e3), "=&v"(e4), "=&v"(e5), "=&v"(e6), "=&v"(e7) : "v"(p0), "v"(p1) : "memory");
    extra(7);
    asm volatile("s_waitcnt vmcnt(13)\n\ts_barrier" ::: "memory");
    extra(8);
    asm volatile("s_waitcnt vmcnt(8)\n\ts_barrier" ::: "memory");
    extra(9);
    asm volatile("s_waitcnt vmcnt(0)" : "+v"(e0), "+v"(e1), "+v"(e2), "+v"(e3), "+v"(e4), "+v"(e5), "+v"(e6), "+v"(e7) :: "memory");
    {
        char* kva = wsp + WS_KV + 32 * KV_HEAD + (mb * 2 + ehalf) * 5120;
        const u32x2 v = {pk_bf16(eacc[0], eacc[1]), pk_bf16(eacc[2], eacc[3])};
        const int t = (w >> 1) & 1, erow = 16 * (r >> 3) + 8 * (w & 1) + (r & 7);
        if (ekind == 0) st8(kva + t * 1024 + (g >> 1) * 512 + erow * 16 + (g & 1) * 8, v);
        else if (ekind == 1) { if (g == 0) st8(kva + 2048 + erow * 16 + t * 8, v); }
        else if (ekind < 4) { if (ekind == 2 || r < 4) st8(kva + 2560 + t * 1280 + g * 320 + ((ekind & 1) * 16 + r) * 16 + (w & 1) * 8, v); }
        else if (ekind == 4) { if (g == 0) ((float*)(wsp + WS_G))[mb * 128 + 16 * ets + r] = 1.0f / (1.0f + __expf(-eacc[0])); }
    }
    if (gw < 1600) st16(wsp + WS_WO + (size_t)fid * 1024 + l * 16, u32x4{e0 | (e1 << 16), e2 | (e3 << 16), e4 | (e5 << 16), e6 | (e7 << 16)});
    if (w == 0 && l == 0) *(unsigned*)(wsp + WS_CNT + blockIdx.x * 128) = 0;
}

#define STR_(x) #x
#define STR(x) STR_(x)
#define PR4(b) "v[" STR(b) ":" STR(b) "+3]"
#define PM_F(j, i, X) "v_mfma_f32_16x16x32_bf16 " PR4(20 + 16 * j + 4 * i) ", " PR4(136 + 4 * j) ", " PR4(X + 4 * i) ", " PR4(20 + 16 * j + 4 * i) "\n\t"
#define PM_T(j, i, X) "v_mfma_f32_16x16x32_bf16 " PR4(20 + 16 * j + 4 * i) ", " PR4(X + 4 * i) ", " PR4(136 + 4 * j) ", " PR4(20 + 16 * j + 4 * i) "\n\t"
#define PTL(j, sl, ks) "ds_read_b64_tr_b16 v[136+4*" #j ":136+4*" #j "+1], %[aw" #j "] offset:" #sl "*40960+" #ks "*10240\n\t"
#define PTH(j, sl, ks) "ds_read_b64_tr_b16 v[136+4*" #j "+2:136+4*" #j "+3], %[aw" #j "] offset:" #sl "*40960+" #ks "*10240+1280\n\t"
#define PBX(X, i, sl, ks) "ds_read_b128 " PR4(X + 4 * i) ", %[ax" #ks "] offset:" #sl "*40960+" #i "*2048\n\t"
#define PTG(a) "v_add_u32 %[" #a "], %[tg], %[" #a "]\n\t"
#define PSTEP(M, X, Y, cs, ck, ns, nk, BAR, F0, F1, F2, F3) \
    M(0, 0, X) PTL(4, cs, ck) M(0, 1, X) PTH(4, cs, ck) M(0, 2, X) BAR PBX(Y, 0, ns, nk) M(0, 3, X) PBX(Y, 1, ns, nk) \
    M(1, 0, X) PTL(0, ns, nk) M(1, 1, X) PTH(0, ns, nk) M(1, 2, X) PBX(Y, 2, ns, nk) M(1, 3, X) "s_waitcnt lgkmcnt(5)\n\t" \
    M(2, 0, X) PTL(1, ns, nk) M(2, 1, X) PTH(1, ns, nk) M(2, 2, X) PBX(Y, 3, ns, nk) M(2, 3, X) F0 \
    M(3, 0, X) PTL(2, ns, nk) M(3, 1, X) PTH(2, ns, nk) M(3, 2, X) F1 M(3, 3, X) F2 \
    M(4, 0, X) PTL(3, ns, nk) M(4, 1, X) PTH(3, ns, nk) M(4, 2, X) F3 M(4, 3, X) "s_waitcnt lgkmcnt(4)\n\t"
#define CL10(a) "v" #a "0", "v" #a "1", "v" #a "2", "v" #a "3", "v" #a "4", "v" #a "5", "v" #a "6", "v" #a "7", "v" #a "8", "v" #a "9"
#define PLOOP(M) \
    asm volatile( \
        "s_mov_b32 %[cnt], 0\n\ts_mov_b32 %[tg], 81920\n\t" \
        "s_barrier\n\t" \
        PBX(104, 0, 0, 0) PBX(104, 1, 0, 0) PTL(0, 0, 0) PTH(0, 0, 0) PBX(104, 2, 0, 0) PTL(1, 0, 0) PTH(1, 0, 0) PBX(104, 3, 0, 0) \
        PTL(2, 0, 0) PTH(2, 0, 0) PTL(3, 0, 0) PTH(3, 0, 0) "s_waitcnt lgkmcnt(4)\n\t" \
        "1:\n\t" \
        PSTEP(M, 104, 120, 0, 0, 0, 1, "", "", "", "", "") \
        PSTEP(M, 120, 104, 0, 1, 1, 0, "s_barrier\n\t", PTG(ax0), "s_add_u32 %[cnt], %[cnt], 1\n\t", "", "") \
        PSTEP(M, 104, 120, 1, 0, 1, 1, "", PTG(aw0), PTG(aw1), PTG(aw2), PTG(ax1)) \
        PSTEP(M, 120, 104, 1, 1, 0, 0, "s_cmp_eq_u32 %[cnt], 5\n\ts_cbranch_scc1 2f\n\ts_barrier\n\t2:\n\t", PTG(aw3), PTG(aw4), "s_sub_u32 %[tg], 0, %[tg]\n\t", \
              "s_cmp_lg_u32 %[cnt], 5\n\t") \
        "s_cbranch_scc1 1b\n\t" \
        "s_waitcnt lgkmcnt(0)\n\ts_nop 7" \
        : "+{v[20:35]}"(acc[0]), "+{v[36:51]}"(acc[1]), "+{v[52:67]}"(acc[2]), "+{v[68:83]}"(acc[3]), "+{v[84:99]}"(acc[4]), \
          [ax0] "+v"(ax0), [ax1] "+v"(ax1), [aw0] "+v"(aw[0]), [aw1] "+v"(aw[1]), [aw2] "+v"(aw[2]), [aw3] "+v"(aw[3]), [aw4] "+v"(aw[4]), \
          [cnt] "=&s"(cnt), [tg] "=&s"(tg) \
        : \
        : "memory", "scc", "v104", "v105", "v106", "v107", "v108", "v109", CL10(11), CL10(12), CL10(13), CL10(14), "v150", "v151", "v152", "v153", "v154", "v155")

template <bool VT>
DI void proj_mma(unsigned lds0, f32x16 (&acc)[5]) {
    const int tid = threadIdx.x, l = tid & 63, w = __builtin_amdgcn_readfirstlane(tid >> 6) - 8;
    const int wm = w >> 1, wn = w & 1, r = l & 15, g = l >> 4;
    const int xsw = (g ^ ((r >> 1) & 7)) << 4;
    unsigned ax0 = lds0 + (64 * wm + r) * 128 + xsw, ax1 = lds0 + (64 * wm + r) * 128 + (xsw ^ 64);
    unsigned aw[5];
    const int q = r >> 2, p = r & 3;
#pragma unroll
    for (int j = 0; j < 5; ++j) {
        int col;
        if (j == 4) col = (4 * wn + p) * 20 + 16;
        else if (VT) col = (4 * wn + j) * 20 + 4 * p;
        else col = (4 * wn + (j & 2) + (p >> 1)) * 20 + 8 * (p & 1) + 4 * (j & 1);
        aw[j] = lds0 + 16384 + (8 * g + q) * 320 + ((col * 2) ^ ((g & 1) << 5));
    }
    unsigned cnt, tg;
    if constexpr (VT) PLOOP(PM_T); else PLOOP(PM_F);
}
#define ACC(i, j) f32x4{acc[j][4 * (i)], acc[j][4 * (i) + 1], acc[j][4 * (i) + 2], acc[j][4 * (i) + 3]}

DI u32x4 pk8(const f32x4& a, const f32x4& b, float sc) {
    return u32x4{pk_bf16(a[0] * sc, a[1] * sc), pk_bf16(a[2] * sc, a[3] * sc), pk_bf16(b[0] * sc, b[1] * sc), pk_bf16(b[2] * sc, b[3] * sc)};
}

#define CODE_PROJ "_Z6k_projPKcS0_S0_S0_S0_S0_S0_S0_Pc"
#define CODE_ATTN "_Z6k_attnPcS_"
#define CODE_BYTES 8448
__global__ __launch_bounds__(768, 3) void k_proj(const char* __restrict__ x, const char* __restrict__ wq, const char* __restrict__ wqkv,
                                                 const char* __restrict__ wk, const char* __restrict__ wv, const char* __restrict__ gw,
                                                 const char* __restrict__ woa, const char* __restrict__ wob, char* __restrict__ ws) {
    __shared__ __attribute__((aligned(16))) char lds[4 * P_SLOT];
    const int b = blockIdx.x, xcd = b & 7, ii = b >> 3;
    const int mb = (xcd & 1) * 8 + (ii & 7), type = xcd >> 1, nq = ii >> 3;
    const int tid = threadIdx.x, l = tid & 63, wv_ = __builtin_amdgcn_readfirstlane(tid >> 6);
    const int ldw2 = type == 0 ? 1280 : 3840;
    const int eidx = ((nq >> 1) | ((nq & 1) << 1)) * 4 + type, ehalf = eidx >= 5, ekind = eidx < 10 ? eidx - 5 * ehalf : 5;
    int rot = (ii & 7) + nq + type; rot = 2 * (rot >= 10 ? rot - 10 : (rot >= 5 ? rot - 5 : rot));
    const int ebytes = ekind < 4 ? 2560 : 128, nbytes = ebytes - (wv_ - 4) * 1024;
    const unsigned long long m5 = ~0ull >> (nbytes >= 1024 ? 0 : (nbytes > 0 ? 64 - (nbytes >> 4) : 63));
    const int sw = 64 * ldw2, se = wv_ < 4 ? sw : ebytes, wofs = (type > 1 ? (type - 1) * 1280 : 0) + nq * 320 + rot * sw;
    unsigned xo[2], wo[3];
    xo[0] = (mb * 128 + (tid >> 3)) * (DM * 2) + (((tid ^ (tid >> 4)) & 7) << 4) + rot * 128; xo[1] = xo[0] + 64 * DM * 2;
#pragma unroll
    for (int n = 0; n < 3; ++n) { const int cid = tid + 512 * n, k = (cid * 3277) >> 16, c = cid - 20 * k; wo[n] = k * ldw2 + ((c ^ (((k >> 3) & 1) << 1)) << 4) + wofs; }
    if (tid >= 256) wo[2] = l * 16 + (nbytes > 0 ? (wv_ - 4) * 1024 : 0) + rot * ebytes;
    asm volatile("" : "+v"(xo[0]), "+v"(xo[1]), "+v"(wo[0]), "+v"(wo[1]), "+v"(wo[2])
                 : "s"(x), "s"(wq), "s"(wqkv), "s"(wk), "s"(wv), "s"(gw), "s"(woa), "s"(wob), "s"(ws), "s"(m5), "s"(se), "s"(ekind));
    if (__builtin_expect(wv_ < 8, 1)) {
        const char* wsel = type == 0 ? wq : wqkv;
        proj_loader(lds, x, wsel, wv_ < 4 ? wsel : (ekind < 2 ? wk : (ekind < 4 ? wv : gw)), sw, se, m5, rot, ekind, ehalf, mb, woa, wob, ws, xo, wo);
        return;
    }
    const int w = wv_ - 8;
    if (w < 2) {
        const unsigned dp = (unsigned)(uintptr_t)lds + P_SLOT - 512, off = (ii * 3 + l) * 128; unsigned sv; unsigned long long pc;
        if (w == 0) asm volatile("s_getpc_b64 s[40:41]\n\ts_add_u32 s40, s40, " CODE_PROJ "@rel32@lo+4\n\ts_addc_u32 s41, s41, " CODE_PROJ "@rel32@hi+12" : "={s[40:41]}"(pc) :: "scc");
        else asm volatile("s_getpc_b64 s[40:41]\n\ts_add_u32 s40, s40, " CODE_ATTN "@rel32@lo+4\n\ts_addc_u32 s41, s41, " CODE_ATTN "@rel32@hi+12" : "={s[40:41]}"(pc) :: "scc");
        if (l < 3 && off < CODE_BYTES)
            asm volatile("s_mov_b32 %0, m0\n\ts_mov_b32 m0, %1\n\ts_nop 0\n\tglobal_load_lds_dword %2, %3\n\ts_mov_b32 m0, %0" : "=&s"(sv) : "s"(dp), "v"(off), "s"(pc) : "memory");
    }
    const int wm = w >> 1, wn = w & 1, r = l & 15, g = l >> 4;
    f32x16 acc[5];
#pragma unroll
    for (int j = 0; j < 5; ++j)
#pragma unroll
        for (int i = 0; i < 16; ++i) acc[j][i] = 0.f;
    const unsigned lds0 = (unsigned)(uintptr_t)lds;
    const int chunk = mb * 2 + wm;
#define ST16(voff, sbase, imm, val) asm volatile("global_store_dwordx4 %0, %1, %2 offset:%3 sc1\n\ts_nop 1" :: "v"(voff), "v"(val), "s"(sbase), "i"(imm) : "memory")
    if (type == 3) {
        const char* sb = ws + WS_KV + (size_t)(nq * 8 + 4 * wn) * KV_HEAD + chunk * 5120 + 2560;
        unsigned v1 = g * 320 + r * 16, v2 = (r >> 2) * (unsigned)KV_HEAD + g * 320 + (16 + (r & 3)) * 16;
        asm volatile("" : "+v"(v1), "+v"(v2) : "s"(sb), "s"(sb + KV_HEAD), "s"(sb + 2 * KV_HEAD), "s"(sb + 3 * KV_HEAD));
        proj_mma<true>(lds0, acc);
#pragma unroll
        for (int j = 0; j < 4; ++j) {
            ST16(v1, sb + j * KV_HEAD, 0, pk8(ACC(0, j), ACC(1, j), 1.0f));
            ST16(v1, sb + j * KV_HEAD, 1280, pk8(ACC(2, j), ACC(3, j), 1.0f));
        }
        ST16(v2, sb, 0, pk8(ACC(0, 4), ACC(1, 4), 1.0f));
        ST16(v2, sb, 1280, pk8(ACC(2, 4), ACC(3, 4), 1.0f));
    } else {
        const float sc = type == 2 ? 1.0f : 0.32259784f;
        const unsigned hstride = type == 2 ? KV_HEAD : Q_HEAD;
        const char* sb = ws + (type == 2 ? WS_KV + chunk * 5120 : WS_Q + type * 32 * Q_HEAD + chunk * 2560) + (nq * 8 + 4 * wn) * hstride;
        const unsigned row0 = (16 * (r >> 3) + (r & 7)) * 16;
        unsigned v1 = (g >> 1) * hstride + (g & 1) * 512 + row0, v2 = g * hstride + row0;
        asm volatile("" : "+v"(v1), "+v"(v2) : "s"(sb), "s"(sb + 2 * hstride));
        proj_mma<false>(lds0, acc);
#pragma unroll
        for (int jj = 0; jj < 2; ++jj)
#pragma unroll
            for (int i = 0; i < 4; ++i) ST16(v1, sb + jj * 2 * hstride, (i >> 1) * 1024 + (i & 1) * 128, pk8(ACC(i, 2 * jj), ACC(i, 2 * jj + 1), sc));
        ST16(v2, sb, 2048, pk8(ACC(0, 4), ACC(2, 4), sc));
        ST16(v2, sb, 2048 + 128, pk8(ACC(1, 4), ACC(3, 4), sc));
    }
}

#define S0R 56
#define S1R 88
#define NMR 120
#define VAR 152
#define VBR 160
#define KAR 168
#define KBR 172
#define WFR 176
#define E_(X, i) "v_exp_f32 v[" STR(X) "+" #i "], v[" STR(X) "+" #i "]\n\t"
#define C_(X, d, a, b) "v_cvt_pk_bf16_f32 v[" STR(X) "+" #d "], v[" STR(X) "+" #a "], v[" STR(X) "+" #b "]\n\t"
#define PV_(O, V, vo, X, po) "v_mfma_f32_32x32x16_bf16 %[" #O "], v[" STR(V) "+" #vo ":" STR(V) "+" #vo "+3], v[" STR(X) "+" #po ":" STR(X) "+" #po "+3], %[" #O "]\n\t"
#define M1_(Y, KB) "v_mfma_f32_32x32x4_2b_bf16 v[" STR(Y) ":" STR(Y) "+31], " KB ", %[q2], v[" STR(NMR) ":" STR(NMR) "+31]\n\t"
#define M2_(Y, o, Q) "v_mfma_f32_32x32x16_bf16 v[" STR(Y) "+" #o ":" STR(Y) "+" #o "+15], " KA_ ", %[" #Q "], v[" STR(Y) "+" #o ":" STR(Y) "+" #o "+15]\n\t"
#define KA_ "v[" STR(KAR) ":" STR(KAR) "+3]"
#define KB0_ "v[" STR(KBR) ":" STR(KBR) "+1]"
#define KB1_ "v[" STR(KBR) "+2:" STR(KBR) "+3]"
#define RDKA(off) "ds_read_b128 " KA_ ", %[ak1] offset:" STR(off) "\n\t"
#define RDKB(off) "ds_read_b128 v[" STR(KBR) ":" STR(KBR) "+3], %[ak2] offset:" STR(off) "\n\t"
#define RDV(V, off) "s_mov_b64 exec, %[vmask]\n\tds_read_b128 v[" STR(V) ":" STR(V) "+3], %[av] offset:" STR(off) "\n\t" \
                    "ds_read_b128 v[" STR(V) "+4:" STR(V) "+7], %[av] offset:" STR(off) "+640\n\ts_mov_b64 exec, -1\n\t"
#define MX3(d, a, i) "v_max3_f32 v[" STR(NMR) "+" #d "], v[" STR(a) "], v[" STR(S0R) "+" #i "], v[" STR(S0R) "+" #i "+1]\n\t"
#define PRIO(n) "s_setprio " #n "\n\t"
#define DMA4 "s_mov_b32 m0, %[dm]\n\ts_add_u32 %[dm], %[dm], 0x8000\n\tglobal_load_lds_dwordx4 %[dv], %[kvp]\n\tglobal_load_lds_dwordx4 %[dv], %[kvp] offset:1024\n\t" \
             "global_load_lds_dwordx4 %[dv], %[kvp] offset:2048\n\tglobal_load_lds_dwordx4 %[dv], %[kvp] offset:3072\n\tv_add_u32 %[dv], 0x8000, %[dv]\n\t"
#define WLD1(s, j) "global_load_dwordx4 v[" STR(WFR) "+20*" #s "+4*" #j ":" STR(WFR) "+20*" #s "+4*" #j "+3], %[wv], %[wp] offset:" #j "*1024-2048\n\t"
#define WLD(s) WLD1(s, 0) WLD1(s, 1) WLD1(s, 2) WLD1(s, 3) WLD1(s, 4) "v_add_u32 %[wv], 0x1400, %[wv]\n\ts_branch 1b\n\t"
#define TILE(X, Y, VC, VP, KAOFF, KB, KBRD, VOFF, PRE) \
    PRE E_(X, 0) E_(X, 1) \
    PV_(ob, VP, 4, Y, 24) \
    E_(X, 2) E_(X, 3) C_(X, 0, 0, 1) E_(X, 4) E_(X, 5) C_(X, 1, 2, 3) \
    "s_waitcnt lgkmcnt(0)\n\t" M1_(Y, KB) KBRD \
    E_(X, 6) E_(X, 7) C_(X, 2, 4, 5) E_(X, 8) C_(X, 3, 6, 7) E_(X, 9) E_(X, 10) E_(X, 11) C_(X, 8, 8, 9) \
    PV_(oa, VC, 0, X, 0) RDV(VP, VOFF) \
    E_(X, 12) C_(X, 9, 10, 11) E_(X, 13) E_(X, 14) E_(X, 15) C_(X, 10, 12, 13) E_(X, 16) C_(X, 11, 14, 15) E_(X, 17) \
    M2_(Y, 0, q1a) \
    E_(X, 18) E_(X, 19) C_(X, 16, 16, 17) E_(X, 20) E_(X, 21) C_(X, 17, 18, 19) \
    PV_(oa, VC, 4, X, 8) \
    E_(X, 22) E_(X, 23) C_(X, 18, 20, 21) E_(X, 24) C_(X, 19, 22, 23) E_(X, 25) \
    M2_(Y, 16, q1b) RDKA(KAOFF) \
    E_(X, 26) E_(X, 27) C_(X, 24, 24, 25) E_(X, 28) E_(X, 29) C_(X, 25, 26, 27) \
    PV_(ob, VC, 0, X, 16) \
    E_(X, 30) E_(X, 31) C_(X, 26, 28, 29) C_(X, 27, 30, 31)

DI int swap34(int x) { return (x & ~24) | ((x & 8) << 1) | ((x & 16) >> 1); }
DI float max_halves(float m) {
    auto rr = __builtin_amdgcn_permlane32_swap(__float_as_uint(m), __float_as_uint(m), false, false);
    return fmaxf(__uint_as_float(rr[0]), __uint_as_float(rr[1]));
}
DI void attn_fallback(const char* lds, int l, const char* qp, int tile, f32x16& o) {
    const int r32 = l & 31, h = l >> 5;
    const s16x4 z4 = {0, 0, 0, 0};
    const bf16x8 q1 = *(const bf16x8*)(qp + tile * 1024 + l * 16);
    const s16x4 q2 = *(const s16x4*)(qp + 2048 + r32 * 16 + tile * 8);
    const bf16x8 q2f = cat8(h == 0 ? q2 : z4, z4);
    const bf16x8 ones = {0x3f80, 0x3f80, 0x3f80, 0x3f80, 0x3f80, 0x3f80, 0x3f80, 0x3f80};
    float m = -INFINITY;
#pragma unroll
    for (int i = 0; i < 16; ++i) o[i] = 0.f;
#pragma unroll 1
    for (int t = 0; t < 64; ++t) {
        const LDSP char* cb = (const LDSP char*)lds + (t >> 1) * 5120;
        const bf16x8 k1 = *(const LDSP bf16x8*)(cb + (t & 1) * 1024 + l * 16);
        const s16x4 k2 = *(const LDSP s16x4*)(cb + 2048 + r32 * 16 + (t & 1) * 8);
        f32x16 s;
#pragma unroll
        for (int i = 0; i < 16; ++i) s[i] = 0.f;
        s = MFMA32(k1, q1, s);
        s = MFMA32(cat8(k2, z4), q2f, s);
        float tm = s[0];
#pragma unroll
        for (int i = 1; i < 16; ++i) tm = fmaxf(tm, s[i]);
        tm = max_halves(tm);
        const float mn = fmaxf(m, tm), alpha = __builtin_amdgcn_exp2f(m - mn);
        m = mn;
        u32x4 p0, p1;
#pragma unroll
        for (int i = 0; i < 4; ++i) {
            p0[i] = pk_bf16(__builtin_amdgcn_exp2f(s[2 * i] - m), __builtin_amdgcn_exp2f(s[2 * i + 1] - m));
            p1[i] = pk_bf16(__builtin_amdgcn_exp2f(s[8 + 2 * i] - m), __builtin_amdgcn_exp2f(s[9 + 2 * i] - m));
        }
#pragma unroll
        for (int i = 0; i < 16; ++i) o[i] *= alpha;
        const int dv = r32 < 20 ? r32 : 0;
        bf16x8 v0 = *(const LDSP bf16x8*)(cb + 2560 + (t & 1) * 1280 + h * 320 + dv * 16);
        bf16x8 v1 = *(const LDSP bf16x8*)(cb + 2560 + (t & 1) * 1280 + 640 + h * 320 + dv * 16);
        if (r32 >= 20) { v0 = ones; v1 = ones; }
        o = MFMA32(v0, __builtin_bit_cast(bf16x8, p0), o);
        o = MFMA32(v1, __builtin_bit_cast(bf16x8, p1), o);
    }
}

DI bool attn_ok(const f32x16& a, const f32x16& b) {
    f32x2 s = {0.f, 0.f};
#pragma unroll
    for (int i = 0; i < 12; i += 2) s += f32x2{a[i], a[i + 1]} + f32x2{b[i], b[i + 1]};
    return (fmaxf(a[12], b[12]) < 0x1p60f) & (fabsf(s[0] + s[1] + a[12] + b[12]) < 0x1p126f);
}
DI void attn_pack(const f32x16& oa, const f32x16& ob, float ga, float gb, u32x4& va, u32x4& vb, u32x2& vt) {
    const float fa = ga * __builtin_amdgcn_rcpf(oa[12]), fb = gb * __builtin_amdgcn_rcpf(ob[12]);
    va = u32x4{pk_bf16(oa[0] * fa, oa[1] * fa), pk_bf16(oa[2] * fa, oa[3] * fa), pk_bf16(oa[4] * fa, oa[5] * fa), pk_bf16(oa[6] * fa, oa[7] * fa)};
    vb = u32x4{pk_bf16(ob[0] * fb, ob[1] * fb), pk_bf16(ob[2] * fb, ob[3] * fb), pk_bf16(ob[4] * fb, ob[5] * fb), pk_bf16(ob[6] * fb, ob[7] * fb)};
    auto s0 = __builtin_amdgcn_permlane32_swap(pk_bf16(oa[8] * fa, oa[9] * fa), pk_bf16(ob[8] * fb, ob[9] * fb), false, false);
    auto s1 = __builtin_amdgcn_permlane32_swap(pk_bf16(oa[10] * fa, oa[11] * fa), pk_bf16(ob[10] * fb, ob[11] * fb), false, false);
    vt = u32x2{s0[0], s1[0]};
}

typedef __attribute__((ext_vector_type(16))) unsigned u32x16;
#define WSUB(v, i) __builtin_bit_cast(bf16x8, __builtin_shufflevector(v, v, 4 * (i), 4 * (i) + 1, 4 * (i) + 2, 4 * (i) + 3))

__global__ __launch_bounds__(512, 2) void k_attn(char* __restrict__ ws, char* __restrict__ out) {
    __shared__ __attribute__((aligned(16))) char lds[163840];
    const int b = blockIdx.x, xcd = b & 7, ii = b >> 3;
    const int pos = ii >> 2, head = (pos >> 2) * 32 + xcd * 4 + (pos & 3), slot = xcd * 8 + pos, qb = ii & 3;
    const int w = __builtin_amdgcn_readfirstlane(threadIdx.x >> 6);
    int l = threadIdx.x & 63, r32 = l & 31, h = l >> 5;
    const int qc = qb * 8 + w;
    const unsigned lds0 = (unsigned)(uintptr_t)lds;
    const unsigned qo = (unsigned)WS_Q + head * (unsigned)Q_HEAD + qc * 2560, kvo = (unsigned)WS_KV + (head < 32 ? 32 : head - 32) * (unsigned)KV_HEAD;
    bf16x8 q1a, q1b, k1f, k2f; s16x4 q2; float ga, gb;
    unsigned dv = kvo + w * 4096 + l * 16;
    {
        unsigned sv, dm = __builtin_amdgcn_readfirstlane(lds0 + w * 4096);
        asm volatile(
            "global_load_dwordx4 v[4:7], %[a0], %[kvp]\n\tglobal_load_dwordx4 v[8:11], %[a0], %[kvp] offset:1024\n\tglobal_load_dwordx2 v[12:13], %[a1], %[kvp] offset:2048\n\t"
            "global_load_dwordx4 " KA_ ", %[a2], %[kvp]\n\tglobal_load_dwordx4 v[" STR(KBR) ":" STR(KBR) "+3], %[a3], %[kvp] offset:2048\n\t"
            "global_load_dword v14, %[a4], %[kvp]\n\tglobal_load_dword v15, %[a4], %[kvp] offset:128\n\t"
            "s_mov_b32 %[sv], m0\n\t" DMA4 "s_mov_b32 m0, %[sv]"
            : "=&{v[4:7]}"(q1a), "=&{v[8:11]}"(q1b), "=&{v[12:13]}"(q2), "=&{v14}"(ga), "=&{v15}"(gb), "=&{v[168:171]}"(k1f), "=&{v[172:175]}"(k2f),
              [sv] "=&s"(sv), [dv] "+v"(dv), [dm] "+s"(dm)
            : [kvp] "s"(ws), [a0] "v"(qo + l * 16), [a1] "v"(qo + r32 * 16 + h * 8), [a2] "v"(kvo + l * 16), [a3] "v"(kvo + r32 * 16), [a4] "v"((unsigned)WS_G + qc * 256 + swap34(r32) * 4)
            : "memory", "scc");
    }
    const int mb = xcd * 4 + (ii & 3), nb = ii >> 2;
    const char* wp = ws + WS_WO + (size_t)(nb * 8 + w) * 25600 + 2048;
    const char* cp = ws + WS_CNT + (((mb >> 3) * 8 + w) * 8 + nb) * 128;
    const char* sgp = ws + WS_CNT + (qb * 8 + xcd) * 8 * 128;
    const char* a1 = ws + WS_O1 + (size_t)w * 8 * 65536 + mb * 2048;
    const char* a2 = ws + WS_O2 + (size_t)w * 4 * 32768 + mb * 1024;
    const char* op = out + (size_t)mb * 64 * 1280 + nb * 160;
    const char* po1 = ws + WS_O1 + (size_t)slot * 65536 + qc * 2048;
    const char* po2 = ws + WS_O2 + (size_t)(slot >> 1) * 32768 + qc * 1024 + (slot & 1) * 8;
    f32x16 oa, ob; u32x16 vini;
#pragma unroll
    for (int i = 0; i < 16; ++i) { oa[i] = 0.f; ob[i] = 0.f; vini[i] = (r32 & 27) == 24 ? 0x3f803f80u : 0u; }
    unsigned ak1 = lds0 + l * 16, ak2 = lds0 + 2048 + r32 * 16, av = lds0 + 2560 + h * 320 + r32 * 16, wv = l * 16;
    unsigned cnt, inc, svm, dm = __builtin_amdgcn_readfirstlane(lds0 + w * 4096 + 32768);
    u32x16 wt0, wt1, wt2, wt3, wt4;
    asm volatile(
        "s_mov_b32 %[sv], m0\n\t"
        ".irp n,24,25,26,27\n\tv_mov_b32 v[" STR(S1R) "+\\n], 0\n\t.endr\n\t"
        "s_mov_b32 %[cnt], 0\n\ts_mov_b32 %[inc], 20480\n\ts_mov_b64 vcc, -1\n\t"
        "s_waitcnt vmcnt(6)\n\t"
        "v_mfma_f32_32x32x4_2b_bf16 v[" STR(S0R) ":" STR(S0R) "+31], " KB0_ ", %[q2], 0\n\ts_nop 15\n\ts_nop 1\n\t"
        M2_(S0R, 0, q1a) M2_(S0R, 16, q1b)
        "s_nop 10\n\t"
        MX3(0, S0R + 0, 1) MX3(16, S0R + 16, 17) MX3(0, NMR, 3) MX3(16, NMR + 16, 19) MX3(0, NMR, 5) MX3(16, NMR + 16, 21) MX3(0, NMR, 7) MX3(16, NMR + 16, 23)
        MX3(0, NMR, 9) MX3(16, NMR + 16, 25) MX3(0, NMR, 11) MX3(16, NMR + 16, 27) MX3(0, NMR, 13) MX3(16, NMR + 16, 29) MX3(0, NMR, 14) MX3(16, NMR + 16, 30)
        "v_mov_b32 v[" STR(NMR) "+1], v[" STR(NMR) "]\n\tv_mov_b32 v[" STR(NMR) "+17], v[" STR(NMR) "+16]\n\ts_nop 1\n\t"
        "v_permlane32_swap_b32 v[" STR(NMR) "], v[" STR(NMR) "+1]\n\tv_permlane32_swap_b32 v[" STR(NMR) "+16], v[" STR(NMR) "+17]\n\ts_nop 1\n\t"
        "v_min_f32 v[" STR(NMR) "], -v[" STR(NMR) "], -v[" STR(NMR) "+1]\n\tv_min_f32 v[" STR(NMR) "+16], -v[" STR(NMR) "+16], -v[" STR(NMR) "+17]\n\t"
        "v_mov_b32 v[" STR(NMR) "+1], v[" STR(NMR) "]\n\tv_mov_b32 v[" STR(NMR) "+17], v[" STR(NMR) "+16]\n\t"
        ".irp n,0,2,4,6,8,10,12,14\n\tv_pk_add_f32 v[" STR(S0R) "+\\n:" STR(S0R) "+\\n+1], v[" STR(S0R) "+\\n:" STR(S0R) "+\\n+1], v[" STR(NMR) ":" STR(NMR) "+1]\n\t"
        "v_pk_add_f32 v[" STR(S0R) "+16+\\n:" STR(S0R) "+17+\\n], v[" STR(S0R) "+16+\\n:" STR(S0R) "+17+\\n], v[" STR(NMR) "+16:" STR(NMR) "+17]\n\t.endr\n\t"
        ".irp n,2,4,6,8,10,12,14\n\tv_mov_b64 v[" STR(NMR) "+\\n:" STR(NMR) "+\\n+1], v[" STR(NMR) ":" STR(NMR) "+1]\n\t"
        "v_mov_b64 v[" STR(NMR) "+16+\\n:" STR(NMR) "+17+\\n], v[" STR(NMR) "+16:" STR(NMR) "+17]\n\t.endr\n\t"
        "s_waitcnt vmcnt(0)\n\ts_barrier\n\t"
        RDKA(1024) RDV(VAR, 0)
        "1:\n\t"
        TILE(S0R, S1R, VAR, VBR, 5120, KB1_, RDKB(5120), 1280, PRIO(3) "s_cbranch_vccnz 11f\n\t21:\n\t")
        TILE(S1R, S0R, VBR, VAR, 5120 + 1024, KB0_, "", 5120, "s_cbranch_vccnz 12f\n\t22:\n\t")
        TILE(S0R, S1R, VAR, VBR, 10240, KB1_, RDKB(10240), 5120 + 1280, "s_cbranch_vccnz 13f\n\t23:\n\t")
        TILE(S1R, S0R, VBR, VAR, 10240 + 1024, KB0_, "", 10240, "s_cbranch_vccnz 14f\n\t24:\n\t")
        TILE(S0R, S1R, VAR, VBR, 15360, KB1_, RDKB(15360), 10240 + 1280, "")
        TILE(S1R, S0R, VBR, VAR, 15360 + 1024, KB0_, "", 15360, PRIO(2) "s_cmp_eq_u32 %[cnt], 7\n\ts_cselect_b32 %[inc], -143360, %[inc]\n\t")
        TILE(S0R, S1R, VAR, VBR, 0, KB1_, RDKB(0), 15360 + 1280, PRIO(1) "v_add_u32 %[ak1], %[inc], %[ak1]\n\tv_add_u32 %[ak2], %[inc], %[ak2]\n\t")
        TILE(S1R, S0R, VBR, VAR, 1024, KB0_, "", 0, PRIO(0) "v_add_u32 %[av], %[inc], %[av]\n\t")
        "s_add_u32 %[cnt], %[cnt], 1\n\t"
        "s_cmp_eq_u32 %[cnt], 1\n\ts_cbranch_scc1 2f\n\t"
        "s_cmp_eq_u32 %[cnt], 3\n\ts_cbranch_scc1 3f\n\t"
        "s_cmp_eq_u32 %[cnt], 4\n\ts_cbranch_scc1 5f\n\t"
        "s_cmp_eq_u32 %[cnt], 5\n\ts_cbranch_scc1 6f\n\t"
        "s_cmp_eq_u32 %[cnt], 6\n\ts_cbranch_scc1 8f\n\t"
        "s_cmp_lt_u32 %[cnt], 8\n\ts_cbranch_scc1 1b\n\t"
        "s_branch 4f\n\t"
        "11:\n\t" DMA4 "s_branch 21b\n\t"
        "12:\n\t" DMA4 "s_branch 22b\n\t"
        "13:\n\t" DMA4 "s_branch 23b\n\t"
        "14:\n\t" DMA4 "s_branch 24b\n\t"
        "2:\n\ts_mov_b64 vcc, 0\n\ts_waitcnt vmcnt(0)\n\ts_barrier\n\ts_branch 1b\n\t"
        "3:\n\t" WLD(0)
        "5:\n\t" WLD(1)
        "6:\n\t" WLD(2)
        "8:\n\tv_and_b32 %[dv], 0x3ff, %[wv]\n\tv_lshlrev_b32 %[dv], 3, %[dv]\n\tv_min_u32 %[dv], 0x1380, %[dv]\n\tv_add_u32 %[dv], 0x5000, %[dv]\n\t"
        "global_load_dword %[dv], %[dv], %[wp] offset:-2048\n\t" WLD(3)
        "4:\n\ts_nop 1\n\t"
        PV_(ob, VBR, 4, S1R, 24)
        "s_mov_b32 m0, %[sv]\n\ts_waitcnt vmcnt(0) lgkmcnt(0)\n\ts_nop 15\n\ts_nop 7"
        : [oa] "+{v[24:39]}"(oa), [ob] "+{v[40:55]}"(ob), [ak1] "+v"(ak1), [ak2] "+v"(ak2), [av] "+v"(av), [dv] "+v"(dv), [wv] "+v"(wv),
          "+{v[152:167]}"(vini), "+{v[168:171]}"(k1f), "+{v[172:175]}"(k2f),
          [q1a] "+{v[4:7]}"(q1a), [q1b] "+{v[8:11]}"(q1b), [q2] "+{v[12:13]}"(q2), "+{v14}"(ga), "+{v15}"(gb),
          "=&{v[176:191]}"(wt0), "=&{v[192:207]}"(wt1), "=&{v[208:223]}"(wt2), "=&{v[224:239]}"(wt3), "=&{v[240:255]}"(wt4),
          [cnt] "=&s"(cnt), [inc] "=&s"(inc), [sv] "=&s"(svm), [dm] "+s"(dm)
        : [vmask] "s"(0x000fffff000fffffull), [kvp] "s"(ws), [wp] "s"(wp),
          "s"(cp), "s"(sgp), "s"(a1), "s"(a2), "s"(op), "s"(po1), "s"(po2)
        : "memory", "scc", "vcc", "v56", "v57", "v58", "v59", CL10(6), CL10(7), CL10(8), CL10(9), CL10(10), CL10(11), CL10(12), CL10(13), CL10(14), "v150", "v151"
          );
    asm volatile("v_mbcnt_lo_u32_b32 %0, -1, 0\n\tv_mbcnt_hi_u32_b32 %0, -1, %0" : "=v"(l));
    r32 = l & 31; h = l >> 5;
    bf16x8 wl[5], af[5][4]; unsigned seen;
    {
        if (head >= 32) { ga = 1.0f - ga; gb = 1.0f - gb; }
        u32x4 va, vb; u32x2 vt;
        attn_pack(oa, ob, ga, gb, va, vb, vt);
        if (__builtin_expect(!__all(attn_ok(oa, ob)), 0)) {
#pragma unroll 1
            for (int t = 0; t < 2; ++t) {
                f32x16 ro; u32x4 v0, v1; u32x2 v2;
                attn_fallback(lds, l, ws + qo, t, ro);
                attn_pack(ro, ro, t ? gb : ga, t ? gb : ga, v0, v1, v2);
                if (t == 0) va = v0; else vb = v0;
                if (h == t) vt = v2;
            }
        }
        asm volatile(
            "global_store_dwordx4 %[lo], %[va], %[o1] sc1\n\tglobal_store_dwordx4 %[lo], %[vb], %[o1] offset:1024 sc1\n\tglobal_store_dwordx2 %[lo], %[vt], %[o2] sc1\n\t"
            "s_nop 0"
            :: [lo] "v"(swap34(l) * 16), [va] "v"(va), [vb] "v"(vb), [vt] "v"(vt), [o1] "s"(po1), [o2] "s"(po2)
            : "memory");
    }
    const int r = l & 15, g = l >> 4, r8 = l & 7, g2 = (l >> 3) & 3;
    unsigned o1 = (g >> 1) * 65536 + (g & 1) * 512 + r * 16, o2 = g * 32768 + r * 16;
    unsigned wbo = w * 20480 + (g * 16 + (r ^ ((g >> 1) << 3))) * 16;
    unsigned rbo = ((w >> 1) * 5 + h) * 1024 + (g2 * 16 + ((8 * (w & 1) + r8) ^ ((g2 >> 1) << 3))) * 16;
    unsigned opo = (8 * w + r8) * 1280 + h * 32 + g2 * 8, sgo = l * 128;
    asm volatile("s_waitcnt vmcnt(0)\n\ts_barrier" : "+v"(o1), "+v"(o2), "+v"(wbo), "+v"(rbo), "+v"(opo), "+v"(sgo) :: "memory");
    if (w == (xcd ^ 1) && l < 8) __hip_atomic_fetch_add((__attribute__((address_space(1))) unsigned*)(sgp + sgo), 1u, __ATOMIC_RELAXED, __HIP_MEMORY_SCOPE_AGENT);
    asm volatile(
        "global_load_dwordx4 %0, %6, %7 offset:-2048\n\tglobal_load_dwordx4 %1, %6, %7 offset:-1024\n\tglobal_load_dwordx4 %2, %6, %7\n\t"
        "global_load_dwordx4 %3, %6, %7 offset:1024\n\tglobal_load_dwordx4 %4, %6, %7 offset:2048\n\tglobal_load_dword %5, %8, %9 sc1\n\ts_waitcnt vmcnt(0)"
        : "=&v"(wl[0]), "=&v"(wl[1]), "=&v"(wl[2]), "=&v"(wl[3]), "=&v"(wl[4]), "=&v"(seen) : "v"(wv), "s"(wp), "v"(0), "s"(cp) : "memory");
#pragma unroll 1
    for (int spin = 0; spin < 400000 && __builtin_amdgcn_readfirstlane(seen) < 8; ++spin) {
        __builtin_amdgcn_s_sleep(4);
        asm volatile("global_load_dword %0, %1, %2 sc1\n\ts_waitcnt vmcnt(0)" : "=v"(seen) : "v"(0), "s"(cp) : "memory");
    }
#define ALD(s, base, off, d1, d2) asm volatile( \
        "global_load_dwordx4 %0, %4, %5 sc1\n\tglobal_load_dwordx4 %1, %4, %5 offset:" #d1 " sc1\n\t" \
        "global_load_dwordx4 %2, %4, %5 offset:" #d2 " sc1\n\tglobal_load_dwordx4 %3, %4, %5 offset:" #d1 "+" #d2 " sc1" \
        : "=&v"(af[s][0]), "=&v"(af[s][1]), "=&v"(af[s][2]), "=&v"(af[s][3]) : "v"(off), "s"(base) : "memory")
#define AWAIT(s, N) asm volatile("s_waitcnt vmcnt(" STR(N) ")" : "+v"(af[s][0]), "+v"(af[s][1]), "+v"(af[s][2]), "+v"(af[s][3]) :: "memory")
#define OMM(s, w0, w1, w2, w3, w4) do { const bf16x8 wf_[5] = {w0, w1, w2, w3, w4}; \
        _Pragma("unroll") for (int j = 0; j < 5; ++j) _Pragma("unroll") for (int i = 0; i < 4; ++i) acc[i][j] = MFMA16(wf_[j], af[s][i], acc[i][j]); \
        __builtin_amdgcn_sched_barrier(0); } while (0)
    f32x4 acc[4][5];
#pragma unroll
    for (int i = 0; i < 4; ++i)
#pragma unroll
        for (int j = 0; j < 5; ++j) acc[i][j] = f32x4{0.f, 0.f, 0.f, 0.f};
    ALD(0, a1, o1, 256, 1024); ALD(1, a1 + 131072, o1, 256, 1024); ALD(2, a1 + 2 * 131072, o1, 256, 1024);
    AWAIT(0, 8); ALD(3, a1 + 3 * 131072, o1, 256, 1024); OMM(0, WSUB(wt0, 0), WSUB(wt0, 1), WSUB(wt0, 2), WSUB(wt0, 3), WSUB(wt1, 0));
    AWAIT(1, 8); ALD(4, a2, o2, 256, 512); OMM(1, WSUB(wt1, 1), WSUB(wt1, 2), WSUB(wt1, 3), WSUB(wt2, 0), WSUB(wt2, 1));
    AWAIT(2, 8); OMM(2, WSUB(wt2, 2), WSUB(wt2, 3), WSUB(wt3, 0), WSUB(wt3, 1), WSUB(wt3, 2));
    AWAIT(3, 4); OMM(3, WSUB(wt3, 3), WSUB(wt4, 0), WSUB(wt4, 1), WSUB(wt4, 2), WSUB(wt4, 3));
    AWAIT(4, 0);
#pragma unroll
    for (int j = 0; j < 6; ++j) {
        if (j < 5) {
#pragma unroll
            for (int i = 0; i < 4; ++i) acc[i][j] = MFMA16(wl[j], af[4][i], acc[i][j]);
        }
        if (j > 0) {
#pragma unroll
            for (int i = 0; i < 4; ++i) *(f32x4*)(lds + wbo + (i * 5 + j - 1) * 1024) = acc[i][j - 1];
        }
        __builtin_amdgcn_sched_barrier(0);
    }
    {
        __syncthreads();
        f32x4 t[3][8];
#pragma unroll
        for (int u = 0; u < 8; ++u)
#pragma unroll
            for (int p = 0; p < 3; ++p) t[p][u] = *(const f32x4*)(lds + rbo + u * 20480 + (p < 2 ? p * 2048 : 4096 - h * 1024));
        __builtin_amdgcn_sched_barrier(0);
#pragma unroll
        for (int p = 0; p < 3; ++p) {
            const f32x4 z = ((t[p][0] + t[p][1]) + (t[p][2] + t[p][3])) + ((t[p][4] + t[p][5]) + (t[p][6] + t[p][7]));
            const u32x2 v = {pk_bf16(z[0], z[1]), pk_bf16(z[2], z[3])};
            if (p < 2 || h == 0) asm volatile("global_store_dwordx2 %0, %1, %2 offset:%3" :: "v"(opo), "v"(v), "s"(op), "i"(p * 64) : "memory");
        }
    }
}

extern "C" void kernel_launch(void* const* d_in, const int* in_sizes, int n_in, void* d_out, int out_size, void* d_ws, size_t ws_size,
                              hipStream_t stream) {
    const char* x = (const char*)d_in[0];
    const char* wq = (const char*)d_in[1];
    const char* wk = (const char*)d_in[2];
    const char* wv = (const char*)d_in[3];
    const char* woa = (const char*)d_in[4];
    const char* wqkv = (const char*)d_in[6];
    const char* wob = (const char*)d_in[7];
    const char* gw = (const char*)d_in[8];
    char* ws = (char*)d_ws;
    k_proj<<<256, 768, 0, stream>>>(x, wq, wqkv, wk, wv, gw, woa, wob, ws);
    k_attn<<<256, 512, 0, stream>>>(ws, (char*)d_out);
}
```

```cpp
#include <hip/hip_runtime.h>
#include <hip/hip_bf16.h>
#include <stdint.h>

typedef __attribute__((ext_vector_type(8))) short bf16x8;
typedef __attribute__((ext_vector_type(4))) short s16x4;
typedef __attribute__((ext_vector_type(4))) float f32x4;
typedef __attribute__((ext_vector_type(16))) float f32x16;
typedef __attribute__((ext_vector_type(2))) unsigned u32x2;
typedef __attribute__((ext_vector_type(4))) unsigned u32x4;
typedef __attribute__((ext_vector_type(2))) __bf16 bf16x2_t;
typedef __attribute__((ext_vector_type(2))) float f32x2;
#define LDSP __attribute__((address_space(3)))
#define DI __device__ __forceinline__

constexpr int DM = 640;
constexpr size_t Q_HEAD = 32 * 2560;
constexpr size_t KV_HEAD = 32 * 5120;
constexpr size_t WS_Q = 0;
constexpr size_t WS_KV = WS_Q + 64 * Q_HEAD;
constexpr size_t WS_G = WS_KV + 33 * KV_HEAD;
constexpr size_t WS_O1 = WS_G + 2048 * 4;
constexpr size_t WS_O2 = WS_O1 + 64 * 65536;
constexpr size_t WS_WO = WS_O2 + 32 * 2048 * 16;
constexpr size_t WS_CNT = WS_WO + 1600 * 1024;

DI unsigned pk_bf16(float a, float b) {
    f32x2 v = {a, b};
    return __builtin_bit_cast(unsigned, __builtin_convertvector(v, bf16x2_t));
}
DI void st16(char* p, u32x4 v) { asm volatile("global_store_dwordx4 %0, %1, off sc1\n\ts_nop 1" :: "v"(p), "v"(v) : "memory"); }
DI void st8(char* p, u32x2 v) { asm volatile("global_store_dwordx2 %0, %1, off sc1" :: "v"(p), "v"(v) : "memory"); }
DI void st4(char* p, unsigned v) { asm volatile("global_store_dword %0, %1, off sc1" :: "v"(p), "v"(v) : "memory"); }
DI s16x4 tr_read(const LDSP char* p) {
    typedef short v4i16_t __attribute__((ext_vector_type(4)));
    return __builtin_amdgcn_ds_read_tr16_b64_v4i16((LDSP v4i16_t*)p);
}
DI bf16x8 cat8(s16x4 lo, s16x4 hi) { return __builtin_shufflevector(lo, hi, 0, 1, 2, 3, 4, 5, 6, 7); }
#define MFMA16(a, b, c) __builtin_amdgcn_mfma_f32_16x16x32_bf16((a), (b), (c), 0, 0, 0)
#define MFMA32(a, b, c) __builtin_amdgcn_mfma_f32_32x32x16_bf16((a), (b), (c), 0, 0, 0)

constexpr int P_SLOT = 40960;

DI void proj_loader(char* lds, const char* xs, const char* ws, const char* es, int sw, int se, unsigned long long m5, int st, int ekind, int ehalf, int mb,
                    const char* woa, const char* wob, char* wsp, const unsigned (&xo)[2], const unsigned (&wo)[3]) {
    const int l = threadIdx.x & 63, w = __builtin_amdgcn_readfirstlane(threadIdx.x >> 6), r = l & 15, g = l >> 4;
    const unsigned dw = __builtin_amdgcn_readfirstlane((unsigned)(uintptr_t)lds + w * 1024);
    auto issue = [&](int slot) {
        const unsigned d0 = dw + slot * P_SLOT;
        unsigned sv;
        asm volatile(
            "s_mov_b32 %[sv], m0\n\ts_mov_b32 m0, %[d0]\n\ts_nop 0\n\t"
            "global_load_lds_dwordx4 %[x0], %[xs]\n\ts_add_u32 m0, m0, 8192\n\ts_nop 0\n\t"
            "global_load_lds_dwordx4 %[x1], %[xs]\n\ts_add_u32 m0, m0, 8192\n\ts_nop 0\n\t"
            "global_load_lds_dwordx4 %[w0], %[ws]\n\ts_add_u32 m0, m0, 8192\n\ts_nop 0\n\t"
            "global_load_lds_dwordx4 %[w1], %[ws]\n\ts_add_u32 m0, m0, 8192\n\ts_mov_b64 exec, %[m5]\n\t"
            "global_load_lds_dwordx4 %[w2], %[es]\n\ts_mov_b64 exec, -1\n\ts_mov_b32 m0, %[sv]"
            : [sv] "=&s"(sv)
            : [d0] "s"(d0), [m5] "s"(m5), [xs] "s"(xs), [ws] "s"(ws), [es] "s"(es),
              [x0] "v"(xo[0]), [x1] "v"(xo[1]), [w0] "v"(wo[0]), [w1] "v"(wo[1]), [w2] "v"(wo[2])
            : "memory", "scc");
        const int d = st == 9 ? -9 : 1;
        st += d; xs += d * 128; ws += d * sw; es += d * se;
    };
    issue(0); issue(1);
    __builtin_amdgcn_s_setprio(3);
    f32x4 eacc = {0.f, 0.f, 0.f, 0.f};
    const int ets = 4 * ehalf + w;
    if (w >= 4) ekind = 5;
    const int exr = (16 * ets + r) * 128 + ((g ^ ((r >> 1) & 7)) << 4);
    const int ewr = ekind < 4 ? 36864 + (8 * g + (r >> 2)) * 40 + ((ekind & 1) ? 32 : 8 * (r & 3)) : 36864 + g * 16;
    auto extra = [&](int k) {
        if (ekind < 5) {
            const LDSP char* sb = (const LDSP char*)lds + (k & 3) * P_SLOT;
            const bf16x8 x0 = *(const LDSP bf16x8*)(sb + exr), x1 = *(const LDSP bf16x8*)(sb + (exr ^ 64));
            bf16x8 w0, w1;
            if (ekind < 4) { w0 = cat8(tr_read(sb + ewr), tr_read(sb + ewr + 160)); w1 = cat8(tr_read(sb + ewr + 1280), tr_read(sb + ewr + 1440)); }
            else { w0 = *(const LDSP bf16x8*)(sb + ewr); w1 = *(const LDSP bf16x8*)(sb + ewr + 64); }
            if ((ekind >> 1) == 1) { eacc = MFMA16(x0, w0, eacc); eacc = MFMA16(x1, w1, eacc); }
            else { eacc = MFMA16(w0, x0, eacc); eacc = MFMA16(w1, x1, eacc); }
            asm volatile("" : "+v"(eacc));
        }
    };
    const int gw = blockIdx.x * 8 + w, fid = gw < 1600 ? gw : 1599;
    const int fj = fid % 5, fs = (fid / 5) % 5, fg = (fid / 25) & 7, fn = fid / 200;
    const int br = fs < 4 ? fs >> 1 : g >> 1;
    const int row = fs < 4 ? (4 * fg + 2 * (fs & 1) + (g >> 1)) * 20 + 4 * (g & 1) : (4 * fg + 2 * (g & 1)) * 20 + 16;
    const char* p0 = (br ? wob : woa) + row * 1280 + (80 * fn + 16 * fj + r) * 2;
    const char* p1 = p0 + (fs < 4 ? 8 : 20) * 1280;
    asm volatile("" : "+v"(p0), "+v"(p1));
#pragma unroll 1
    for (int k = 0; k < 8; ++k) {
        asm volatile("s_waitcnt vmcnt(5)\n\ts_barrier" ::: "memory");
        issue((k + 2) & 3);
        if (k < 7) extra(k);
    }
    unsigned e0, e1, e2, e3, e4, e5, e6, e7;
    asm volatile(
        "global_load_ushort %0, %8, off\n\tglobal_load_ushort %1, %8, off offset:1280\n\tglobal_load_ushort %2, %8, off offset:2560\n\tglobal_load_ushort %3, %8, off offset:3840\n\t"
        "global_load_ushort %4, %9, off\n\tglobal_load_ushort %5, %9, off offset:1280\n\tglobal_load_ushort %6, %9, off offset:2560\n\tglobal_load_ushort %7, %9, off offset:3840"
        : "=&v"(e0), "=&v"(e1), "=&v"(e2), "=&v"(e3), "=&v"(e4), "=&v"(e5), "=&v"(e6), "=&v"(e7) : "v"(p0), "v"(p1) : "memory");
    extra(7);
    asm volatile("s_waitcnt vmcnt(13)\n\ts_barrier" ::: "memory");
    extra(8);
    asm volatile("s_waitcnt vmcnt(8)\n\ts_barrier" ::: "memory");
    extra(9);
    asm volatile("s_waitcnt vmcnt(0)" : "+v"(e0), "+v"(e1), "+v"(e2), "+v"(e3), "+v"(e4), "+v"(e5), "+v"(e6), "+v"(e7) :: "memory");
    {
        char* kva = wsp + WS_KV + 32 * KV_HEAD + (mb * 2 + ehalf) * 5120;
        const u32x2 v = {pk_bf16(eacc[0], eacc[1]), pk_bf16(eacc[2], eacc[3])};
        const int t = (w >> 1) & 1, erow = 16 * (r >> 3) + 8 * (w & 1) + (r & 7);
        if (ekind == 0) st8(kva + t * 1024 + (g >> 1) * 512 + erow * 16 + (g & 1) * 8, v);
        else if (ekind == 1) { if (g == 0) st8(kva + 2048 + erow * 16 + t * 8, v); }
        else if (ekind < 4) { if (ekind == 2 || r < 4) st8(kva + 2560 + t * 1280 + g * 320 + ((ekind & 1) * 16 + r) * 16 + (w & 1) * 8, v); }
        else if (ekind == 4) { if (g == 0) st4(wsp + WS_G + (mb * 128 + 16 * ets + r) * 4, __float_as_uint(1.0f / (1.0f + __expf(-eacc[0])))); }
    }
    if (gw < 1600) st16(wsp + WS_WO + (size_t)fid * 1024 + l * 16, u32x4{e0 | (e1 << 16), e2 | (e3 << 16), e4 | (e5 << 16), e6 | (e7 << 16)});
    if (w == 0 && l == 0) st4(wsp + WS_CNT + blockIdx.x * 128, 0);
}

#define STR_(x) #x
#define STR(x) STR_(x)
#define PR4(b) "v[" STR(b) ":" STR(b) "+3]"
#define PM_F(j, i, X) "v_mfma_f32_16x16x32_bf16 " PR4(20 + 16 * j + 4 * i) ", " PR4(136 + 4 * j) ", " PR4(X + 4 * i) ", " PR4(20 + 16 * j + 4 * i) "\n\t"
#define PM_T(j, i, X) "v_mfma_f32_16x16x32_bf16 " PR4(20 + 16 * j + 4 * i) ", " PR4(X + 4 * i) ", " PR4(136 + 4 * j) ", " PR4(20 + 16 * j + 4 * i) "\n\t"
#define PTL(j, sl, ks) "ds_read_b64_tr_b16 v[136+4*" #j ":136+4*" #j "+1], %[aw" #j "] offset:" #sl "*40960+" #ks "*10240\n\t"
#define PTH(j, sl, ks) "ds_read_b64_tr_b16 v[136+4*" #j "+2:136+4*" #j "+3], %[aw" #j "] offset:" #sl "*40960+" #ks "*10240+1280\n\t"
#define PBX(X, i, sl, ks) "ds_read_b128 " PR4(X + 4 * i) ", %[ax" #ks "] offset:" #sl "*40960+" #i "*2048\n\t"
#define PTG(a) "v_add_u32 %[" #a "], %[tg], %[" #a "]\n\t"
#define PSTEP(M, X, Y, cs, ck, ns, nk, BAR, F0, F1, F2, F3) \
    M(0, 0, X) PTL(4, cs, ck) M(0, 1, X) PTH(4, cs, ck) M(0, 2, X) BAR PBX(Y, 0, ns, nk) M(0, 3, X) PBX(Y, 1, ns, nk) \
    M(1, 0, X) PTL(0, ns, nk) M(1, 1, X) PTH(0, ns, nk) M(1, 2, X) PBX(Y, 2, ns, nk) M(1, 3, X) "s_waitcnt lgkmcnt(5)\n\t" \
    M(2, 0, X) PTL(1, ns, nk) M(2, 1, X) PTH(1, ns, nk) M(2, 2, X) PBX(Y, 3, ns, nk) M(2, 3, X) F0 \
    M(3, 0, X) PTL(2, ns, nk) M(3, 1, X) PTH(2, ns, nk) M(3, 2, X) F1 M(3, 3, X) F2 \
    M(4, 0, X) PTL(3, ns, nk) M(4, 1, X) PTH(3, ns, nk) M(4, 2, X) F3 M(4, 3, X) "s_waitcnt lgkmcnt(4)\n\t"
#define CL10(a) "v" #a "0", "v" #a "1", "v" #a "2", "v" #a "3", "v" #a "4", "v" #a "5", "v" #a "6", "v" #a "7", "v" #a "8", "v" #a "9"
#define PLOOP(M) \
    asm volatile( \
        "s_mov_b32 %[cnt], 0\n\ts_mov_b32 %[tg], 81920\n\t" \
        "s_barrier\n\t" \
        PBX(104, 0, 0, 0) PBX(104, 1, 0, 0) PTL(0, 0, 0) PTH(0, 0, 0) PBX(104, 2, 0, 0) PTL(1, 0, 0) PTH(1, 0, 0) PBX(104, 3, 0, 0) \
        PTL(2, 0, 0) PTH(2, 0, 0) PTL(3, 0, 0) PTH(3, 0, 0) "s_waitcnt lgkmcnt(4)\n\t" \
        "1:\n\t" \
        PSTEP(M, 104, 120, 0, 0, 0, 1, "", "", "", "", "") \
        PSTEP(M, 120, 104, 0, 1, 1, 0, "s_barrier\n\t", PTG(ax0), "s_add_u32 %[cnt], %[cnt], 1\n\t", "", "") \
        PSTEP(M, 104, 120, 1, 0, 1, 1, "", PTG(aw0), PTG(aw1), PTG(aw2), PTG(ax1)) \
        PSTEP(M, 120, 104, 1, 1, 0, 0, "s_cmp_eq_u32 %[cnt], 5\n\ts_cbranch_scc1 2f\n\ts_barrier\n\t2:\n\t", PTG(aw3), PTG(aw4), "s_sub_u32 %[tg], 0, %[tg]\n\t", \
              "s_cmp_lg_u32 %[cnt], 5\n\t") \
        "s_cbranch_scc1 1b\n\t" \
        "s_waitcnt lgkmcnt(0)\n\ts_nop 7" \
        : "+{v[20:35]}"(acc[0]), "+{v[36:51]}"(acc[1]), "+{v[52:67]}"(acc[2]), "+{v[68:83]}"(acc[3]), "+{v[84:99]}"(acc[4]), \
          [ax0] "+v"(ax0), [ax1] "+v"(ax1), [aw0] "+v"(aw[0]), [aw1] "+v"(aw[1]), [aw2] "+v"(aw[2]), [aw3] "+v"(aw[3]), [aw4] "+v"(aw[4]), \
          [cnt] "=&s"(cnt), [tg] "=&s"(tg) \
        : \
        : "memory", "scc", "v104", "v105", "v106", "v107", "v108", "v109", CL10(11), CL10(12), CL10(13), CL10(14), "v150", "v151", "v152", "v153", "v154", "v155")

template <bool VT>
DI void proj_mma(unsigned lds0, f32x16 (&acc)[5]) {
    const int tid = threadIdx.x, l = tid & 63, w = __builtin_amdgcn_readfirstlane(tid >> 6) - 8;
    const int wm = w >> 1, wn = w & 1, r = l & 15, g = l >> 4;
    const int xsw = (g ^ ((r >> 1) & 7)) << 4;
    unsigned ax0 = lds0 + (64 * wm + r) * 128 + xsw, ax1 = lds0 + (64 * wm + r) * 128 + (xsw ^ 64);
    unsigned aw[5];
    const int q = r >> 2, p = r & 3;
#pragma unroll
    for (int j = 0; j < 5; ++j) {
        int col;
        if (j == 4) col = (4 * wn + p) * 20 + 16;
        else if (VT) col = (4 * wn + j) * 20 + 4 * p;
        else col = (4 * wn + (j & 2) + (p >> 1)) * 20 + 8 * (p & 1) + 4 * (j & 1);
        aw[j] = lds0 + 16384 + (8 * g + q) * 320 + ((col * 2) ^ ((g & 1) << 5));
    }
    unsigned cnt, tg;
    if constexpr (VT) PLOOP(PM_T); else PLOOP(PM_F);
}
#define ACC(i, j) f32x4{acc[j][4 * (i)], acc[j][4 * (i) + 1], acc[j][4 * (i) + 2], acc[j][4 * (i) + 3]}

DI u32x4 pk8(const f32x4& a, const f32x4& b, float sc) {
    return u32x4{pk_bf16(a[0] * sc, a[1] * sc), pk_bf16(a[2] * sc, a[3] * sc), pk_bf16(b[0] * sc, b[1] * sc), pk_bf16(b[2] * sc, b[3] * sc)};
}

#define CODE_PROJ "_Z6k_projPKcS0_S0_S0_S0_S0_S0_S0_Pc"
#define CODE_ATTN "_Z6k_attnPcS_"
#define CODE_BYTES 8448
__global__ __launch_bounds__(768, 3) void k_proj(const char* __restrict__ x, const char* __restrict__ wq, const char* __restrict__ wqkv,
                                                 const char* __restrict__ wk, const char* __restrict__ wv, const char* __restrict__ gw,
                                                 const char* __restrict__ woa, const char* __restrict__ wob, char* __restrict__ ws) {
    __shared__ __attribute__((aligned(16))) char lds[4 * P_SLOT];
    const int b = blockIdx.x, xcd = b & 7, ii = b >> 3;
    const int mb = (xcd & 1) * 8 + (ii & 7), type = xcd >> 1, nq = ii >> 3;
    const int tid = threadIdx.x, l = tid & 63, wv_ = __builtin_amdgcn_readfirstlane(tid >> 6);
    const int ldw2 = type == 0 ? 1280 : 3840;
    const int eidx = ((nq >> 1) | ((nq & 1) << 1)) * 4 + type, ehalf = eidx >= 5, ekind = eidx < 10 ? eidx - 5 * ehalf : 5;
    int rot = (ii & 7) + nq + type; rot = 2 * (rot >= 10 ? rot - 10 : (rot >= 5 ? rot - 5 : rot));
    const int ebytes = ekind < 4 ? 2560 : 128, nbytes = ebytes - (wv_ - 4) * 1024;
    const unsigned long long m5 = ~0ull >> (nbytes >= 1024 ? 0 : (nbytes > 0 ? 64 - (nbytes >> 4) : 63));
    const int sw = 64 * ldw2, se = wv_ < 4 ? sw : ebytes, wofs = (type > 1 ? (type - 1) * 1280 : 0) + nq * 320 + rot * sw;
    unsigned xo[2], wo[3];
    xo[0] = (mb * 128 + (tid >> 3)) * (DM * 2) + (((tid ^ (tid >> 4)) & 7) << 4) + rot * 128; xo[1] = xo[0] + 64 * DM * 2;
#pragma unroll
    for (int n = 0; n < 3; ++n) { const int cid = tid + 512 * n, k = (cid * 3277) >> 16, c = cid - 20 * k; wo[n] = k * ldw2 + ((c ^ (((k >> 3) & 1) << 1)) << 4) + wofs; }
    if (tid >= 256) wo[2] = l * 16 + (nbytes > 0 ? (wv_ - 4) * 1024 : 0) + rot * ebytes;
    asm volatile("" : "+v"(xo[0]), "+v"(xo[1]), "+v"(wo[0]), "+v"(wo[1]), "+v"(wo[2])
                 : "s"(x), "s"(wq), "s"(wqkv), "s"(wk), "s"(wv), "s"(gw), "s"(woa), "s"(wob), "s"(ws), "s"(m5), "s"(se), "s"(ekind));
    if (__builtin_expect(wv_ < 8, 1)) {
        const char* wsel = type == 0 ? wq : wqkv;
        proj_loader(lds, x, wsel, wv_ < 4 ? wsel : (ekind < 2 ? wk : (ekind < 4 ? wv : gw)), sw, se, m5, rot, ekind, ehalf, mb, woa, wob, ws, xo, wo);
        return;
    }
    const int w = wv_ - 8;
    if (w < 2) {
        const unsigned dp = (unsigned)(uintptr_t)lds + P_SLOT - 512, off = (ii * 3 + l) * 128; unsigned sv; unsigned long long pc;
        if (w == 0) asm volatile("s_getpc_b64 s[40:41]\n\ts_add_u32 s40, s40, " CODE_PROJ "@rel32@lo+4\n\ts_addc_u32 s41, s41, " CODE_PROJ "@rel32@hi+12" : "={s[40:41]}"(pc) :: "scc");
        else asm volatile("s_getpc_b64 s[40:41]\n\ts_add_u32 s40, s40, " CODE_ATTN "@rel32@lo+4\n\ts_addc_u32 s41, s41, " CODE_ATTN "@rel32@hi+12" : "={s[40:41]}"(pc) :: "scc");
        if (l < 3 && off < CODE_BYTES)
            asm volatile("s_mov_b32 %0, m0\n\ts_mov_b32 m0, %1\n\ts_nop 0\n\tglobal_load_lds_dword %2, %3\n\ts_mov_b32 m0, %0" : "=&s"(sv) : "s"(dp), "v"(off), "s"(pc) : "memory");
    }
    const int wm = w >> 1, wn = w & 1, r = l & 15, g = l >> 4;
    f32x16 acc[5];
#pragma unroll
    for (int j = 0; j < 5; ++j)
#pragma unroll
        for (int i = 0; i < 16; ++i) acc[j][i] = 0.f;
    const unsigned lds0 = (unsigned)(uintptr_t)lds;
    const int chunk = mb * 2 + wm;
#define ST16(voff, sbase, imm, val) asm volatile("global_store_dwordx4 %0, %1, %2 offset:%3 sc1\n\ts_nop 1" :: "v"(voff), "v"(val), "s"(sbase), "i"(imm) : "memory")
    if (type == 3) {
        const char* sb = ws + WS_KV + (size_t)(nq * 8 + 4 * wn) * KV_HEAD + chunk * 5120 + 2560;
        unsigned v1 = g * 320 + r * 16, v2 = (r >> 2) * (unsigned)KV_HEAD + g * 320 + (16 + (r & 3)) * 16;
        asm volatile("" : "+v"(v1), "+v"(v2) : "s"(sb), "s"(sb + KV_HEAD), "s"(sb + 2 * KV_HEAD), "s"(sb + 3 * KV_HEAD));
        proj_mma<true>(lds0, acc);
#pragma unroll
        for (int j = 0; j < 4; ++j) {
            ST16(v1, sb + j * KV_HEAD, 0, pk8(ACC(0, j), ACC(1, j), 1.0f));
            ST16(v1, sb + j * KV_HEAD, 1280, pk8(ACC(2, j), ACC(3, j), 1.0f));
        }
        ST16(v2, sb, 0, pk8(ACC(0, 4), ACC(1, 4), 1.0f));
        ST16(v2, sb, 1280, pk8(ACC(2, 4), ACC(3, 4), 1.0f));
    } else {
        const float sc = type == 2 ? 1.0f : 0.32259642f;
        const unsigned hstride = type == 2 ? KV_HEAD : Q_HEAD;
        const char* sb = ws + (type == 2 ? WS_KV + chunk * 5120 : WS_Q + type * 32 * Q_HEAD + chunk * 2560) + (nq * 8 + 4 * wn) * hstride;
        const unsigned row0 = (16 * (r >> 3) + (r & 7)) * 16;
        unsigned v1 = (g >> 1) * hstride + (g & 1) * 512 + row0, v2 = g * hstride + row0;
        asm volatile("" : "+v"(v1), "+v"(v2) : "s"(sb), "s"(sb + 2 * hstride));
        proj_mma<false>(lds0, acc);
#pragma unroll
        for (int jj = 0; jj < 2; ++jj)
#pragma unroll
            for (int i = 0; i < 4; ++i) ST16(v1, sb + jj * 2 * hstride, (i >> 1) * 1024 + (i & 1) * 128, pk8(ACC(i, 2 * jj), ACC(i, 2 * jj + 1), sc));
        ST16(v2, sb, 2048, pk8(ACC(0, 4), ACC(2, 4), sc));
        ST16(v2, sb, 2048 + 128, pk8(ACC(1, 4), ACC(3, 4), sc));
    }
}

#define S0R 56
#define S1R 88
#define NMR 120
#define VAR 152
#define VBR 160
#define KAR 168
#define KBR 172
#define WFR 176
#define E_(X, i) "v_exp_f32 v[" STR(X) "+" #i "], v[" STR(X) "+" #i "]\n\t"
#define C_(X, d, a, b) "v_cvt_pk_bf16_f32 v[" STR(X) "+" #d "], v[" STR(X) "+" #a "], v[" STR(X) "+" #b "]\n\t"
#define PV_(O, V, vo, X, po) "v_mfma_f32_32x32x16_bf16 %[" #O "], v[" STR(V) "+" #vo ":" STR(V) "+" #vo "+3], v[" STR(X) "+" #po ":" STR(X) "+" #po "+3], %[" #O "]\n\t"
#define M1_(Y, KB) "v_mfma_f32_32x32x4_2b_bf16 v[" STR(Y) ":" STR(Y) "+31], " KB ", %[q2], v[" STR(NMR) ":" STR(NMR) "+31]\n\t"
#define M2_(Y, o, Q) "v_mfma_f32_32x32x16_bf16 v[" STR(Y) "+" #o ":" STR(Y) "+" #o "+15], " KA_ ", %[" #Q "], v[" STR(Y) "+" #o ":" STR(Y) "+" #o "+15]\n\t"
#define KA_ "v[" STR(KAR) ":" STR(KAR) "+3]"
#define KB0_ "v[" STR(KBR) ":" STR(KBR) "+1]"
#define KB1_ "v[" STR(KBR) "+2:" STR(KBR) "+3]"
#define RDKA(off) "ds_read_b128 " KA_ ", %[ak1] offset:" STR(off) "\n\t"
#define RDKB(off) "ds_read_b128 v[" STR(KBR) ":" STR(KBR) "+3], %[ak2] offset:" STR(off) "\n\t"
#define RDV(V, off) "s_mov_b64 exec, %[vmask]\n\tds_read_b128 v[" STR(V) ":" STR(V) "+3], %[av] offset:" STR(off) "\n\t" \
                    "ds_read_b128 v[" STR(V) "+4:" STR(V) "+7], %[av] offset:" STR(off) "+640\n\ts_mov_b64 exec, -1\n\t"
#define MX3(d, a, i) "v_max3_f32 v[" STR(NMR) "+" #d "], v[" STR(a) "], v[" STR(S0R) "+" #i "], v[" STR(S0R) "+" #i "+1]\n\t"
#define PRIO(n) "s_setprio " #n "\n\t"
#define DMA4 "s_mov_b32 m0, %[dm]\n\ts_add_u32 %[dm], %[dm], 0x8000\n\tglobal_load_lds_dwordx4 %[dv], %[kvp]\n\tglobal_load_lds_dwordx4 %[dv], %[kvp] offset:1024\n\t" \
             "global_load_lds_dwordx4 %[dv], %[kvp] offset:2048\n\tglobal_load_lds_dwordx4 %[dv], %[kvp] offset:3072\n\tv_add_u32 %[dv], 0x8000, %[dv]\n\t"
#define WLD1(s, j) "global_load_dwordx4 v[" STR(WFR) "+20*" #s "+4*" #j ":" STR(WFR) "+20*" #s "+4*" #j "+3], %[wv], %[wp] offset:" #j "*1024-2048\n\t"
#define WLD(s) WLD1(s, 0) WLD1(s, 1) WLD1(s, 2) WLD1(s, 3) WLD1(s, 4) "v_add_u32 %[wv], 0x1400, %[wv]\n\ts_branch 1b\n\t"
#define TILE(X, Y, VC, VP, KAOFF, KB, KBRD, VOFF, PRE) \
    PRE E_(X, 0) E_(X, 1) \
    PV_(ob, VP, 4, Y, 24) \
    E_(X, 2) E_(X, 3) C_(X, 0, 0, 1) E_(X, 4) E_(X, 5) C_(X, 1, 2, 3) \
    "s_waitcnt lgkmcnt(0)\n\t" M1_(Y, KB) KBRD \
    E_(X, 6) E_(X, 7) C_(X, 2, 4, 5) E_(X, 8) C_(X, 3, 6, 7) E_(X, 9) E_(X, 10) E_(X, 11) C_(X, 8, 8, 9) \
    PV_(oa, VC, 0, X, 0) RDV(VP, VOFF) \
    E_(X, 12) C_(X, 9, 10, 11) E_(X, 13) E_(X, 14) E_(X, 15) C_(X, 10, 12, 13) E_(X, 16) C_(X, 11, 14, 15) E_(X, 17) \
    M2_(Y, 0, q1a) \
    E_(X, 18) E_(X, 19) C_(X, 16, 16, 17) E_(X, 20) E_(X, 21) C_(X, 17, 18, 19) \
    PV_(oa, VC, 4, X, 8) \
    E_(X, 22) E_(X, 23) C_(X, 18, 20, 21) E_(X, 24) C_(X, 19, 22, 23) E_(X, 25) \
    M2_(Y, 16, q1b) RDKA(KAOFF) \
    E_(X, 26) E_(X, 27) C_(X, 24, 24, 25) E_(X, 28) E_(X, 29) C_(X, 25, 26, 27) \
    PV_(ob, VC, 0, X, 16) \
    E_(X, 30) E_(X, 31) C_(X, 26, 28, 29) C_(X, 27, 30, 31)

DI int swap34(int x) { return (x & ~24) | ((x & 8) << 1) | ((x & 16) >> 1); }
DI float max_halves(float m) {
    auto rr = __builtin_amdgcn_permlane32_swap(__float_as_uint(m), __float_as_uint(m), false, false);
    return fmaxf(__uint_as_float(rr[0]), __uint_as_float(rr[1]));
}
DI void attn_fallback(const char* lds, int l, const char* qp, int tile, f32x16& o) {
    const int r32 = l & 31, h = l >> 5;
    const s16x4 z4 = {0, 0, 0, 0};
    const bf16x8 q1 = *(const bf16x8*)(qp + tile * 1024 + l * 16);
    const s16x4 q2 = *(const s16x4*)(qp + 2048 + r32 * 16 + tile * 8);
    const bf16x8 q2f = cat8(h == 0 ? q2 : z4, z4);
    const bf16x8 ones = {0x3f80, 0x3f80, 0x3f80, 0x3f80, 0x3f80, 0x3f80, 0x3f80, 0x3f80};
    float m = -INFINITY;
#pragma unroll
    for (int i = 0; i < 16; ++i) o[i] = 0.f;
#pragma unroll 1
    for (int t = 0; t < 64; ++t) {
        const LDSP char* cb = (const LDSP char*)lds + (t >> 1) * 5120;
        const bf16x8 k1 = *(const LDSP bf16x8*)(cb + (t & 1) * 1024 + l * 16);
        const s16x4 k2 = *(const LDSP s16x4*)(cb + 2048 + r32 * 16 + (t & 1) * 8);
        f32x16 s;
#pragma unroll
        for (int i = 0; i < 16; ++i) s[i] = 0.f;
        s = MFMA32(k1, q1, s);
        s = MFMA32(cat8(k2, z4), q2f, s);
        float tm = s[0];
#pragma unroll
        for (int i = 1; i < 16; ++i) tm = fmaxf(tm, s[i]);
        tm = max_halves(tm);
        const float mn = fmaxf(m, tm), alpha = __builtin_amdgcn_exp2f(m - mn);
        m = mn;
        u32x4 p0, p1;
#pragma unroll
        for (int i = 0; i < 4; ++i) {
            p0[i] = pk_bf16(__builtin_amdgcn_exp2f(s[2 * i] - m), __builtin_amdgcn_exp2f(s[2 * i + 1] - m));
            p1[i] = pk_bf16(__builtin_amdgcn_exp2f(s[8 + 2 * i] - m), __builtin_amdgcn_exp2f(s[9 + 2 * i] - m));
        }
#pragma unroll
        for (int i = 0; i < 16; ++i) o[i] *= alpha;
        const int dv = r32 < 20 ? r32 : 0;
        bf16x8 v0 = *(const LDSP bf16x8*)(cb + 2560 + (t & 1) * 1280 + h * 320 + dv * 16);
        bf16x8 v1 = *(const LDSP bf16x8*)(cb + 2560 + (t & 1) * 1280 + 640 + h * 320 + dv * 16);
        if (r32 >= 20) { v0 = ones; v1 = ones; }
        o = MFMA32(v0, __builtin_bit_cast(bf16x8, p0), o);
        o = MFMA32(v1, __builtin_bit_cast(bf16x8, p1), o);
    }
}

DI bool attn_ok(const f32x16& a, const f32x16& b) {
    f32x2 s = {0.f, 0.f};
#pragma unroll
    for (int i = 0; i < 12; i += 2) s += f32x2{a[i], a[i + 1]} + f32x2{b[i], b[i + 1]};
    return (fmaxf(a[12], b[12]) < 0x1p60f) & (fabsf(s[0] + s[1] + a[12] + b[12]) < 0x1p126f);
}
DI void attn_pack(const f32x16& oa, const f32x16& ob, float ga, float gb, u32x4& va, u32x4& vb, u32x2& vt) {
    const float fa = ga * __builtin_amdgcn_rcpf(oa[12]), fb = gb * __builtin_amdgcn_rcpf(ob[12]);
    va = u32x4{pk_bf16(oa[0] * fa, oa[1] * fa), pk_bf16(oa[2] * fa, oa[3] * fa), pk_bf16(oa[4] * fa, oa[5] * fa), pk_bf16(oa[6] * fa, oa[7] * fa)};
    vb = u32x4{pk_bf16(ob[0] * fb, ob[1] * fb), pk_bf16(ob[2] * fb, ob[3] * fb), pk_bf16(ob[4] * fb, ob[5] * fb), pk_bf16(ob[6] * fb, ob[7] * fb)};
    auto s0 = __builtin_amdgcn_permlane32_swap(pk_bf16(oa[8] * fa, oa[9] * fa), pk_bf16(ob[8] * fb, ob[9] * fb), false, false);
    auto s1 = __builtin_amdgcn_permlane32_swap(pk_bf16(oa[10] * fa, oa[11] * fa), pk_bf16(ob[10] * fb, ob[11] * fb), false, false);
    vt = u32x2{s0[0], s1[0]};
}

typedef __attribute__((ext_vector_type(16))) unsigned u32x16;
#define WSUB(v, i) __builtin_bit_cast(bf16x8, __builtin_shufflevector(v, v, 4 * (i), 4 * (i) + 1, 4 * (i) + 2, 4 * (i) + 3))

__global__ __launch_bounds__(512, 2) void k_attn(char* __restrict__ ws, char* __restrict__ out) {
    __shared__ __attribute__((aligned(16))) char lds[163840];
    const int b = blockIdx.x, xcd = b & 7, ii = b >> 3;
    const int pos = ii >> 2, head = (pos >> 2) * 32 + xcd * 4 + (pos & 3), slot = xcd * 8 + pos, qb = ii & 3;
    const int w = __builtin_amdgcn_readfirstlane(threadIdx.x >> 6);
    int l = threadIdx.x & 63, r32 = l & 31, h = l >> 5;
    const int qc = qb * 8 + w;
    const unsigned lds0 = (unsigned)(uintptr_t)lds;
    const unsigned qo = (unsigned)WS_Q + head * (unsigned)Q_HEAD + qc * 2560, kvo = (unsigned)WS_KV + (head < 32 ? 32 : head - 32) * (unsigned)KV_HEAD;
    bf16x8 q1a, q1b, k1f, k2f; s16x4 q2; float ga, gb;
    unsigned dv = kvo + w * 4096 + l * 16;
    {
        unsigned sv, dm = __builtin_amdgcn_readfirstlane(lds0 + w * 4096);
        asm volatile(
            "global_load_dwordx4 v[4:7], %[a0], %[kvp]\n\tglobal_load_dwordx4 v[8:11], %[a0], %[kvp] offset:1024\n\tglobal_load_dwordx2 v[12:13], %[a1], %[kvp] offset:2048\n\t"
            "global_load_dwordx4 " KA_ ", %[a2], %[kvp]\n\tglobal_load_dwordx4 v[" STR(KBR) ":" STR(KBR) "+3], %[a3], %[kvp] offset:2048\n\t"
            "global_load_dword v14, %[a4], %[kvp]\n\tglobal_load_dword v15, %[a4], %[kvp] offset:128\n\t"
            "s_mov_b32 %[sv], m0\n\t" DMA4 "s_mov_b32 m0, %[sv]"
            : "=&{v[4:7]}"(q1a), "=&{v[8:11]}"(q1b), "=&{v[12:13]}"(q2), "=&{v14}"(ga), "=&{v15}"(gb), "=&{v[168:171]}"(k1f), "=&{v[172:175]}"(k2f),
              [sv] "=&s"(sv), [dv] "+v"(dv), [dm] "+s"(dm)
            : [kvp] "s"(ws), [a0] "v"(qo + l * 16), [a1] "v"(qo + r32 * 16 + h * 8), [a2] "v"(kvo + l * 16), [a3] "v"(kvo + r32 * 16), [a4] "v"((unsigned)WS_G + qc * 256 + swap34(r32) * 4)
            : "memory", "scc");
    }
    const int mb = xcd * 4 + (ii & 3), nb = ii >> 2;
    const char* wp = ws + WS_WO + (size_t)(nb * 8 + w) * 25600 + 2048;
    const char* cp = ws + WS_CNT + (((mb >> 3) * 8 + w) * 8 + nb) * 128;
    const char* sgp = ws + WS_CNT + (qb * 8 + xcd) * 8 * 128;
    const char* a1 = ws + WS_O1 + (size_t)w * 8 * 65536 + mb * 2048;
    const char* a2 = ws + WS_O2 + (size_t)w * 4 * 32768 + mb * 1024;
    const char* op = out + (size_t)mb * 64 * 1280 + nb * 160;
    const char* po1 = ws + WS_O1 + (size_t)slot * 65536 + qc * 2048;
    const char* po2 = ws + WS_O2 + (size_t)(slot >> 1) * 32768 + qc * 1024 + (slot & 1) * 8;
    f32x16 oa, ob; u32x16 vini;
#pragma unroll
    for (int i = 0; i < 16; ++i) { oa[i] = 0.f; ob[i] = 0.f; vini[i] = (r32 & 27) == 24 ? 0x3f803f80u : 0u; }
    unsigned ak1 = lds0 + l * 16, ak2 = lds0 + 2048 + r32 * 16, av = lds0 + 2560 + h * 320 + r32 * 16, wv = l * 16;
    unsigned cnt, inc, svm, dm = __builtin_amdgcn_readfirstlane(lds0 + w * 4096 + 32768);
    u32x16 wt0, wt1, wt2, wt3, wt4;
    asm volatile(
        "s_mov_b32 %[sv], m0\n\t"
        ".irp n,24,25,26,27\n\tv_mov_b32 v[" STR(S1R) "+\\n], 0\n\t.endr\n\t"
        "s_mov_b32 %[cnt], 0\n\ts_mov_b32 %[inc], 20480\n\ts_mov_b64 vcc, -1\n\t"
        "s_waitcnt vmcnt(6)\n\t"
        "v_mfma_f32_32x32x4_2b_bf16 v[" STR(S0R) ":" STR(S0R) "+31], " KB0_ ", %[q2], 0\n\ts_nop 15\n\ts_nop 1\n\t"
        M2_(S0R, 0, q1a) M2_(S0R, 16, q1b)
        "s_nop 10\n\t"
        MX3(0, S0R + 0, 1) MX3(16, S0R + 16, 17) MX3(0, NMR, 3) MX3(16, NMR + 16, 19) MX3(0, NMR, 5) MX3(16, NMR + 16, 21) MX3(0, NMR, 7) MX3(16, NMR + 16, 23)
        MX3(0, NMR, 9) MX3(16, NMR + 16, 25) MX3(0, NMR, 11) MX3(16, NMR + 16, 27) MX3(0, NMR, 13) MX3(16, NMR + 16, 29) MX3(0, NMR, 14) MX3(16, NMR + 16, 30)
        "v_mov_b32 v[" STR(NMR) "+1], v[" STR(NMR) "]\n\tv_mov_b32 v[" STR(NMR) "+17], v[" STR(NMR) "+16]\n\ts_nop 1\n\t"
        "v_permlane32_swap_b32 v[" STR(NMR) "], v[" STR(NMR) "+1]\n\tv_permlane32_swap_b32 v[" STR(NMR) "+16], v[" STR(NMR) "+17]\n\ts_nop 1\n\t"
        "v_min_f32 v[" STR(NMR) "], -v[" STR(NMR) "], -v[" STR(NMR) "+1]\n\tv_min_f32 v[" STR(NMR) "+16], -v[" STR(NMR) "+16], -v[" STR(NMR) "+17]\n\t"
        "v_mov_b32 v[" STR(NMR) "+1], v[" STR(NMR) "]\n\tv_mov_b32 v[" STR(NMR) "+17], v[" STR(NMR) "+16]\n\t"
        ".irp n,0,2,4,6,8,10,12,14\n\tv_pk_add_f32 v[" STR(S0R) "+\\n:" STR(S0R) "+\\n+1], v[" STR(S0R) "+\\n:" STR(S0R) "+\\n+1], v[" STR(NMR) ":" STR(NMR) "+1]\n\t"
        "v_pk_add_f32 v[" STR(S0R) "+16+\\n:" STR(S0R) "+17+\\n], v[" STR(S0R) "+16+\\n:" STR(S0R) "+17+\\n], v[" STR(NMR) "+16:" STR(NMR) "+17]\n\t.endr\n\t"
        ".irp n,2,4,6,8,10,12,14\n\tv_mov_b64 v[" STR(NMR) "+\\n:" STR(NMR) "+\\n+1], v[" STR(NMR) ":" STR(NMR) "+1]\n\t"
        "v_mov_b64 v[" STR(NMR) "+16+\\n:" STR(NMR) "+17+\\n], v[" STR(NMR) "+16:" STR(NMR) "+17]\n\t.endr\n\t"
        "s_waitcnt vmcnt(0)\n\ts_barrier\n\t"
        RDKA(1024) RDV(VAR, 0)
        "1:\n\t"
        TILE(S0R, S1R, VAR, VBR, 5120, KB1_, RDKB(5120), 1280, PRIO(3) "s_cbranch_vccnz 11f\n\t21:\n\t")
        TILE(S1R, S0R, VBR, VAR, 5120 + 1024, KB0_, "", 5120, "s_cbranch_vccnz 12f\n\t22:\n\t")
        TILE(S0R, S1R, VAR, VBR, 10240, KB1_, RDKB(10240), 5120 + 1280, "s_cbranch_vccnz 13f\n\t23:\n\t")
        TILE(S1R, S0R, VBR, VAR, 10240 + 1024, KB0_, "", 10240, "s_cbranch_vccnz 14f\n\t24:\n\t")
        TILE(S0R, S1R, VAR, VBR, 15360, KB1_, RDKB(15360), 10240 + 1280, "")
        TILE(S1R, S0R, VBR, VAR, 15360 + 1024, KB0_, "", 15360, PRIO(2) "s_cmp_eq_u32 %[cnt], 7\n\ts_cselect_b32 %[inc], -143360, %[inc]\n\t")
        TILE(S0R, S1R, VAR, VBR, 0, KB1_, RDKB(0), 15360 + 1280, PRIO(1) "v_add_u32 %[ak1], %[inc], %[ak1]\n\tv_add_u32 %[ak2], %[inc], %[ak2]\n\t")
        TILE(S1R, S0R, VBR, VAR, 1024, KB0_, "", 0, PRIO(0) "v_add_u32 %[av], %[inc], %[av]\n\t")
        "s_add_u32 %[cnt], %[cnt], 1\n\t"
        "s_cmp_eq_u32 %[cnt], 1\n\ts_cbranch_scc1 2f\n\t"
        "s_cmp_eq_u32 %[cnt], 3\n\ts_cbranch_scc1 3f\n\t"
        "s_cmp_eq_u32 %[cnt], 4\n\ts_cbranch_scc1 5f\n\t"
        "s_cmp_eq_u32 %[cnt], 5\n\ts_cbranch_scc1 6f\n\t"
        "s_cmp_eq_u32 %[cnt], 6\n\ts_cbranch_scc1 8f\n\t"
        "s_cmp_lt_u32 %[cnt], 8\n\ts_cbranch_scc1 1b\n\t"
        "s_branch 4f\n\t"
        "11:\n\t" DMA4 "s_branch 21b\n\t"
        "12:\n\t" DMA4 "s_branch 22b\n\t"
        "13:\n\t" DMA4 "s_branch 23b\n\t"
        "14:\n\t" DMA4 "s_branch 24b\n\t"
        "2:\n\ts_mov_b64 vcc, 0\n\ts_waitcnt vmcnt(0)\n\ts_barrier\n\ts_branch 1b\n\t"
        "3:\n\t" WLD(0)
        "5:\n\t" WLD(1)
        "6:\n\t" WLD(2)
        "8:\n\tv_and_b32 %[dv], 0x3ff, %[wv]\n\tv_lshlrev_b32 %[dv], 3, %[dv]\n\tv_min_u32 %[dv], 0x1380, %[dv]\n\tv_add_u32 %[dv], 0x5000, %[dv]\n\t"
        "global_load_dword %[dv], %[dv], %[wp] offset:-2048\n\t" WLD(3)
        "4:\n\ts_nop 1\n\t"
        PV_(ob, VBR, 4, S1R, 24)
        "s_mov_b32 m0, %[sv]\n\ts_waitcnt vmcnt(0) lgkmcnt(0)\n\ts_nop 15\n\ts_nop 7"
        : [oa] "+{v[24:39]}"(oa), [ob] "+{v[40:55]}"(ob), [ak1] "+v"(ak1), [ak2] "+v"(ak2), [av] "+v"(av), [dv] "+v"(dv), [wv] "+v"(wv),
          "+{v[152:167]}"(vini), "+{v[168:171]}"(k1f), "+{v[172:175]}"(k2f),
          [q1a] "+{v[4:7]}"(q1a), [q1b] "+{v[8:11]}"(q1b), [q2] "+{v[12:13]}"(q2), "+{v14}"(ga), "+{v15}"(gb),
          "=&{v[176:191]}"(wt0), "=&{v[192:207]}"(wt1), "=&{v[208:223]}"(wt2), "=&{v[224:239]}"(wt3), "=&{v[240:255]}"(wt4),
          [cnt] "=&s"(cnt), [inc] "=&s"(inc), [sv] "=&s"(svm), [dm] "+s"(dm)
        : [vmask] "s"(0x000fffff000fffffull), [kvp] "s"(ws), [wp] "s"(wp),
          "s"(cp), "s"(sgp), "s"(a1), "s"(a2), "s"(op), "s"(po1), "s"(po2)
        : "memory", "scc", "vcc", "v56", "v57", "v58", "v59", CL10(6), CL10(7), CL10(8), CL10(9), CL10(10), CL10(11), CL10(12), CL10(13), CL10(14), "v150", "v151"
          );
    asm volatile("v_mbcnt_lo_u32_b32 %0, -1, 0\n\tv_mbcnt_hi_u32_b32 %0, -1, %0" : "=v"(l));
    r32 = l & 31; h = l >> 5;
    bf16x8 wl[5], af[5][4]; unsigned seen;
    {
        if (head >= 32) { ga = 1.0f - ga; gb = 1.0f - gb; }
        u32x4 va, vb; u32x2 vt;
        attn_pack(oa, ob, ga, gb, va, vb, vt);
        if (__builtin_expect(!__all(attn_ok(oa, ob)), 0)) {
#pragma unroll 1
            for (int t = 0; t < 2; ++t) {
                f32x16 ro; u32x4 v0, v1; u32x2 v2;
                attn_fallback(lds, l, ws + qo, t, ro);
                attn_pack(ro, ro, t ? gb : ga, t ? gb : ga, v0, v1, v2);
                if (t == 0) va = v0; else vb = v0;
                if (h == t) vt = v2;
            }
        }
        asm volatile(
            "global_store_dwordx4 %[lo], %[va], %[o1] sc1\n\tglobal_store_dwordx4 %[lo], %[vb], %[o1] offset:1024 sc1\n\tglobal_store_dwordx2 %[lo], %[vt], %[o2] sc1\n\t"
            "s_nop 0"
            :: [lo] "v"(swap34(l) * 16), [va] "v"(va), [vb] "v"(vb), [vt] "v"(vt), [o1] "s"(po1), [o2] "s"(po2)
            : "memory");
    }
    const int r = l & 15, g = l >> 4, r8 = l & 7, g2 = (l >> 3) & 3;
    unsigned o1 = (g >> 1) * 65536 + (g & 1) * 512 + r * 16, o2 = g * 32768 + r * 16;
    unsigned wbo = w * 20480 + (g * 16 + (r ^ ((g >> 1) << 3))) * 16;
    unsigned rbo = ((w >> 1) * 5 + h) * 1024 + (g2 * 16 + ((8 * (w & 1) + r8) ^ ((g2 >> 1) << 3))) * 16;
    unsigned opo = (8 * w + r8) * 1280 + h * 32 + g2 * 8, sgo = l * 128;
    asm volatile("s_waitcnt vmcnt(0)\n\ts_barrier" : "+v"(o1), "+v"(o2), "+v"(wbo), "+v"(rbo), "+v"(opo), "+v"(sgo) :: "memory");
    if (w == (xcd ^ 1) && l < 8) __hip_atomic_fetch_add((__attribute__((address_space(1))) unsigned*)(sgp + sgo), 1u, __ATOMIC_RELAXED, __HIP_MEMORY_SCOPE_AGENT);
    asm volatile(
        "global_load_dwordx4 %0, %6, %7 offset:-2048\n\tglobal_load_dwordx4 %1, %6, %7 offset:-1024\n\tglobal_load_dwordx4 %2, %6, %7\n\t"
        "global_load_dwordx4 %3, %6, %7 offset:1024\n\tglobal_load_dwordx4 %4, %6, %7 offset:2048\n\tglobal_load_dword %5, %8, %9 sc1\n\ts_waitcnt vmcnt(0)"
        : "=&v"(wl[0]), "=&v"(wl[1]), "=&v"(wl[2]), "=&v"(wl[3]), "=&v"(wl[4]), "=&v"(seen) : "v"(wv), "s"(wp), "v"(0), "s"(cp) : "memory");
#pragma unroll 1
    for (int spin = 0; spin < 400000 && __builtin_amdgcn_readfirstlane(seen) < 8; ++spin) {
        __builtin_amdgcn_s_sleep(4);
        asm volatile("global_load_dword %0, %1, %2 sc1\n\ts_waitcnt vmcnt(0)" : "=v"(seen) : "v"(0), "s"(cp) : "memory");
    }
#define ALD(s, base, off, d1, d2) asm volatile( \
        "global_load_dwordx4 %0, %4, %5 sc1\n\tglobal_load_dwordx4 %1, %4, %5 offset:" #d1 " sc1\n\t" \
        "global_load_dwordx4 %2, %4, %5 offset:" #d2 " sc1\n\tglobal_load_dwordx4 %3, %4, %5 offset:" #d1 "+" #d2 " sc1" \
        : "=&v"(af[s][0]), "=&v"(af[s][1]), "=&v"(af[s][2]), "=&v"(af[s][3]) : "v"(off), "s"(base) : "memory")
#define AWAIT(s, N) asm volatile("s_waitcnt vmcnt(" STR(N) ")" : "+v"(af[s][0]), "+v"(af[s][1]), "+v"(af[s][2]), "+v"(af[s][3]) :: "memory")
#define OMM(s, w0, w1, w2, w3, w4) do { const bf16x8 wf_[5] = {w0, w1, w2, w3, w4}; \
        _Pragma("unroll") for (int j = 0; j < 5; ++j) _Pragma("unroll") for (int i = 0; i < 4; ++i) acc[i][j] = MFMA16(wf_[j], af[s][i], acc[i][j]); \
        __builtin_amdgcn_sched_barrier(0); } while (0)
    f32x4 acc[4][5];
#pragma unroll
    for (int i = 0; i < 4; ++i)
#pragma unroll
        for (int j = 0; j < 5; ++j) acc[i][j] = f32x4{0.f, 0.f, 0.f, 0.f};
    ALD(0, a1, o1, 256, 1024); ALD(1, a1 + 131072, o1, 256, 1024); ALD(2, a1 + 2 * 131072, o1, 256, 1024);
    AWAIT(0, 8); ALD(3, a1 + 3 * 131072, o1, 256, 1024); OMM(0, WSUB(wt0, 0), WSUB(wt0, 1), WSUB(wt0, 2), WSUB(wt0, 3), WSUB(wt1, 0));
    AWAIT(1, 8); ALD(4, a2, o2, 256, 512); OMM(1, WSUB(wt1, 1), WSUB(wt1, 2), WSUB(wt1, 3), WSUB(wt2, 0), WSUB(wt2, 1));
    AWAIT(2, 8); OMM(2, WSUB(wt2, 2), WSUB(wt2, 3), WSUB(wt3, 0), WSUB(wt3, 1), WSUB(wt3, 2));
    AWAIT(3, 4); OMM(3, WSUB(wt3, 3), WSUB(wt4, 0), WSUB(wt4, 1), WSUB(wt4, 2), WSUB(wt4, 3));
    AWAIT(4, 0);
#pragma unroll
    for (int j = 0; j < 6; ++j) {
        if (j < 5) {
#pragma unroll
            for (int i = 0; i < 4; ++i) acc[i][j] = MFMA16(wl[j], af[4][i], acc[i][j]);
        }
        if (j > 0) {
#pragma unroll
            for (int i = 0; i < 4; ++i) *(f32x4*)(lds + wbo + (i * 5 + j - 1) * 1024) = acc[i][j - 1];
        }
        __builtin_amdgcn_sched_barrier(0);
    }
    {
        __syncthreads();
        f32x4 t[3][8];
#pragma unroll
        for (int u = 0; u < 8; ++u)
#pragma unroll
            for (int p = 0; p < 3; ++p) t[p][u] = *(const f32x4*)(lds + rbo + u * 20480 + (p < 2 ? p * 2048 : 4096 - h * 1024));
        __builtin_amdgcn_sched_barrier(0);
#pragma unroll
        for (int p = 0; p < 3; ++p) {
            const f32x4 z = ((t[p][0] + t[p][1]) + (t[p][2] + t[p][3])) + ((t[p][4] + t[p][5]) + (t[p][6] + t[p][7]));
            const u32x2 v = {pk_bf16(z[0], z[1]), pk_bf16(z[2], z[3])};
            if (p < 2 || h == 0) asm volatile("global_store_dwordx2 %0, %1, %2 offset:%3" :: "v"(opo), "v"(v), "s"(op), "i"(p * 64) : "memory");
        }
    }
}

extern "C" void kernel_launch(void* const* d_in, const int* in_sizes, int n_in, void* d_out, int out_size, void* d_ws, size_t ws_size,
                              hipStream_t stream) {
    const char* x = (const char*)d_in[0];
    const char* wq = (const char*)d_in[1];
    const char* wk = (const char*)d_in[2];
    const char* wv = (const char*)d_in[3];
    const char* woa = (const char*)d_in[4];
    const char* wqkv = (const char*)d_in[6];
    const char* wob = (const char*)d_in[7];
    const char* gw = (const char*)d_in[8];
    char* ws = (char*)d_ws;
    k_proj<<<256, 768, 0, stream>>>(x, wq, wqkv, wk, wv, gw, woa, wob, ws);
    k_attn<<<256, 512, 0, stream>>>(ws, (char*)d_out);
}
```

```cpp
#include <hip/hip_runtime.h>
#include <hip/hip_bf16.h>
#include <stdint.h>

typedef __attribute__((ext_vector_type(8))) short bf16x8;
typedef __attribute__((ext_vector_type(4))) short s16x4;
typedef __attribute__((ext_vector_type(4))) float f32x4;
typedef __attribute__((ext_vector_type(16))) float f32x16;
typedef __attribute__((ext_vector_type(2))) unsigned u32x2;
typedef __attribute__((ext_vector_type(4))) unsigned u32x4;
typedef __attribute__((ext_vector_type(2))) __bf16 bf16x2_t;
typedef __attribute__((ext_vector_type(2))) float f32x2;
#define LDSP __attribute__((address_space(3)))
#define DI __device__ __forceinline__

constexpr int DM = 640;
constexpr size_t Q_HEAD = 32 * 2560;
constexpr size_t KV_HEAD = 32 * 5120;
constexpr size_t WS_Q = 0;
constexpr size_t WS_KV = WS_Q + 64 * Q_HEAD;
constexpr size_t WS_G = WS_KV + 33 * KV_HEAD;
constexpr size_t WS_O1 = WS_G + 2048 * 4;
constexpr size_t WS_O2 = WS_O1 + 64 * 65536;
constexpr size_t WS_WO = WS_O2 + 32 * 2048 * 16;
constexpr size_t WS_CNT = WS_WO + 1600 * 1024;

DI unsigned pk_bf16(float a, float b) {
    f32x2 v = {a, b};
    return __builtin_bit_cast(unsigned, __builtin_convertvector(v, bf16x2_t));
}
DI void st16(char* p, u32x4 v) { asm volatile("global_store_dwordx4 %0, %1, off sc1\n\ts_nop 1" :: "v"(p), "v"(v) : "memory"); }
DI void st8(char* p, u32x2 v) { asm volatile("global_store_dwordx2 %0, %1, off sc1" :: "v"(p), "v"(v) : "memory"); }
DI void st4(char* p, unsigned v) { asm volatile("global_store_dword %0, %1, off sc1" :: "v"(p), "v"(v) : "memory"); }
DI s16x4 tr_read(const LDSP char* p) {
    typedef short v4i16_t __attribute__((ext_vector_type(4)));
    return __builtin_amdgcn_ds_read_tr16_b64_v4i16((LDSP v4i16_t*)p);
}
DI bf16x8 cat8(s16x4 lo, s16x4 hi) { return __builtin_shufflevector(lo, hi, 0, 1, 2, 3, 4, 5, 6, 7); }
#define MFMA16(a, b, c) __builtin_amdgcn_mfma_f32_16x16x32_bf16((a), (b), (c), 0, 0, 0)
#define MFMA32(a, b, c) __builtin_amdgcn_mfma_f32_32x32x16_bf16((a), (b), (c), 0, 0, 0)

constexpr int P_SLOT = 40960;

DI void proj_loader(char* lds, const char* xs, const char* ws, const char* es, int sw, int se, unsigned long long m5, int st, int ekind, int ehalf, int mb, bool vperm,
                    const char* woa, const char* wob, char* wsp, const unsigned (&xo)[2], const unsigned (&wo)[3]) {
    const int l = threadIdx.x & 63, w = __builtin_amdgcn_readfirstlane(threadIdx.x >> 6), r = l & 15, g = l >> 4;
    const unsigned dw = __builtin_amdgcn_readfirstlane((unsigned)(uintptr_t)lds + w * 1024);
    auto issue = [&](int slot) {
        const unsigned d0 = dw + slot * P_SLOT;
        unsigned sv;
        asm volatile(
            "s_mov_b32 %[sv], m0\n\ts_mov_b32 m0, %[d0]\n\ts_nop 0\n\t"
            "global_load_lds_dwordx4 %[x0], %[xs]\n\ts_add_u32 m0, m0, 8192\n\ts_nop 0\n\t"
            "global_load_lds_dwordx4 %[x1], %[xs]\n\ts_add_u32 m0, m0, 8192\n\ts_nop 0\n\t"
            "global_load_lds_dwordx4 %[w0], %[ws]\n\ts_add_u32 m0, m0, 8192\n\ts_nop 0\n\t"
            "global_load_lds_dwordx4 %[w1], %[ws]\n\ts_add_u32 m0, m0, 8192\n\ts_mov_b64 exec, %[m5]\n\t"
            "global_load_lds_dwordx4 %[w2], %[es]\n\ts_mov_b64 exec, -1\n\ts_mov_b32 m0, %[sv]"
            : [sv] "=&s"(sv)
            : [d0] "s"(d0), [m5] "s"(m5), [xs] "s"(xs), [ws] "s"(ws), [es] "s"(es),
              [x0] "v"(xo[0]), [x1] "v"(xo[1]), [w0] "v"(wo[0]), [w1] "v"(wo[1]), [w2] "v"(wo[2])
            : "memory", "scc");
        const int d = st == 9 ? -9 : 1;
        st += d; xs += d * 128; ws += d * sw; es += d * se;
    };
    issue(0); issue(1);
    __builtin_amdgcn_s_setprio(3);
    f32x4 eacc = {0.f, 0.f, 0.f, 0.f};
    const int ets = 4 * ehalf + w;
    if (w >= 4) ekind = 5;
    const int etok = 16 * ets + r, erow = vperm ? (etok & 99) | ((etok & 4) << 2) | ((etok & 24) >> 1) : etok;
    const int exr = erow * 128 + ((g ^ ((erow >> 1) & 7)) << 4);
    const int ewr = ekind < 4 ? 36864 + (8 * g + (r >> 2)) * 40 + ((ekind & 1) ? 32 : 8 * (r & 3)) : 36864 + g * 16;
    auto extra = [&](int k) {
        if (ekind < 5) {
            const LDSP char* sb = (const LDSP char*)lds + (k & 3) * P_SLOT;
            const bf16x8 x0 = *(const LDSP bf16x8*)(sb + exr), x1 = *(const LDSP bf16x8*)(sb + (exr ^ 64));
            bf16x8 w0, w1;
            if (ekind < 4) { w0 = cat8(tr_read(sb + ewr), tr_read(sb + ewr + 160)); w1 = cat8(tr_read(sb + ewr + 1280), tr_read(sb + ewr + 1440)); }
            else { w0 = *(const LDSP bf16x8*)(sb + ewr); w1 = *(const LDSP bf16x8*)(sb + ewr + 64); }
            if ((ekind >> 1) == 1) { eacc = MFMA16(x0, w0, eacc); eacc = MFMA16(x1, w1, eacc); }
            else { eacc = MFMA16(w0, x0, eacc); eacc = MFMA16(w1, x1, eacc); }
            asm volatile("" : "+v"(eacc));
        }
    };
    const int gw = blockIdx.x * 8 + w, fid = gw < 1600 ? gw : 1599;
    const int fj = fid % 5, fs = (fid / 5) % 5, fg = (fid / 25) & 7, fn = fid / 200;
    const int br = fs < 4 ? fs >> 1 : g >> 1;
    const int row = fs < 4 ? (4 * fg + 2 * (fs & 1) + (g >> 1)) * 20 + 4 * (g & 1) : (4 * fg + 2 * (g & 1)) * 20 + 16;
    const char* p0 = (br ? wob : woa) + row * 1280 + (80 * fn + 16 * fj + r) * 2;
    const char* p1 = p0 + (fs < 4 ? 8 : 20) * 1280;
    asm volatile("" : "+v"(p0), "+v"(p1));
#pragma unroll 1
    for (int k = 0; k < 8; ++k) {
        asm volatile("s_waitcnt vmcnt(5)\n\ts_barrier" ::: "memory");
        issue((k + 2) & 3);
        if (k < 7) extra(k);
    }
    unsigned e0, e1, e2, e3, e4, e5, e6, e7;
    asm volatile(
        "global_load_ushort %0, %8, off\n\tglobal_load_ushort %1, %8, off offset:1280\n\tglobal_load_ushort %2, %8, off offset:2560\n\tglobal_load_ushort %3, %8, off offset:3840\n\t"
        "global_load_ushort %4, %9, off\n\tglobal_load_ushort %5, %9, off offset:1280\n\tglobal_load_ushort %6, %9, off offset:2560\n\tglobal_load_ushort %7, %9, off offset:3840"
        : "=&v"(e0), "=&v"(e1), "=&v"(e2), "=&v"(e3), "=&v"(e4), "=&v"(e5), "=&v"(e6), "=&v"(e7) : "v"(p0), "v"(p1) : "memory");
    extra(7);
    asm volatile("s_waitcnt vmcnt(13)\n\ts_barrier" ::: "memory");
    extra(8);
    asm volatile("s_waitcnt vmcnt(8)\n\ts_barrier" ::: "memory");
    extra(9);
    asm volatile("s_waitcnt vmcnt(0)" : "+v"(e0), "+v"(e1), "+v"(e2), "+v"(e3), "+v"(e4), "+v"(e5), "+v"(e6), "+v"(e7) :: "memory");
    {
        char* kva = wsp + WS_KV + 32 * KV_HEAD + (mb * 2 + ehalf) * 5120;
        const u32x2 v = {pk_bf16(eacc[0], eacc[1]), pk_bf16(eacc[2], eacc[3])};
        const int t = (w >> 1) & 1, krow = 16 * (w & 1) + r;
        if (ekind == 0) st8(kva + t * 1024 + (g >> 1) * 512 + krow * 16 + (g & 1) * 8, v);
        else if (ekind == 1) { if (g == 0) st8(kva + 2048 + krow * 16 + t * 8, v); }
        else if (ekind < 4) { if (ekind == 2 || r < 4) st8(kva + 2560 + t * 1280 + (2 * (w & 1) + (g >> 1)) * 320 + ((ekind & 1) * 16 + r) * 16 + (g & 1) * 8, v); }
        else if (ekind == 4) { if (g == 0) st4(wsp + WS_G + (mb * 128 + 16 * ets + r) * 4, __float_as_uint(1.0f / (1.0f + __expf(-eacc[0])))); }
    }
    if (gw < 1600) st16(wsp + WS_WO + (size_t)fid * 1024 + l * 16, u32x4{e0 | (e1 << 16), e2 | (e3 << 16), e4 | (e5 << 16), e6 | (e7 << 16)});
    if (w == 0 && l == 0) st4(wsp + WS_CNT + blockIdx.x * 128, 0);
}

#define STR_(x) #x
#define STR(x) STR_(x)
#define PR4(b) "v[" STR(b) ":" STR(b) "+3]"
#define PM_F(j, i, X) "v_mfma_f32_16x16x32_bf16 " PR4(20 + 16 * j + 4 * i) ", " PR4(136 + 4 * j) ", " PR4(X + 4 * i) ", " PR4(20 + 16 * j + 4 * i) "\n\t"
#define PM_T(j, i, X) "v_mfma_f32_16x16x32_bf16 " PR4(20 + 16 * j + 4 * i) ", " PR4(X + 4 * i) ", " PR4(136 + 4 * j) ", " PR4(20 + 16 * j + 4 * i) "\n\t"
#define PTL(j, sl, ks) "ds_read_b64_tr_b16 v[136+4*" #j ":136+4*" #j "+1], %[aw" #j "] offset:" #sl "*40960+" #ks "*10240\n\t"
#define PTH(j, sl, ks) "ds_read_b64_tr_b16 v[136+4*" #j "+2:136+4*" #j "+3], %[aw" #j "] offset:" #sl "*40960+" #ks "*10240+1280\n\t"
#define PBX(X, i, sl, ks) "ds_read_b128 " PR4(X + 4 * i) ", %[ax" #ks "] offset:" #sl "*40960+" #i "*2048\n\t"
#define PTG(a) "v_add_u32 %[" #a "], %[tg], %[" #a "]\n\t"
#define PSTEP(M, X, Y, cs, ck, ns, nk, BAR, F0, F1, F2, F3) \
    M(0, 0, X) PTL(4, cs, ck) M(0, 1, X) PTH(4, cs, ck) M(0, 2, X) BAR PBX(Y, 0, ns, nk) M(0, 3, X) PBX(Y, 1, ns, nk) \
    M(1, 0, X) PTL(0, ns, nk) M(1, 1, X) PTH(0, ns, nk) M(1, 2, X) PBX(Y, 2, ns, nk) M(1, 3, X) "s_waitcnt lgkmcnt(5)\n\t" \
    M(2, 0, X) PTL(1, ns, nk) M(2, 1, X) PTH(1, ns, nk) M(2, 2, X) PBX(Y, 3, ns, nk) M(2, 3, X) F0 \
    M(3, 0, X) PTL(2, ns, nk) M(3, 1, X) PTH(2, ns, nk) M(3, 2, X) F1 M(3, 3, X) F2 \
    M(4, 0, X) PTL(3, ns, nk) M(4, 1, X) PTH(3, ns, nk) M(4, 2, X) F3 M(4, 3, X) "s_waitcnt lgkmcnt(4)\n\t"
#define CL10(a) "v" #a "0", "v" #a "1", "v" #a "2", "v" #a "3", "v" #a "4", "v" #a "5", "v" #a "6", "v" #a "7", "v" #a "8", "v" #a "9"
#define PLOOP(M) \
    asm volatile( \
        "s_mov_b32 %[cnt], 0\n\ts_mov_b32 %[tg], 81920\n\t" \
        "s_barrier\n\t" \
        PBX(104, 0, 0, 0) PBX(104, 1, 0, 0) PTL(0, 0, 0) PTH(0, 0, 0) PBX(104, 2, 0, 0) PTL(1, 0, 0) PTH(1, 0, 0) PBX(104, 3, 0, 0) \
        PTL(2, 0, 0) PTH(2, 0, 0) PTL(3, 0, 0) PTH(3, 0, 0) "s_waitcnt lgkmcnt(4)\n\t" \
        "1:\n\t" \
        PSTEP(M, 104, 120, 0, 0, 0, 1, "", "", "", "", "") \
        PSTEP(M, 120, 104, 0, 1, 1, 0, "s_barrier\n\t", PTG(ax0), "s_add_u32 %[cnt], %[cnt], 1\n\t", "", "") \
        PSTEP(M, 104, 120, 1, 0, 1, 1, "", PTG(aw0), PTG(aw1), PTG(aw2), PTG(ax1)) \
        PSTEP(M, 120, 104, 1, 1, 0, 0, "s_cmp_eq_u32 %[cnt], 5\n\ts_cbranch_scc1 2f\n\ts_barrier\n\t2:\n\t", PTG(aw3), PTG(aw4), "s_sub_u32 %[tg], 0, %[tg]\n\t", \
              "s_cmp_lg_u32 %[cnt], 5\n\t") \
        "s_cbranch_scc1 1b\n\t" \
        "s_waitcnt lgkmcnt(0)\n\ts_nop 7" \
        : "+{v[20:35]}"(acc[0]), "+{v[36:51]}"(acc[1]), "+{v[52:67]}"(acc[2]), "+{v[68:83]}"(acc[3]), "+{v[84:99]}"(acc[4]), \
          [ax0] "+v"(ax0), [ax1] "+v"(ax1), [aw0] "+v"(aw[0]), [aw1] "+v"(aw[1]), [aw2] "+v"(aw[2]), [aw3] "+v"(aw[3]), [aw4] "+v"(aw[4]), \
          [cnt] "=&s"(cnt), [tg] "=&s"(tg) \
        : \
        : "memory", "scc", "v104", "v105", "v106", "v107", "v108", "v109", CL10(11), CL10(12), CL10(13), CL10(14), "v150", "v151", "v152", "v153", "v154", "v155")

template <bool VT>
DI void proj_mma(unsigned lds0, f32x16 (&acc)[5]) {
    const int tid = threadIdx.x, l = tid & 63, w = __builtin_amdgcn_readfirstlane(tid >> 6) - 8;
    const int wm = w >> 1, wn = w & 1, r = l & 15, g = l >> 4;
    const int xsw = (g ^ ((r >> 1) & 7)) << 4;
    unsigned ax0 = lds0 + (64 * wm + r) * 128 + xsw, ax1 = lds0 + (64 * wm + r) * 128 + (xsw ^ 64);
    unsigned aw[5];
    const int q = r >> 2, p = r & 3;
#pragma unroll
    for (int j = 0; j < 5; ++j) {
        int col;
        if (j == 4) col = (4 * wn + p) * 20 + 16;
        else if (VT) col = (4 * wn + j) * 20 + 4 * p;
        else col = (4 * wn + (j & 2) + (p >> 1)) * 20 + 8 * (p & 1) + 4 * (j & 1);
        aw[j] = lds0 + 16384 + (8 * g + q) * 320 + ((col * 2) ^ ((g & 1) << 5));
    }
    unsigned cnt, tg;
    if constexpr (VT) PLOOP(PM_T); else PLOOP(PM_F);
}
#define ACC(i, j) f32x4{acc[j][4 * (i)], acc[j][4 * (i) + 1], acc[j][4 * (i) + 2], acc[j][4 * (i) + 3]}

DI u32x4 pk8(const f32x4& a, const f32x4& b, float sc) {
    return u32x4{pk_bf16(a[0] * sc, a[1] * sc), pk_bf16(a[2] * sc, a[3] * sc), pk_bf16(b[0] * sc, b[1] * sc), pk_bf16(b[2] * sc, b[3] * sc)};
}

#define CODE_PROJ "_Z6k_projPKcS0_S0_S0_S0_S0_S0_S0_Pc"
#define CODE_ATTN "_Z6k_attnPcS_"
#define CODE_BYTES(w) ((w) ? 6912 : 8704)
__global__ __launch_bounds__(768, 3) void k_proj(const char* __restrict__ x, const char* __restrict__ wq, const char* __restrict__ wqkv,
                                                 const char* __restrict__ wk, const char* __restrict__ wv, const char* __restrict__ gw,
                                                 const char* __restrict__ woa, const char* __restrict__ wob, char* __restrict__ ws) {
    __shared__ __attribute__((aligned(16))) char lds[4 * P_SLOT];
    const int b = blockIdx.x, xcd = b & 7, ii = b >> 3;
    const int mb = (xcd & 1) * 8 + (ii & 7), type = xcd >> 1, nq = ii >> 3;
    const int tid = threadIdx.x, l = tid & 63, wv_ = __builtin_amdgcn_readfirstlane(tid >> 6);
    const int ldw2 = type == 0 ? 1280 : 3840;
    const int eidx = ((nq >> 1) | ((nq & 1) << 1)) * 4 + type, ehalf = eidx >= 5, ekind = eidx < 10 ? eidx - 5 * ehalf : 5;
    int rot = (ii & 7) + nq + type; rot = 2 * (rot >= 10 ? rot - 10 : (rot >= 5 ? rot - 5 : rot));
    const int ebytes = ekind < 4 ? 2560 : 128, nbytes = ebytes - (wv_ - 4) * 1024;
    const unsigned long long m5 = ~0ull >> (nbytes >= 1024 ? 0 : (nbytes > 0 ? 64 - (nbytes >> 4) : 63));
    const int sw = 64 * ldw2, se = wv_ < 4 ? sw : ebytes, wofs = (type > 1 ? (type - 1) * 1280 : 0) + nq * 320 + rot * sw;
    unsigned xo[2], wo[3];
    const int lrow = tid >> 3, xrow = type == 3 ? (lrow & 35) | ((lrow & 16) >> 2) | ((lrow & 12) << 1) : lrow;
    xo[0] = (mb * 128 + xrow) * (DM * 2) + (((tid ^ (tid >> 4)) & 7) << 4) + rot * 128; xo[1] = xo[0] + 64 * DM * 2;
#pragma unroll
    for (int n = 0; n < 3; ++n) { const int cid = tid + 512 * n, k = (cid * 3277) >> 16, c = cid - 20 * k; wo[n] = k * ldw2 + ((c ^ (((k >> 3) & 1) << 1)) << 4) + wofs; }
    if (tid >= 256) wo[2] = l * 16 + (nbytes > 0 ? (wv_ - 4) * 1024 : 0) + rot * ebytes;
    asm volatile("" : "+v"(xo[0]), "+v"(xo[1]), "+v"(wo[0]), "+v"(wo[1]), "+v"(wo[2])
                 : "s"(x), "s"(wq), "s"(wqkv), "s"(wk), "s"(wv), "s"(gw), "s"(woa), "s"(wob), "s"(ws), "s"(m5), "s"(se), "s"(ekind));
    if (__builtin_expect(wv_ < 8, 1)) {
        const char* wsel = type == 0 ? wq : wqkv;
        proj_loader(lds, x, wsel, wv_ < 4 ? wsel : (ekind < 2 ? wk : (ekind < 4 ? wv : gw)), sw, se, m5, rot, ekind, ehalf, mb, type == 3, woa, wob, ws, xo, wo);
        return;
    }
    const int w = wv_ - 8;
    if (w < 2) {
        const unsigned dp = (unsigned)(uintptr_t)lds + P_SLOT - 512, off = (ii * 3 + l) * 128; unsigned sv; unsigned long long pc;
        if (w == 0) asm volatile("s_getpc_b64 s[40:41]\n\ts_add_u32 s40, s40, " CODE_PROJ "@rel32@lo+4\n\ts_addc_u32 s41, s41, " CODE_PROJ "@rel32@hi+12" : "={s[40:41]}"(pc) :: "scc");
        else asm volatile("s_getpc_b64 s[40:41]\n\ts_add_u32 s40, s40, " CODE_ATTN "@rel32@lo+4\n\ts_addc_u32 s41, s41, " CODE_ATTN "@rel32@hi+12" : "={s[40:41]}"(pc) :: "scc");
        if (l < 3 && off < CODE_BYTES(w))
            asm volatile("s_mov_b32 %0, m0\n\ts_mov_b32 m0, %1\n\ts_nop 0\n\tglobal_load_lds_dword %2, %3\n\ts_mov_b32 m0, %0" : "=&s"(sv) : "s"(dp), "v"(off), "s"(pc) : "memory");
    }
    const int wm = w >> 1, wn = w & 1, r = l & 15, g = l >> 4;
    f32x16 acc[5];
#pragma unroll
    for (int j = 0; j < 5; ++j)
#pragma unroll
        for (int i = 0; i < 16; ++i) acc[j][i] = 0.f;
    const unsigned lds0 = (unsigned)(uintptr_t)lds;
    const int chunk = mb * 2 + wm;
#define ST16(voff, sbase, imm, val) asm volatile("global_store_dwordx4 %0, %1, %2 offset:%3 sc1\n\ts_nop 1" :: "v"(voff), "v"(val), "s"(sbase), "i"(imm) : "memory")
    if (type == 3) {
        const char* sb = ws + WS_KV + (size_t)(nq * 8 + 4 * wn) * KV_HEAD + chunk * 5120 + 2560;
        unsigned v1 = g * 320 + r * 16, v2 = (r >> 2) * (unsigned)KV_HEAD + g * 320 + (16 + (r & 3)) * 16;
        asm volatile("" : "+v"(v1), "+v"(v2) : "s"(sb), "s"(sb + KV_HEAD), "s"(sb + 2 * KV_HEAD), "s"(sb + 3 * KV_HEAD));
        proj_mma<true>(lds0, acc);
#pragma unroll
        for (int j = 0; j < 4; ++j) {
            ST16(v1, sb + j * KV_HEAD, 0, pk8(ACC(0, j), ACC(1, j), 1.0f));
            ST16(v1, sb + j * KV_HEAD, 1280, pk8(ACC(2, j), ACC(3, j), 1.0f));
        }
        ST16(v2, sb, 0, pk8(ACC(0, 4), ACC(1, 4), 1.0f));
        ST16(v2, sb, 1280, pk8(ACC(2, 4), ACC(3, 4), 1.0f));
    } else {
        const float sc = type == 2 ? 1.0f : 0.32259642f;
        const unsigned hstride = type == 2 ? KV_HEAD : Q_HEAD;
        const char* sb = ws + (type == 2 ? WS_KV + chunk * 5120 : WS_Q + type * 32 * Q_HEAD + chunk * 2560) + (nq * 8 + 4 * wn) * hstride;
        const unsigned row0 = r * 16;
        unsigned v1 = (g >> 1) * hstride + (g & 1) * 512 + row0, v2 = g * hstride + row0;
        asm volatile("" : "+v"(v1), "+v"(v2) : "s"(sb), "s"(sb + 2 * hstride));
        proj_mma<false>(lds0, acc);
#pragma unroll
        for (int jj = 0; jj < 2; ++jj)
#pragma unroll
            for (int i = 0; i < 4; ++i) ST16(v1, sb + jj * 2 * hstride, (i >> 1) * 1024 + (i & 1) * 256, pk8(ACC(i, 2 * jj), ACC(i, 2 * jj + 1), sc));
        ST16(v2, sb, 2048, pk8(ACC(0, 4), ACC(2, 4), sc));
        ST16(v2, sb, 2048 + 256, pk8(ACC(1, 4), ACC(3, 4), sc));
    }
}

#define S0R 56
#define S1R 88
#define NMR 120
#define VAR 152
#define VBR 160
#define KAR 168
#define KBR 172
#define WFR 176
#define E_(X, i) "v_exp_f32 v[" STR(X) "+" #i "], v[" STR(X) "+" #i "]\n\t"
#define PV_(O, V, vo, X, po) "v_smfmac_f32_32x32x32_bf16 %[" #O "], v[" STR(V) "+" #vo ":" STR(V) "+" #vo "+3], v[" STR(X) "+" #po ":" STR(X) "+" #po "+7], %[idx]\n\t"
#define M1_(Y, KB) "v_mfma_f32_32x32x4_2b_bf16 v[" STR(Y) ":" STR(Y) "+31], " KB ", %[q2], v[" STR(NMR) ":" STR(NMR) "+31]\n\t"
#define M2_(Y, o, Q) "v_mfma_f32_32x32x16_bf16 v[" STR(Y) "+" #o ":" STR(Y) "+" #o "+15], " KA_ ", %[" #Q "], v[" STR(Y) "+" #o ":" STR(Y) "+" #o "+15]\n\t"
#define KA_ "v[" STR(KAR) ":" STR(KAR) "+3]"
#define KB0_ "v[" STR(KBR) ":" STR(KBR) "+1]"
#define KB1_ "v[" STR(KBR) "+2:" STR(KBR) "+3]"
#define RDKA(off) "ds_read_b128 " KA_ ", %[ak1] offset:" STR(off) "\n\t"
#define RDKB(off) "ds_read_b128 v[" STR(KBR) ":" STR(KBR) "+3], %[ak2] offset:" STR(off) "\n\t"
#define RDV(V, off) "s_mov_b64 exec, %[vmask]\n\tds_read_b128 v[" STR(V) ":" STR(V) "+3], %[av] offset:" STR(off) "\n\t" \
                    "ds_read_b128 v[" STR(V) "+4:" STR(V) "+7], %[av] offset:" STR(off) "+640\n\ts_mov_b64 exec, -1\n\t"
#define MX3(d, a, i) "v_max3_f32 v[" STR(NMR) "+" #d "], v[" STR(a) "], v[" STR(S0R) "+" #i "], v[" STR(S0R) "+" #i "+1]\n\t"
#define PRIO(n) "s_setprio " #n "\n\t"
#define DMA4 "s_mov_b32 m0, %[dm]\n\ts_add_u32 %[dm], %[dm], 0x8000\n\tglobal_load_lds_dwordx4 %[dv], %[kvp]\n\tglobal_load_lds_dwordx4 %[dv], %[kvp] offset:1024\n\t" \
             "global_load_lds_dwordx4 %[dv], %[kvp] offset:2048\n\tglobal_load_lds_dwordx4 %[dv], %[kvp] offset:3072\n\tv_add_u32 %[dv], 0x8000, %[dv]\n\t"
#define WLD1(s, j) "global_load_dwordx4 v[" STR(WFR) "+20*" #s "+4*" #j ":" STR(WFR) "+20*" #s "+4*" #j "+3], %[wv], %[wp] offset:" #j "*1024-2048\n\t"
#define WLD(s) WLD1(s, 0) WLD1(s, 1) WLD1(s, 2) WLD1(s, 3) WLD1(s, 4) "v_add_u32 %[wv], 0x1400, %[wv]\n\ts_branch 1b\n\t"
#define E4_(X, a, b, c, d) E_(X, a) E_(X, b) E_(X, c) E_(X, d)
#define TILE(X, Y, VC, VP, KAOFF, KB, KBRD, VOFF, PRE) \
    PRE E_(X, 0) E_(X, 1) E_(X, 2) \
    PV_(ob, VP, 4, Y, 24) \
    E4_(X, 3, 4, 5, 6) \
    "s_waitcnt lgkmcnt(1)\n\t" M1_(Y, KB) KBRD \
    E4_(X, 7, 8, 9, 10) E4_(X, 11, 12, 13, 14) \
    PV_(oa, VC, 0, X, 0) RDV(VP, VOFF) \
    E4_(X, 15, 16, 17, 18) \
    PV_(oa, VC, 4, X, 8) \
    E4_(X, 19, 20, 21, 22) \
    "s_waitcnt lgkmcnt(2)\n\t" M2_(Y, 0, q1a) \
    E4_(X, 23, 24, 25, 26) \
    PV_(ob, VC, 0, X, 16) \
    E4_(X, 27, 28, 29, 30) \
    M2_(Y, 16, q1b) RDKA(KAOFF) \
    E_(X, 31)

DI float max_halves(float m) {
    auto rr = __builtin_amdgcn_permlane32_swap(__float_as_uint(m), __float_as_uint(m), false, false);
    return fmaxf(__uint_as_float(rr[0]), __uint_as_float(rr[1]));
}
DI void attn_fallback(const char* lds, int l, const char* qp, int tile, f32x16& o) {
    const int r32 = l & 31, h = l >> 5;
    const s16x4 z4 = {0, 0, 0, 0};
    const bf16x8 q1 = *(const bf16x8*)(qp + tile * 1024 + l * 16);
    const s16x4 q2 = *(const s16x4*)(qp + 2048 + r32 * 16 + tile * 8);
    const bf16x8 q2f = cat8(h == 0 ? q2 : z4, z4);
    const bf16x8 ones = {0x3f80, 0x3f80, 0x3f80, 0x3f80, 0x3f80, 0x3f80, 0x3f80, 0x3f80};
    float m = -INFINITY;
#pragma unroll
    for (int i = 0; i < 16; ++i) o[i] = 0.f;
#pragma unroll 1
    for (int t = 0; t < 64; ++t) {
        const LDSP char* cb = (const LDSP char*)lds + (t >> 1) * 5120;
        const bf16x8 k1 = *(const LDSP bf16x8*)(cb + (t & 1) * 1024 + l * 16);
        const s16x4 k2 = *(const LDSP s16x4*)(cb + 2048 + r32 * 16 + (t & 1) * 8);
        f32x16 s;
#pragma unroll
        for (int i = 0; i < 16; ++i) s[i] = 0.f;
        s = MFMA32(k1, q1, s);
        s = MFMA32(cat8(k2, z4), q2f, s);
        float tm = s[0];
#pragma unroll
        for (int i = 1; i < 16; ++i) tm = fmaxf(tm, s[i]);
        tm = max_halves(tm);
        const float mn = fmaxf(m, tm), alpha = __builtin_amdgcn_exp2f(m - mn);
        m = mn;
        u32x4 p0, p1;
#pragma unroll
        for (int i = 0; i < 4; ++i) {
            p0[i] = pk_bf16(__builtin_amdgcn_exp2f(s[2 * i] - m), __builtin_amdgcn_exp2f(s[2 * i + 1] - m));
            p1[i] = pk_bf16(__builtin_amdgcn_exp2f(s[8 + 2 * i] - m), __builtin_amdgcn_exp2f(s[9 + 2 * i] - m));
        }
#pragma unroll
        for (int i = 0; i < 16; ++i) o[i] *= alpha;
        const int dv = r32 < 20 ? r32 : 0;
        const LDSP char* vb = cb + 2560 + (t & 1) * 1280 + dv * 16 + h * 8;
        bf16x8 v0 = cat8(*(const LDSP s16x4*)vb, *(const LDSP s16x4*)(vb + 320)), v1 = cat8(*(const LDSP s16x4*)(vb + 640), *(const LDSP s16x4*)(vb + 960));
        if (r32 >= 20) { v0 = ones; v1 = ones; }
        o = MFMA32(v0, __builtin_bit_cast(bf16x8, p0), o);
        o = MFMA32(v1, __builtin_bit_cast(bf16x8, p1), o);
    }
}

DI bool attn_ok(const f32x16& a, const f32x16& b) {
    f32x2 s = {0.f, 0.f};
#pragma unroll
    for (int i = 0; i < 12; i += 2) s += f32x2{a[i], a[i + 1]} + f32x2{b[i], b[i + 1]};
    return (fmaxf(a[12], b[12]) < 0x1p60f) & (fabsf(s[0] + s[1] + a[12] + b[12]) < 0x1p126f);
}
DI void attn_pack(const f32x16& oa, const f32x16& ob, float ga, float gb, u32x4& va, u32x4& vb, u32x2& vt) {
    const float fa = ga * __builtin_amdgcn_rcpf(oa[12]), fb = gb * __builtin_amdgcn_rcpf(ob[12]);
    va = u32x4{pk_bf16(oa[0] * fa, oa[1] * fa), pk_bf16(oa[2] * fa, oa[3] * fa), pk_bf16(oa[4] * fa, oa[5] * fa), pk_bf16(oa[6] * fa, oa[7] * fa)};
    vb = u32x4{pk_bf16(ob[0] * fb, ob[1] * fb), pk_bf16(ob[2] * fb, ob[3] * fb), pk_bf16(ob[4] * fb, ob[5] * fb), pk_bf16(ob[6] * fb, ob[7] * fb)};
    auto s0 = __builtin_amdgcn_permlane32_swap(pk_bf16(oa[8] * fa, oa[9] * fa), pk_bf16(ob[8] * fb, ob[9] * fb), false, false);
    auto s1 = __builtin_amdgcn_permlane32_swap(pk_bf16(oa[10] * fa, oa[11] * fa), pk_bf16(ob[10] * fb, ob[11] * fb), false, false);
    vt = u32x2{s0[0], s1[0]};
}

typedef __attribute__((ext_vector_type(16))) unsigned u32x16;
#define WSUB(v, i) __builtin_bit_cast(bf16x8, __builtin_shufflevector(v, v, 4 * (i), 4 * (i) + 1, 4 * (i) + 2, 4 * (i) + 3))

__global__ __launch_bounds__(512, 2) void k_attn(char* __restrict__ ws, char* __restrict__ out) {
    __shared__ __attribute__((aligned(16))) char lds[163840];
    const int b = blockIdx.x, xcd = b & 7, ii = b >> 3;
    const int pos = ii >> 2, head = (pos >> 2) * 32 + xcd * 4 + (pos & 3), slot = xcd * 8 + pos, qb = ii & 3;
    const int w = __builtin_amdgcn_readfirstlane(threadIdx.x >> 6);
    int l = threadIdx.x & 63, r32 = l & 31, h = l >> 5;
    const int qc = qb * 8 + w;
    const unsigned lds0 = (unsigned)(uintptr_t)lds;
    const unsigned qo = (unsigned)WS_Q + head * (unsigned)Q_HEAD + qc * 2560, kvo = (unsigned)WS_KV + (head < 32 ? 32 : head - 32) * (unsigned)KV_HEAD;
    bf16x8 q1a, q1b, k1f, k2f; s16x4 q2; float ga, gb;
    unsigned dv = kvo + w * 4096 + l * 16;
    {
        unsigned sv, dm = __builtin_amdgcn_readfirstlane(lds0 + w * 4096);
        asm volatile(
            "global_load_dwordx4 v[4:7], %[a0], %[kvp]\n\tglobal_load_dwordx4 v[8:11], %[a0], %[kvp] offset:1024\n\tglobal_load_dwordx2 v[12:13], %[a1], %[kvp] offset:2048\n\t"
            "global_load_dwordx4 " KA_ ", %[a2], %[kvp]\n\tglobal_load_dwordx4 v[" STR(KBR) ":" STR(KBR) "+3], %[a3], %[kvp] offset:2048\n\t"
            "global_load_dword v14, %[a4], %[kvp]\n\tglobal_load_dword v15, %[a4], %[kvp] offset:128\n\t"
            "s_mov_b32 %[sv], m0\n\t" DMA4 "s_mov_b32 m0, %[sv]"
            : "=&{v[4:7]}"(q1a), "=&{v[8:11]}"(q1b), "=&{v[12:13]}"(q2), "=&{v14}"(ga), "=&{v15}"(gb), "=&{v[168:171]}"(k1f), "=&{v[172:175]}"(k2f),
              [sv] "=&s"(sv), [dv] "+v"(dv), [dm] "+s"(dm)
            : [kvp] "s"(ws), [a0] "v"(qo + l * 16), [a1] "v"(qo + r32 * 16 + h * 8), [a2] "v"(kvo + l * 16), [a3] "v"(kvo + r32 * 16), [a4] "v"((unsigned)WS_G + qc * 256 + r32 * 4)
            : "memory", "scc");
    }
    const int mb = xcd * 4 + (ii & 3), nb = ii >> 2;
    const char* wp = ws + WS_WO + (size_t)(nb * 8 + w) * 25600 + 2048;
    const char* cp = ws + WS_CNT + (((mb >> 3) * 8 + w) * 8 + nb) * 128;
    const char* sgp = ws + WS_CNT + (qb * 8 + xcd) * 8 * 128;
    const char* a1 = ws + WS_O1 + (size_t)w * 8 * 65536 + mb * 2048;
    const char* a2 = ws + WS_O2 + (size_t)w * 4 * 32768 + mb * 1024;
    const char* op = out + (size_t)mb * 64 * 1280 + nb * 160;
    const char* po1 = ws + WS_O1 + (size_t)slot * 65536 + qc * 2048;
    const char* po2 = ws + WS_O2 + (size_t)(slot >> 1) * 32768 + qc * 1024 + (slot & 1) * 8;
    f32x16 oa, ob; u32x16 vini;
#pragma unroll
    for (int i = 0; i < 16; ++i) { oa[i] = 0.f; ob[i] = 0.f; vini[i] = (r32 & 27) == 24 ? 0x3f803f80u : 0u; }
    unsigned ak1 = lds0 + l * 16, ak2 = lds0 + 2048 + r32 * 16, av = lds0 + 2560 + h * 320 + r32 * 16, wv = l * 16;
    unsigned cnt, inc, svm, dm = __builtin_amdgcn_readfirstlane(lds0 + w * 4096 + 32768);
    u32x16 wt0, wt1, wt2, wt3, wt4;
    asm volatile(
        "s_mov_b32 %[sv], m0\n\t"
        ".irp n,24,25,26,27,28,29,30,31\n\tv_mov_b32 v[" STR(S1R) "+\\n], 0\n\t.endr\n\t"
        "s_mov_b32 %[cnt], 0\n\ts_mov_b32 %[inc], 20480\n\ts_mov_b64 vcc, -1\n\t"
        "s_waitcnt vmcnt(6)\n\t"
        "v_mfma_f32_32x32x4_2b_bf16 v[" STR(S0R) ":" STR(S0R) "+31], " KB0_ ", %[q2], 0\n\ts_nop 15\n\ts_nop 1\n\t"
        M2_(S0R, 0, q1a) M2_(S0R, 16, q1b)
        "s_nop 10\n\t"
        MX3(0, S0R + 0, 1) MX3(16, S0R + 16, 17) MX3(0, NMR, 3) MX3(16, NMR + 16, 19) MX3(0, NMR, 5) MX3(16, NMR + 16, 21) MX3(0, NMR, 7) MX3(16, NMR + 16, 23)
        MX3(0, NMR, 9) MX3(16, NMR + 16, 25) MX3(0, NMR, 11) MX3(16, NMR + 16, 27) MX3(0, NMR, 13) MX3(16, NMR + 16, 29) MX3(0, NMR, 14) MX3(16, NMR + 16, 30)
        "v_mov_b32 v[" STR(NMR) "+1], v[" STR(NMR) "]\n\tv_mov_b32 v[" STR(NMR) "+17], v[" STR(NMR) "+16]\n\ts_nop 1\n\t"
        "v_permlane32_swap_b32 v[" STR(NMR) "], v[" STR(NMR) "+1]\n\tv_permlane32_swap_b32 v[" STR(NMR) "+16], v[" STR(NMR) "+17]\n\ts_nop 1\n\t"
        "v_min_f32 v[" STR(NMR) "], -v[" STR(NMR) "], -v[" STR(NMR) "+1]\n\tv_min_f32 v[" STR(NMR) "+16], -v[" STR(NMR) "+16], -v[" STR(NMR) "+17]\n\t"
        "v_mov_b32 v[" STR(NMR) "+1], v[" STR(NMR) "]\n\tv_mov_b32 v[" STR(NMR) "+17], v[" STR(NMR) "+16]\n\t"
        ".irp n,0,2,4,6,8,10,12,14\n\tv_pk_add_f32 v[" STR(S0R) "+\\n:" STR(S0R) "+\\n+1], v[" STR(S0R) "+\\n:" STR(S0R) "+\\n+1], v[" STR(NMR) ":" STR(NMR) "+1]\n\t"
        "v_pk_add_f32 v[" STR(S0R) "+16+\\n:" STR(S0R) "+17+\\n], v[" STR(S0R) "+16+\\n:" STR(S0R) "+17+\\n], v[" STR(NMR) "+16:" STR(NMR) "+17]\n\t.endr\n\t"
        ".irp n,2,4,6,8,10,12,14\n\tv_mov_b64 v[" STR(NMR) "+\\n:" STR(NMR) "+\\n+1], v[" STR(NMR) ":" STR(NMR) "+1]\n\t"
        "v_mov_b64 v[" STR(NMR) "+16+\\n:" STR(NMR) "+17+\\n], v[" STR(NMR) "+16:" STR(NMR) "+17]\n\t.endr\n\t"
        "s_waitcnt vmcnt(0)\n\ts_barrier\n\t"
        RDV(VAR, 0) RDKA(1024)
        "1:\n\t"
        TILE(S0R, S1R, VAR, VBR, 5120, KB1_, RDKB(5120), 1280, PRIO(3) "s_cbranch_vccnz 11f\n\t21:\n\t")
        TILE(S1R, S0R, VBR, VAR, 5120 + 1024, KB0_, "", 5120, "s_cbranch_vccnz 12f\n\t22:\n\t")
        TILE(S0R, S1R, VAR, VBR, 10240, KB1_, RDKB(10240), 5120 + 1280, "s_cbranch_vccnz 13f\n\t23:\n\t")
        TILE(S1R, S0R, VBR, VAR, 10240 + 1024, KB0_, "", 10240, "s_cbranch_vccnz 14f\n\t24:\n\t")
        TILE(S0R, S1R, VAR, VBR, 15360, KB1_, RDKB(15360), 10240 + 1280, "")
        TILE(S1R, S0R, VBR, VAR, 15360 + 1024, KB0_, "", 15360, PRIO(2) "s_cmp_eq_u32 %[cnt], 7\n\ts_cselect_b32 %[inc], -143360, %[inc]\n\t")
        TILE(S0R, S1R, VAR, VBR, 0, KB1_, RDKB(0), 15360 + 1280, PRIO(1) "v_add_u32 %[ak1], %[inc], %[ak1]\n\tv_add_u32 %[ak2], %[inc], %[ak2]\n\t")
        TILE(S1R, S0R, VBR, VAR, 1024, KB0_, "", 0, PRIO(0) "v_add_u32 %[av], %[inc], %[av]\n\t")
        "s_add_u32 %[cnt], %[cnt], 1\n\t"
        "s_cmp_eq_u32 %[cnt], 1\n\ts_cbranch_scc1 2f\n\t"
        "s_cmp_eq_u32 %[cnt], 3\n\ts_cbranch_scc1 3f\n\t"
        "s_cmp_eq_u32 %[cnt], 4\n\ts_cbranch_scc1 5f\n\t"
        "s_cmp_eq_u32 %[cnt], 5\n\ts_cbranch_scc1 6f\n\t"
        "s_cmp_eq_u32 %[cnt], 6\n\ts_cbranch_scc1 8f\n\t"
        "s_cmp_lt_u32 %[cnt], 8\n\ts_cbranch_scc1 1b\n\t"
        "s_branch 4f\n\t"
        "11:\n\t" DMA4 "s_branch 21b\n\t"
        "12:\n\t" DMA4 "s_branch 22b\n\t"
        "13:\n\t" DMA4 "s_branch 23b\n\t"
        "14:\n\t" DMA4 "s_branch 24b\n\t"
        "2:\n\ts_mov_b64 vcc, 0\n\ts_waitcnt vmcnt(0)\n\ts_barrier\n\ts_branch 1b\n\t"
        "3:\n\t" WLD(0)
        "5:\n\t" WLD(1)
        "6:\n\t" WLD(2)
        "8:\n\tv_and_b32 %[dv], 0x3ff, %[wv]\n\tv_lshlrev_b32 %[dv], 3, %[dv]\n\tv_min_u32 %[dv], 0x1380, %[dv]\n\tv_add_u32 %[dv], 0x5000, %[dv]\n\t"
        "global_load_dword %[dv], %[dv], %[wp] offset:-2048\n\t" WLD(3)
        "4:\n\ts_nop 2\n\t"
        PV_(ob, VBR, 4, S1R, 24)
        "s_mov_b32 m0, %[sv]\n\ts_waitcnt vmcnt(0) lgkmcnt(0)\n\ts_nop 15\n\ts_nop 7"
        : [oa] "+{v[24:39]}"(oa), [ob] "+{v[40:55]}"(ob), [ak1] "+v"(ak1), [ak2] "+v"(ak2), [av] "+v"(av), [dv] "+v"(dv), [wv] "+v"(wv),
          "+{v[152:167]}"(vini), "+{v[168:171]}"(k1f), "+{v[172:175]}"(k2f),
          [q1a] "+{v[4:7]}"(q1a), [q1b] "+{v[8:11]}"(q1b), [q2] "+{v[12:13]}"(q2), "+{v14}"(ga), "+{v15}"(gb),
          "=&{v[176:191]}"(wt0), "=&{v[192:207]}"(wt1), "=&{v[208:223]}"(wt2), "=&{v[224:239]}"(wt3), "=&{v[240:255]}"(wt4),
          [cnt] "=&s"(cnt), [inc] "=&s"(inc), [sv] "=&s"(svm), [dm] "+s"(dm)
        : [vmask] "s"(0x000fffff000fffffull), [kvp] "s"(ws), [wp] "s"(wp), [idx] "v"(0xddddu),
          "s"(cp), "s"(sgp), "s"(a1), "s"(a2), "s"(op), "s"(po1), "s"(po2)
        : "memory", "scc", "vcc", "v56", "v57", "v58", "v59", CL10(6), CL10(7), CL10(8), CL10(9), CL10(10), CL10(11), CL10(12), CL10(13), CL10(14), "v150", "v151"
          );
    asm volatile("v_mbcnt_lo_u32_b32 %0, -1, 0\n\tv_mbcnt_hi_u32_b32 %0, -1, %0" : "=v"(l));
    r32 = l & 31; h = l >> 5;
    bf16x8 wl[5], af[5][4]; unsigned seen;
    {
        if (head >= 32) { ga = 1.0f - ga; gb = 1.0f - gb; }
        u32x4 va, vb; u32x2 vt;
        attn_pack(oa, ob, ga, gb, va, vb, vt);
        if (__builtin_expect(!__all(attn_ok(oa, ob)), 0)) {
#pragma unroll 1
            for (int t = 0; t < 2; ++t) {
                f32x16 ro; u32x4 v0, v1; u32x2 v2;
                attn_fallback(lds, l, ws + qo, t, ro);
                attn_pack(ro, ro, t ? gb : ga, t ? gb : ga, v0, v1, v2);
                if (t == 0) va = v0; else vb = v0;
                if (h == t) vt = v2;
            }
        }
        asm volatile(
            "global_store_dwordx4 %[lo], %[va], %[o1] sc1\n\tglobal_store_dwordx4 %[lo], %[vb], %[o1] offset:1024 sc1\n\tglobal_store_dwordx2 %[lo], %[vt], %[o2] sc1\n\t"
            "s_nop 0"
            :: [lo] "v"(l * 16), [va] "v"(va), [vb] "v"(vb), [vt] "v"(vt), [o1] "s"(po1), [o2] "s"(po2)
            : "memory");
    }
    const int r = l & 15, g = l >> 4, r8 = l & 7, g2 = (l >> 3) & 3;
    unsigned o1 = (g >> 1) * 65536 + (g & 1) * 512 + r * 16, o2 = g * 32768 + r * 16;
    unsigned wbo = w * 20480 + (g * 16 + (r ^ ((g >> 1) << 3))) * 16;
    unsigned rbo = ((w >> 1) * 5 + h) * 1024 + (g2 * 16 + ((8 * (w & 1) + r8) ^ ((g2 >> 1) << 3))) * 16;
    unsigned opo = (8 * w + r8) * 1280 + h * 32 + g2 * 8, sgo = l * 128;
    asm volatile("s_waitcnt vmcnt(0)\n\ts_barrier" : "+v"(o1), "+v"(o2), "+v"(wbo), "+v"(rbo), "+v"(opo), "+v"(sgo) :: "memory");
    if (w == (xcd ^ 1) && l < 8) __hip_atomic_fetch_add((__attribute__((address_space(1))) unsigned*)(sgp + sgo), 1u, __ATOMIC_RELAXED, __HIP_MEMORY_SCOPE_AGENT);
    asm volatile(
        "global_load_dwordx4 %0, %6, %7 offset:-2048\n\tglobal_load_dwordx4 %1, %6, %7 offset:-1024\n\tglobal_load_dwordx4 %2, %6, %7\n\t"
        "global_load_dwordx4 %3, %6, %7 offset:1024\n\tglobal_load_dwordx4 %4, %6, %7 offset:2048\n\tglobal_load_dword %5, %8, %9 sc1\n\ts_waitcnt vmcnt(0)"
        : "=&v"(wl[0]), "=&v"(wl[1]), "=&v"(wl[2]), "=&v"(wl[3]), "=&v"(wl[4]), "=&v"(seen) : "v"(wv), "s"(wp), "v"(0), "s"(cp) : "memory");
#pragma unroll 1
    for (int spin = 0; spin < 400000 && __builtin_amdgcn_readfirstlane(seen) < 8; ++spin) {
        __builtin_amdgcn_s_sleep(4);
        asm volatile("global_load_dword %0, %1, %2 sc1\n\ts_waitcnt vmcnt(0)" : "=v"(seen) : "v"(0), "s"(cp) : "memory");
    }
#define ALD(s, base, off, d1, d2) asm volatile( \
        "global_load_dwordx4 %0, %4, %5 sc1\n\tglobal_load_dwordx4 %1, %4, %5 offset:" #d1 " sc1\n\t" \
        "global_load_dwordx4 %2, %4, %5 offset:" #d2 " sc1\n\tglobal_load_dwordx4 %3, %4, %5 offset:" #d1 "+" #d2 " sc1" \
        : "=&v"(af[s][0]), "=&v"(af[s][1]), "=&v"(af[s][2]), "=&v"(af[s][3]) : "v"(off), "s"(base) : "memory")
#define AWAIT(s, N) asm volatile("s_waitcnt vmcnt(" STR(N) ")" : "+v"(af[s][0]), "+v"(af[s][1]), "+v"(af[s][2]), "+v"(af[s][3]) :: "memory")
#define OMM(s, w0, w1, w2, w3, w4) do { const bf16x8 wf_[5] = {w0, w1, w2, w3, w4}; \
        _Pragma("unroll") for (int j = 0; j < 5; ++j) _Pragma("unroll") for (int i = 0; i < 4; ++i) acc[i][j] = MFMA16(wf_[j], af[s][i], acc[i][j]); \
        __builtin_amdgcn_sched_barrier(0); } while (0)
    f32x4 acc[4][5];
#pragma unroll
    for (int i = 0; i < 4; ++i)
#pragma unroll
        for (int j = 0; j < 5; ++j) acc[i][j] = f32x4{0.f, 0.f, 0.f, 0.f};
    ALD(0, a1, o1, 256, 1024); ALD(1, a1 + 131072, o1, 256, 1024); ALD(2, a1 + 2 * 131072, o1, 256, 1024);
    AWAIT(0, 8); ALD(3, a1 + 3 * 131072, o1, 256, 1024); OMM(0, WSUB(wt0, 0), WSUB(wt0, 1), WSUB(wt0, 2), WSUB(wt0, 3), WSUB(wt1, 0));
    AWAIT(1, 8); ALD(4, a2, o2, 256, 512); OMM(1, WSUB(wt1, 1), WSUB(wt1, 2), WSUB(wt1, 3), WSUB(wt2, 0), WSUB(wt2, 1));
    AWAIT(2, 8); OMM(2, WSUB(wt2, 2), WSUB(wt2, 3), WSUB(wt3, 0), WSUB(wt3, 1), WSUB(wt3, 2));
    AWAIT(3, 4); OMM(3, WSUB(wt3, 3), WSUB(wt4, 0), WSUB(wt4, 1), WSUB(wt4, 2), WSUB(wt4, 3));
    AWAIT(4, 0);
#pragma unroll
    for (int j = 0; j < 6; ++j) {
        if (j < 5) {
#pragma unroll
            for (int i = 0; i < 4; ++i) acc[i][j] = MFMA16(wl[j], af[4][i], acc[i][j]);
        }
        if (j > 0) {
#pragma unroll
            for (int i = 0; i < 4; ++i) *(f32x4*)(lds + wbo + (i * 5 + j - 1) * 1024) = acc[i][j - 1];
        }
        __builtin_amdgcn_sched_barrier(0);
    }
    {
        __syncthreads();
        f32x4 t[3][8];
#pragma unroll
        for (int u = 0; u < 8; ++u)
#pragma unroll
            for (int p = 0; p < 3; ++p) t[p][u] = *(const f32x4*)(lds + rbo + u * 20480 + (p < 2 ? p * 2048 : 4096 - h * 1024));
        __builtin_amdgcn_sched_barrier(0);
#pragma unroll
        for (int p = 0; p < 3; ++p) {
            const f32x4 z = ((t[p][0] + t[p][1]) + (t[p][2] + t[p][3])) + ((t[p][4] + t[p][5]) + (t[p][6] + t[p][7]));
            const u32x2 v = {pk_bf16(z[0], z[1]), pk_bf16(z[2], z[3])};
            if (p < 2 || h == 0) asm volatile("global_store_dwordx2 %0, %1, %2 offset:%3" :: "v"(opo), "v"(v), "s"(op), "i"(p * 64) : "memory");
        }
    }
}

extern "C" void kernel_launch(void* const* d_in, const int* in_sizes, int n_in, void* d_out, int out_size, void* d_ws, size_t ws_size,
                              hipStream_t stream) {
    const char* x = (const char*)d_in[0];
    const char* wq = (const char*)d_in[1];
    const char* wk = (const char*)d_in[2];
    const char* wv = (const char*)d_in[3];
    const char* woa = (const char*)d_in[4];
    const char* wqkv = (const char*)d_in[6];
    const char* wob = (const char*)d_in[7];
    const char* gw = (const char*)d_in[8];
    char* ws = (char*)d_ws;
    k_proj<<<256, 768, 0, stream>>>(x, wq, wqkv, wk, wv, gw, woa, wob, ws);
    k_attn<<<256, 512, 0, stream>>>(ws, (char*)d_out);
}
```

```cpp
#include <hip/hip_runtime.h>
#include <hip/hip_bf16.h>
#include <stdint.h>

typedef __attribute__((ext_vector_type(8))) short bf16x8;
typedef __attribute__((ext_vector_type(4))) short s16x4;
typedef __attribute__((ext_vector_type(4))) float f32x4;
typedef __attribute__((ext_vector_type(16))) float f32x16;
typedef __attribute__((ext_vector_type(2))) unsigned u32x2;
typedef __attribute__((ext_vector_type(4))) unsigned u32x4;
typedef __attribute__((ext_vector_type(2))) __bf16 bf16x2_t;
typedef __attribute__((ext_vector_type(2))) float f32x2;
#define LDSP __attribute__((address_space(3)))
#define DI __device__ __forceinline__

constexpr int DM = 640;
constexpr size_t Q_HEAD = 32 * 2560;
constexpr size_t KV_HEAD = 32 * 5120;
constexpr size_t WS_Q = 0;
constexpr size_t WS_KV = WS_Q + 64 * Q_HEAD;
constexpr size_t WS_G = WS_KV + 33 * KV_HEAD;
constexpr size_t WS_O1 = WS_G + 2048 * 4;
constexpr size_t WS_O2 = WS_O1 + 64 * 65536;
constexpr size_t WS_WO = WS_O2 + 32 * 2048 * 16;
constexpr size_t WS_CNT = WS_WO + 1600 * 1024;

DI unsigned pk_bf16(float a, float b) {
    f32x2 v = {a, b};
    return __builtin_bit_cast(unsigned, __builtin_convertvector(v, bf16x2_t));
}
DI void st16(char* p, u32x4 v) { asm volatile("global_store_dwordx4 %0, %1, off sc1\n\ts_nop 1" :: "v"(p), "v"(v) : "memory"); }
DI void st8(char* p, u32x2 v) { asm volatile("global_store_dwordx2 %0, %1, off sc1" :: "v"(p), "v"(v) : "memory"); }
DI void st4(char* p, unsigned v) { asm volatile("global_store_dword %0, %1, off sc1" :: "v"(p), "v"(v) : "memory"); }
DI s16x4 tr_read(const LDSP char* p) {
    typedef short v4i16_t __attribute__((ext_vector_type(4)));
    return __builtin_amdgcn_ds_read_tr16_b64_v4i16((LDSP v4i16_t*)p);
}
DI bf16x8 cat8(s16x4 lo, s16x4 hi) { return __builtin_shufflevector(lo, hi, 0, 1, 2, 3, 4, 5, 6, 7); }
#define MFMA16(a, b, c) __builtin_amdgcn_mfma_f32_16x16x32_bf16((a), (b), (c), 0, 0, 0)
#define MFMA32(a, b, c) __builtin_amdgcn_mfma_f32_32x32x16_bf16((a), (b), (c), 0, 0, 0)

constexpr int P_SLOT = 40960;

DI void proj_loader(char* lds, const char* xs, const char* ws, const char* es, int sw, int se, unsigned long long m5, int st, int ekind, int ehalf, int mb, bool vperm,
                    const char* woa, const char* wob, char* wsp, const unsigned (&xo)[2], const unsigned (&wo)[3]) {
    const int l = threadIdx.x & 63, w = __builtin_amdgcn_readfirstlane(threadIdx.x >> 6), r = l & 15, g = l >> 4;
    const unsigned dw = __builtin_amdgcn_readfirstlane((unsigned)(uintptr_t)lds + w * 1024);
    auto issue = [&](int slot) {
        const unsigned d0 = dw + slot * P_SLOT;
        unsigned sv;
        asm volatile(
            "s_mov_b32 %[sv], m0\n\ts_mov_b32 m0, %[d0]\n\ts_nop 0\n\t"
            "global_load_lds_dwordx4 %[x0], %[xs]\n\ts_add_u32 m0, m0, 8192\n\ts_nop 0\n\t"
            "global_load_lds_dwordx4 %[x1], %[xs]\n\ts_add_u32 m0, m0, 8192\n\ts_nop 0\n\t"
            "global_load_lds_dwordx4 %[w0], %[ws]\n\ts_add_u32 m0, m0, 8192\n\ts_nop 0\n\t"
            "global_load_lds_dwordx4 %[w1], %[ws]\n\ts_add_u32 m0, m0, 8192\n\ts_mov_b64 exec, %[m5]\n\t"
            "global_load_lds_dwordx4 %[w2], %[es]\n\ts_mov_b64 exec, -1\n\ts_mov_b32 m0, %[sv]"
            : [sv] "=&s"(sv)
            : [d0] "s"(d0), [m5] "s"(m5), [xs] "s"(xs), [ws] "s"(ws), [es] "s"(es),
              [x0] "v"(xo[0]), [x1] "v"(xo[1]), [w0] "v"(wo[0]), [w1] "v"(wo[1]), [w2] "v"(wo[2])
            : "memory", "scc");
        const int d = st == 9 ? -9 : 1;
        st += d; xs += d * 128; ws += d * sw; es += d * se;
    };
    issue(0); issue(1);
    __builtin_amdgcn_s_setprio(3);
    f32x4 eacc = {0.f, 0.f, 0.f, 0.f};
    const int ets = 4 * ehalf + w;
    if (w >= 4) ekind = 5;
    const int etok = 16 * ets + r, erow = vperm ? (etok & 99) | ((etok & 4) << 2) | ((etok & 24) >> 1) : etok;
    const int exr = erow * 128 + ((g ^ ((erow >> 1) & 7)) << 4);
    const int ewr = ekind < 4 ? 36864 + (8 * g + (r >> 2)) * 40 + ((ekind & 1) ? 32 : 8 * (r & 3)) : 36864 + g * 16;
    auto extra = [&](int k) {
        if (ekind < 5) {
            const LDSP char* sb = (const LDSP char*)lds + (k & 3) * P_SLOT;
            const bf16x8 x0 = *(const LDSP bf16x8*)(sb + exr), x1 = *(const LDSP bf16x8*)(sb + (exr ^ 64));
            bf16x8 w0, w1;
            if (ekind < 4) { w0 = cat8(tr_read(sb + ewr), tr_read(sb + ewr + 160)); w1 = cat8(tr_read(sb + ewr + 1280), tr_read(sb + ewr + 1440)); }
            else { w0 = *(const LDSP bf16x8*)(sb + ewr); w1 = *(const LDSP bf16x8*)(sb + ewr + 64); }
            if ((ekind >> 1) == 1) { eacc = MFMA16(x0, w0, eacc); eacc = MFMA16(x1, w1, eacc); }
            else { eacc = MFMA16(w0, x0, eacc); eacc = MFMA16(w1, x1, eacc); }
            asm volatile("" : "+v"(eacc));
        }
    };
    const int gw = blockIdx.x * 8 + w, fid = gw < 1600 ? gw : 1599;
    const int fj = fid % 5, fs = (fid / 5) % 5, fg = (fid / 25) & 7, fn = fid / 200;
    const int br = fs < 4 ? fs >> 1 : g >> 1;
    const int row = fs < 4 ? (4 * fg + 2 * (fs & 1) + (g >> 1)) * 20 + 4 * (g & 1) : (4 * fg + 2 * (g & 1)) * 20 + 16;
    const char* p0 = (br ? wob : woa) + row * 1280 + (80 * fn + 16 * fj + r) * 2;
    const char* p1 = p0 + (fs < 4 ? 8 : 20) * 1280;
    asm volatile("" : "+v"(p0), "+v"(p1));
#pragma unroll 1
    for (int k = 0; k < 8; ++k) {
        asm volatile("s_waitcnt vmcnt(5)\n\ts_barrier" ::: "memory");
        issue((k + 2) & 3);
        if (k < 7) extra(k);
    }
    unsigned e0, e1, e2, e3, e4, e5, e6, e7;
    asm volatile(
        "global_load_ushort %0, %8, off\n\tglobal_load_ushort %1, %8, off offset:1280\n\tglobal_load_ushort %2, %8, off offset:2560\n\tglobal_load_ushort %3, %8, off offset:3840\n\t"
        "global_load_ushort %4, %9, off\n\tglobal_load_ushort %5, %9, off offset:1280\n\tglobal_load_ushort %6, %9, off offset:2560\n\tglobal_load_ushort %7, %9, off offset:3840"
        : "=&v"(e0), "=&v"(e1), "=&v"(e2), "=&v"(e3), "=&v"(e4), "=&v"(e5), "=&v"(e6), "=&v"(e7) : "v"(p0), "v"(p1) : "memory");
    extra(7);
    asm volatile("s_waitcnt vmcnt(13)\n\ts_barrier" ::: "memory");
    extra(8);
    asm volatile("s_waitcnt vmcnt(8)\n\ts_barrier" ::: "memory");
    extra(9);
    asm volatile("s_waitcnt vmcnt(0)" : "+v"(e0), "+v"(e1), "+v"(e2), "+v"(e3), "+v"(e4), "+v"(e5), "+v"(e6), "+v"(e7) :: "memory");
    {
        char* kva = wsp + WS_KV + 32 * KV_HEAD + (mb * 2 + ehalf) * 5120;
        const u32x2 v = {pk_bf16(eacc[0], eacc[1]), pk_bf16(eacc[2], eacc[3])};
        const int t = (w >> 1) & 1, krow = 16 * (w & 1) + r;
        if (ekind == 0) st8(kva + t * 1024 + (g >> 1) * 512 + krow * 16 + (g & 1) * 8, v);
        else if (ekind == 1) { if (g == 0) st8(kva + 2048 + krow * 16 + t * 8, v); }
        else if (ekind < 4) { if (ekind == 2 || r < 4) st8(kva + 2560 + t * 1280 + (2 * (w & 1) + (g >> 1)) * 320 + ((ekind & 1) * 16 + r) * 16 + (g & 1) * 8, v); }
        else if (ekind == 4) { if (g == 0) st4(wsp + WS_G + (mb * 128 + 16 * ets + r) * 4, __float_as_uint(1.0f / (1.0f + __expf(-eacc[0])))); }
    }
    if (gw < 1600) st16(wsp + WS_WO + (size_t)fid * 1024 + l * 16, u32x4{e0 | (e1 << 16), e2 | (e3 << 16), e4 | (e5 << 16), e6 | (e7 << 16)});
    if (w == 0 && l == 0) st4(wsp + WS_CNT + blockIdx.x * 128, 0);
}

#define STR_(x) #x
#define STR(x) STR_(x)
#define PR4(b) "v[" STR(b) ":" STR(b) "+3]"
#define PM_F(j, i, X) "v_mfma_f32_16x16x32_bf16 " PR4(20 + 16 * j + 4 * i) ", " PR4(136 + 4 * j) ", " PR4(X + 4 * i) ", " PR4(20 + 16 * j + 4 * i) "\n\t"
#define PM_T(j, i, X) "v_mfma_f32_16x16x32_bf16 " PR4(20 + 16 * j + 4 * i) ", " PR4(X + 4 * i) ", " PR4(136 + 4 * j) ", " PR4(20 + 16 * j + 4 * i) "\n\t"
#define PTL(j, sl, ks) "ds_read_b64_tr_b16 v[136+4*" #j ":136+4*" #j "+1], %[aw" #j "] offset:" #sl "*40960+" #ks "*10240\n\t"
#define PTH(j, sl, ks) "ds_read_b64_tr_b16 v[136+4*" #j "+2:136+4*" #j "+3], %[aw" #j "] offset:" #sl "*40960+" #ks "*10240+1280\n\t"
#define PBX(X, i, sl, ks) "ds_read_b128 " PR4(X + 4 * i) ", %[ax" #ks "] offset:" #sl "*40960+" #i "*2048\n\t"
#define PTG(a) "v_add_u32 %[" #a "], %[tg], %[" #a "]\n\t"
#define PSTEP(M, X, Y, cs, ck, ns, nk, BAR, F0, F1, F2, F3) \
    M(0, 0, X) PTL(4, cs, ck) M(0, 1, X) PTH(4, cs, ck) M(0, 2, X) BAR PBX(Y, 0, ns, nk) M(0, 3, X) PBX(Y, 1, ns, nk) \
    M(1, 0, X) PTL(0, ns, nk) M(1, 1, X) PTH(0, ns, nk) M(1, 2, X) PBX(Y, 2, ns, nk) M(1, 3, X) "s_waitcnt lgkmcnt(5)\n\t" \
    M(2, 0, X) PTL(1, ns, nk) M(2, 1, X) PTH(1, ns, nk) M(2, 2, X) PBX(Y, 3, ns, nk) M(2, 3, X) F0 \
    M(3, 0, X) PTL(2, ns, nk) M(3, 1, X) PTH(2, ns, nk) M(3, 2, X) F1 M(3, 3, X) F2 \
    M(4, 0, X) PTL(3, ns, nk) M(4, 1, X) PTH(3, ns, nk) M(4, 2, X) F3 M(4, 3, X) "s_waitcnt lgkmcnt(4)\n\t"
#define CL10(a) "v" #a "0", "v" #a "1", "v" #a "2", "v" #a "3", "v" #a "4", "v" #a "5", "v" #a "6", "v" #a "7", "v" #a "8", "v" #a "9"
#define PLOOP(M) \
    asm volatile( \
        "s_mov_b32 %[cnt], 0\n\ts_mov_b32 %[tg], 81920\n\t" \
        "s_barrier\n\t" \
        PBX(104, 0, 0, 0) PBX(104, 1, 0, 0) PTL(0, 0, 0) PTH(0, 0, 0) PBX(104, 2, 0, 0) PTL(1, 0, 0) PTH(1, 0, 0) PBX(104, 3, 0, 0) \
        PTL(2, 0, 0) PTH(2, 0, 0) PTL(3, 0, 0) PTH(3, 0, 0) "s_waitcnt lgkmcnt(4)\n\t" \
        "1:\n\t" \
        PSTEP(M, 104, 120, 0, 0, 0, 1, "", "", "", "", "") \
        PSTEP(M, 120, 104, 0, 1, 1, 0, "s_barrier\n\t", PTG(ax0), "s_add_u32 %[cnt], %[cnt], 1\n\t", "", "") \
        PSTEP(M, 104, 120, 1, 0, 1, 1, "", PTG(aw0), PTG(aw1), PTG(aw2), PTG(ax1)) \
        PSTEP(M, 120, 104, 1, 1, 0, 0, "s_cmp_eq_u32 %[cnt], 5\n\ts_cbranch_scc1 2f\n\ts_barrier\n\t2:\n\t", PTG(aw3), PTG(aw4), "s_sub_u32 %[tg], 0, %[tg]\n\t", \
              "s_cmp_lg_u32 %[cnt], 5\n\t") \
        "s_cbranch_scc1 1b\n\t" \
        "s_waitcnt lgkmcnt(0)\n\ts_nop 7" \
        : "+{v[20:35]}"(acc[0]), "+{v[36:51]}"(acc[1]), "+{v[52:67]}"(acc[2]), "+{v[68:83]}"(acc[3]), "+{v[84:99]}"(acc[4]), \
          [ax0] "+v"(ax0), [ax1] "+v"(ax1), [aw0] "+v"(aw[0]), [aw1] "+v"(aw[1]), [aw2] "+v"(aw[2]), [aw3] "+v"(aw[3]), [aw4] "+v"(aw[4]), \
          [cnt] "=&s"(cnt), [tg] "=&s"(tg) \
        : \
        : "memory", "scc", "v104", "v105", "v106", "v107", "v108", "v109", CL10(11), CL10(12), CL10(13), CL10(14), "v150", "v151", "v152", "v153", "v154", "v155")

template <bool VT>
DI void proj_mma(unsigned lds0, f32x16 (&acc)[5]) {
    const int tid = threadIdx.x, l = tid & 63, w = __builtin_amdgcn_readfirstlane(tid >> 6) - 8;
    const int wm = w >> 1, wn = w & 1, r = l & 15, g = l >> 4;
    const int xsw = (g ^ ((r >> 1) & 7)) << 4;
    unsigned ax0 = lds0 + (64 * wm + r) * 128 + xsw, ax1 = lds0 + (64 * wm + r) * 128 + (xsw ^ 64);
    unsigned aw[5];
    const int q = r >> 2, p = r & 3;
#pragma unroll
    for (int j = 0; j < 5; ++j) {
        int col;
        if (j == 4) col = (4 * wn + p) * 20 + 16;
        else if (VT) col = (4 * wn + j) * 20 + 4 * p;
        else col = (4 * wn + (j & 2) + (p >> 1)) * 20 + 8 * (p & 1) + 4 * (j & 1);
        aw[j] = lds0 + 16384 + (8 * g + q) * 320 + ((col * 2) ^ ((g & 1) << 5));
    }
    unsigned cnt, tg;
    if constexpr (VT) PLOOP(PM_T); else PLOOP(PM_F);
}
#define ACC(i, j) f32x4{acc[j][4 * (i)], acc[j][4 * (i) + 1], acc[j][4 * (i) + 2], acc[j][4 * (i) + 3]}

DI u32x4 pk8(const f32x4& a, const f32x4& b, float sc) {
    return u32x4{pk_bf16(a[0] * sc, a[1] * sc), pk_bf16(a[2] * sc, a[3] * sc), pk_bf16(b[0] * sc, b[1] * sc), pk_bf16(b[2] * sc, b[3] * sc)};
}

#define CODE_PROJ "_Z6k_projPKcS0_S0_S0_S0_S0_S0_S0_Pc"
#define CODE_ATTN "_Z6k_attnPcS_"
#define CODE_BYTES(w) ((w) ? 6912 : 8704)
__global__ __launch_bounds__(768, 3) void k_proj(const char* __restrict__ x, const char* __restrict__ wq, const char* __restrict__ wqkv,
                                                 const char* __restrict__ wk, const char* __restrict__ wv, const char* __restrict__ gw,
                                                 const char* __restrict__ woa, const char* __restrict__ wob, char* __restrict__ ws) {
    __shared__ __attribute__((aligned(16))) char lds[4 * P_SLOT];
    const int b = blockIdx.x, xcd = b & 7, ii = b >> 3;
    const int mb = (xcd & 1) * 8 + (ii & 7), type = xcd >> 1, nq = ii >> 3;
    const int tid = threadIdx.x, l = tid & 63, wv_ = __builtin_amdgcn_readfirstlane(tid >> 6);
    const int ldw2 = type == 0 ? 1280 : 3840;
    const int eidx = ((nq >> 1) | ((nq & 1) << 1)) * 4 + type, ehalf = eidx >= 5, ekind = eidx < 10 ? eidx - 5 * ehalf : 5;
    int rot = (ii & 7) + nq + type; rot = 2 * (rot >= 10 ? rot - 10 : (rot >= 5 ? rot - 5 : rot));
    const int ebytes = ekind < 4 ? 2560 : 128, nbytes = ebytes - (wv_ - 4) * 1024;
    const unsigned long long m5 = ~0ull >> (nbytes >= 1024 ? 0 : (nbytes > 0 ? 64 - (nbytes >> 4) : 63));
    const int sw = 64 * ldw2, se = wv_ < 4 ? sw : ebytes, wofs = (type > 1 ? (type - 1) * 1280 : 0) + nq * 320 + rot * sw;
    unsigned xo[2], wo[3];
    const int lrow = tid >> 3, xrow = type == 3 ? (lrow & 35) | ((lrow & 16) >> 2) | ((lrow & 12) << 1) : lrow;
    xo[0] = (mb * 128 + xrow) * (DM * 2) + (((tid ^ (tid >> 4)) & 7) << 4) + rot * 128; xo[1] = xo[0] + 64 * DM * 2;
#pragma unroll
    for (int n = 0; n < 3; ++n) { const int cid = tid + 512 * n, k = (cid * 3277) >> 16, c = cid - 20 * k; wo[n] = k * ldw2 + ((c ^ (((k >> 3) & 1) << 1)) << 4) + wofs; }
    if (tid >= 256) wo[2] = l * 16 + (nbytes > 0 ? (wv_ - 4) * 1024 : 0) + rot * ebytes;
    asm volatile("" : "+v"(xo[0]), "+v"(xo[1]), "+v"(wo[0]), "+v"(wo[1]), "+v"(wo[2])
                 : "s"(x), "s"(wq), "s"(wqkv), "s"(wk), "s"(wv), "s"(gw), "s"(woa), "s"(wob), "s"(ws), "s"(m5), "s"(se), "s"(ekind));
    if (__builtin_expect(wv_ < 8, 1)) {
        const char* wsel = type == 0 ? wq : wqkv;
        proj_loader(lds, x, wsel, wv_ < 4 ? wsel : (ekind < 2 ? wk : (ekind < 4 ? wv : gw)), sw, se, m5, rot, ekind, ehalf, mb, type == 3, woa, wob, ws, xo, wo);
        return;
    }
    const int w = wv_ - 8;
    if (w < 2) {
        const unsigned dp = (unsigned)(uintptr_t)lds + P_SLOT - 512, off = (ii * 3 + l) * 128; unsigned sv; unsigned long long pc;
        if (w == 0) asm volatile("s_getpc_b64 s[40:41]\n\ts_add_u32 s40, s40, " CODE_PROJ "@rel32@lo+4\n\ts_addc_u32 s41, s41, " CODE_PROJ "@rel32@hi+12" : "={s[40:41]}"(pc) :: "scc");
        else asm volatile("s_getpc_b64 s[40:41]\n\ts_add_u32 s40, s40, " CODE_ATTN "@rel32@lo+4\n\ts_addc_u32 s41, s41, " CODE_ATTN "@rel32@hi+12" : "={s[40:41]}"(pc) :: "scc");
        if (l < 3 && off < CODE_BYTES(w))
            asm volatile("s_mov_b32 %0, m0\n\ts_mov_b32 m0, %1\n\ts_nop 0\n\tglobal_load_lds_dword %2, %3\n\ts_mov_b32 m0, %0" : "=&s"(sv) : "s"(dp), "v"(off), "s"(pc) : "memory");
    }
    const int wm = w >> 1, wn = w & 1, r = l & 15, g = l >> 4;
    f32x16 acc[5];
#pragma unroll
    for (int j = 0; j < 5; ++j)
#pragma unroll
        for (int i = 0; i < 16; ++i) acc[j][i] = 0.f;
    const unsigned lds0 = (unsigned)(uintptr_t)lds;
    const int chunk = mb * 2 + wm;
#define ST16(voff, sbase, imm, val) asm volatile("global_store_dwordx4 %0, %1, %2 offset:%3 sc1\n\ts_nop 1" :: "v"(voff), "v"(val), "s"(sbase), "i"(imm) : "memory")
    if (type == 3) {
        const char* sb = ws + WS_KV + (size_t)(nq * 8 + 4 * wn) * KV_HEAD + chunk * 5120 + 2560;
        unsigned v1 = g * 320 + r * 16, v2 = (r >> 2) * (unsigned)KV_HEAD + g * 320 + (16 + (r & 3)) * 16;
        asm volatile("" : "+v"(v1), "+v"(v2) : "s"(sb), "s"(sb + KV_HEAD), "s"(sb + 2 * KV_HEAD), "s"(sb + 3 * KV_HEAD));
        proj_mma<true>(lds0, acc);
#pragma unroll
        for (int j = 0; j < 4; ++j) {
            ST16(v1, sb + j * KV_HEAD, 0, pk8(ACC(0, j), ACC(1, j), 1.0f));
            ST16(v1, sb + j * KV_HEAD, 1280, pk8(ACC(2, j), ACC(3, j), 1.0f));
        }
        ST16(v2, sb, 0, pk8(ACC(0, 4), ACC(1, 4), 1.0f));
        ST16(v2, sb, 1280, pk8(ACC(2, 4), ACC(3, 4), 1.0f));
    } else {
        const float sc = type == 2 ? 1.0f : 0.32259642f;
        const unsigned hstride = type == 2 ? KV_HEAD : Q_HEAD;
        const char* sb = ws + (type == 2 ? WS_KV + chunk * 5120 : WS_Q + type * 32 * Q_HEAD + chunk * 2560) + (nq * 8 + 4 * wn) * hstride;
        const unsigned row0 = r * 16;
        unsigned v1 = (g >> 1) * hstride + (g & 1) * 512 + row0, v2 = g * hstride + row0;
        asm volatile("" : "+v"(v1), "+v"(v2) : "s"(sb), "s"(sb + 2 * hstride));
        proj_mma<false>(lds0, acc);
#pragma unroll
        for (int jj = 0; jj < 2; ++jj)
#pragma unroll
            for (int i = 0; i < 4; ++i) ST16(v1, sb + jj * 2 * hstride, (i >> 1) * 1024 + (i & 1) * 256, pk8(ACC(i, 2 * jj), ACC(i, 2 * jj + 1), sc));
        ST16(v2, sb, 2048, pk8(ACC(0, 4), ACC(2, 4), sc));
        ST16(v2, sb, 2048 + 256, pk8(ACC(1, 4), ACC(3, 4), sc));
    }
}

#define S0R 56
#define S1R 88
#define NMR 120
#define VAR 152
#define VBR 160
#define KAR 168
#define KBR 172
#define WFR 176
#define E_(X, i) "v_exp_f32 v[" STR(X) "+" #i "], v[" STR(X) "+" #i "]\n\t"
#define PV_(O, V, vo, X, po) "v_smfmac_f32_32x32x32_bf16 %[" #O "], v[" STR(V) "+" #vo ":" STR(V) "+" #vo "+3], v[" STR(X) "+" #po ":" STR(X) "+" #po "+7], %[idx]\n\t"
#define M1_(Y, KB) "v_mfma_f32_32x32x4_2b_bf16 v[" STR(Y) ":" STR(Y) "+31], " KB ", %[q2], v[" STR(NMR) ":" STR(NMR) "+31]\n\t"
#define M2_(Y, o, Q) "v_mfma_f32_32x32x16_bf16 v[" STR(Y) "+" #o ":" STR(Y) "+" #o "+15], " KA_ ", %[" #Q "], v[" STR(Y) "+" #o ":" STR(Y) "+" #o "+15]\n\t"
#define KA_ "v[" STR(KAR) ":" STR(KAR) "+3]"
#define KB0_ "v[" STR(KBR) ":" STR(KBR) "+1]"
#define KB1_ "v[" STR(KBR) "+2:" STR(KBR) "+3]"
#define RDKA(off) "ds_read_b128 " KA_ ", %[ak1] offset:" STR(off) "\n\t"
#define RDKB(off) "ds_read_b128 v[" STR(KBR) ":" STR(KBR) "+3], %[ak2] offset:" STR(off) "\n\t"
#define RDV(V, off) "s_mov_b64 exec, %[vmask]\n\tds_read_b128 v[" STR(V) ":" STR(V) "+3], %[av] offset:" STR(off) "\n\t" \
                    "ds_read_b128 v[" STR(V) "+4:" STR(V) "+7], %[av] offset:" STR(off) "+640\n\ts_mov_b64 exec, -1\n\t"
#define MX3(d, a, i) "v_max3_f32 v[" STR(NMR) "+" #d "], v[" STR(a) "], v[" STR(S0R) "+" #i "], v[" STR(S0R) "+" #i "+1]\n\t"
#define PRIO(n) "s_setprio " #n "\n\t"
#define DMA4 "s_mov_b32 m0, %[dm]\n\ts_add_u32 %[dm], %[dm], 0x8000\n\tglobal_load_lds_dwordx4 %[dv], %[kvp]\n\tglobal_load_lds_dwordx4 %[dv], %[kvp] offset:1024\n\t" \
             "global_load_lds_dwordx4 %[dv], %[kvp] offset:2048\n\tglobal_load_lds_dwordx4 %[dv], %[kvp] offset:3072\n\tv_add_u32 %[dv], 0x8000, %[dv]\n\t"
#define WLD1(s, j) "global_load_dwordx4 v[" STR(WFR) "+20*" #s "+4*" #j ":" STR(WFR) "+20*" #s "+4*" #j "+3], %[wv], %[wp] offset:" #j "*1024-2048\n\t"
#define WLD(s) WLD1(s, 0) WLD1(s, 1) WLD1(s, 2) WLD1(s, 3) WLD1(s, 4) "v_add_u32 %[wv], 0x1400, %[wv]\n\ts_branch 1b\n\t"
#define TILE(X, Y, VC, VP, KAOFF, KB, KBRD, VOFF, PRE) \
    PRE E_(X, 0) E_(X, 1) E_(X, 2) \
    PV_(ob, VP, 4, Y, 24) \
    E_(X, 3) E_(X, 4) E_(X, 5) \
    "s_waitcnt lgkmcnt(1)\n\t" M1_(Y, KB) KBRD \
    E_(X, 6) E_(X, 7) E_(X, 8) E_(X, 9) E_(X, 10) E_(X, 11) E_(X, 12) E_(X, 13) E_(X, 14) E_(X, 15) \
    PV_(oa, VC, 0, X, 0) RDV(VP, VOFF) \
    E_(X, 16) E_(X, 17) E_(X, 18) \
    PV_(oa, VC, 4, X, 8) \
    E_(X, 19) E_(X, 20) E_(X, 21) \
    "s_waitcnt lgkmcnt(2)\n\t" M2_(Y, 0, q1a) \
    E_(X, 22) E_(X, 23) E_(X, 24) E_(X, 25) \
    PV_(ob, VC, 0, X, 16) \
    E_(X, 26) E_(X, 27) E_(X, 28) \
    M2_(Y, 16, q1b) RDKA(KAOFF) \
    E_(X, 29) E_(X, 30) E_(X, 31)

DI float max_halves(float m) {
    auto rr = __builtin_amdgcn_permlane32_swap(__float_as_uint(m), __float_as_uint(m), false, false);
    return fmaxf(__uint_as_float(rr[0]), __uint_as_float(rr[1]));
}
DI void attn_fallback(const char* lds, int l, const char* qp, int tile, f32x16& o) {
    const int r32 = l & 31, h = l >> 5;
    const s16x4 z4 = {0, 0, 0, 0};
    const bf16x8 q1 = *(const bf16x8*)(qp + tile * 1024 + l * 16);
    const s16x4 q2 = *(const s16x4*)(qp + 2048 + r32 * 16 + tile * 8);
    const bf16x8 q2f = cat8(h == 0 ? q2 : z4, z4);
    const bf16x8 ones = {0x3f80, 0x3f80, 0x3f80, 0x3f80, 0x3f80, 0x3f80, 0x3f80, 0x3f80};
    float m = -INFINITY;
#pragma unroll
    for (int i = 0; i < 16; ++i) o[i] = 0.f;
#pragma unroll 1
    for (int t = 0; t < 64; ++t) {
        const LDSP char* cb = (const LDSP char*)lds + (t >> 1) * 5120;
        const bf16x8 k1 = *(const LDSP bf16x8*)(cb + (t & 1) * 1024 + l * 16);
        const s16x4 k2 = *(const LDSP s16x4*)(cb + 2048 + r32 * 16 + (t & 1) * 8);
        f32x16 s;
#pragma unroll
        for (int i = 0; i < 16; ++i) s[i] = 0.f;
        s = MFMA32(k1, q1, s);
        s = MFMA32(cat8(k2, z4), q2f, s);
        float tm = s[0];
#pragma unroll
        for (int i = 1; i < 16; ++i) tm = fmaxf(tm, s[i]);
        tm = max_halves(tm);
        const float mn = fmaxf(m, tm), alpha = __builtin_amdgcn_exp2f(m - mn);
        m = mn;
        u32x4 p0, p1;
#pragma unroll
        for (int i = 0; i < 4; ++i) {
            p0[i] = pk_bf16(__builtin_amdgcn_exp2f(s[2 * i] - m), __builtin_amdgcn_exp2f(s[2 * i + 1] - m));
            p1[i] = pk_bf16(__builtin_amdgcn_exp2f(s[8 + 2 * i] - m), __builtin_amdgcn_exp2f(s[9 + 2 * i] - m));
        }
#pragma unroll
        for (int i = 0; i < 16; ++i) o[i] *= alpha;
        const int dv = r32 < 20 ? r32 : 0;
        const LDSP char* vb = cb + 2560 + (t & 1) * 1280 + dv * 16 + h * 8;
        bf16x8 v0 = cat8(*(const LDSP s16x4*)vb, *(const LDSP s16x4*)(vb + 320)), v1 = cat8(*(const LDSP s16x4*)(vb + 640), *(const LDSP s16x4*)(vb + 960));
        if (r32 >= 20) { v0 = ones; v1 = ones; }
        o = MFMA32(v0, __builtin_bit_cast(bf16x8, p0), o);
        o = MFMA32(v1, __builtin_bit_cast(bf16x8, p1), o);
    }
}

DI bool attn_ok(const f32x16& a, const f32x16& b) {
    f32x2 s = {0.f, 0.f};
#pragma unroll
    for (int i = 0; i < 12; i += 2) s += f32x2{a[i], a[i + 1]} + f32x2{b[i], b[i + 1]};
    return (fmaxf(a[12], b[12]) < 0x1p60f) & (fabsf(s[0] + s[1] + a[12] + b[12]) < 0x1p126f);
}
DI void attn_pack(const f32x16& oa, const f32x16& ob, float ga, float gb, u32x4& va, u32x4& vb, u32x2& vt) {
    const float fa = ga * __builtin_amdgcn_rcpf(oa[12]), fb = gb * __builtin_amdgcn_rcpf(ob[12]);
    va = u32x4{pk_bf16(oa[0] * fa, oa[1] * fa), pk_bf16(oa[2] * fa, oa[3] * fa), pk_bf16(oa[4] * fa, oa[5] * fa), pk_bf16(oa[6] * fa, oa[7] * fa)};
    vb = u32x4{pk_bf16(ob[0] * fb, ob[1] * fb), pk_bf16(ob[2] * fb, ob[3] * fb), pk_bf16(ob[4] * fb, ob[5] * fb), pk_bf16(ob[6] * fb, ob[7] * fb)};
    auto s0 = __builtin_amdgcn_permlane32_swap(pk_bf16(oa[8] * fa, oa[9] * fa), pk_bf16(ob[8] * fb, ob[9] * fb), false, false);
    auto s1 = __builtin_amdgcn_permlane32_swap(pk_bf16(oa[10] * fa, oa[11] * fa), pk_bf16(ob[10] * fb, ob[11] * fb), false, false);
    vt = u32x2{s0[0], s1[0]};
}

typedef __attribute__((ext_vector_type(16))) unsigned u32x16;
#define WSUB(v, i) __builtin_bit_cast(bf16x8, __builtin_shufflevector(v, v, 4 * (i), 4 * (i) + 1, 4 * (i) + 2, 4 * (i) + 3))

__global__ __launch_bounds__(512, 2) void k_attn(char* __restrict__ ws, char* __restrict__ out) {
    __shared__ __attribute__((aligned(16))) char lds[163840];
    const int b = blockIdx.x, xcd = b & 7, ii = b >> 3;
    const int pos = ii >> 2, head = (pos >> 2) * 32 + xcd * 4 + (pos & 3), slot = xcd * 8 + pos, qb = ii & 3;
    const int w = __builtin_amdgcn_readfirstlane(threadIdx.x >> 6);
    int l = threadIdx.x & 63, r32 = l & 31, h = l >> 5;
    const int qc = qb * 8 + w;
    const unsigned lds0 = (unsigned)(uintptr_t)lds;
    const unsigned qo = (unsigned)WS_Q + head * (unsigned)Q_HEAD + qc * 2560, kvo = (unsigned)WS_KV + (head < 32 ? 32 : head - 32) * (unsigned)KV_HEAD;
    bf16x8 q1a, q1b, k1f, k2f; s16x4 q2; float ga, gb;
    unsigned dv = kvo + w * 4096 + l * 16;
    {
        unsigned sv, dm = __builtin_amdgcn_readfirstlane(lds0 + w * 4096);
        asm volatile(
            "global_load_dwordx4 v[4:7], %[a0], %[kvp]\n\tglobal_load_dwordx4 v[8:11], %[a0], %[kvp] offset:1024\n\tglobal_load_dwordx2 v[12:13], %[a1], %[kvp] offset:2048\n\t"
            "global_load_dwordx4 " KA_ ", %[a2], %[kvp]\n\tglobal_load_dwordx4 v[" STR(KBR) ":" STR(KBR) "+3], %[a3], %[kvp] offset:2048\n\t"
            "global_load_dword v14, %[a4], %[kvp]\n\tglobal_load_dword v15, %[a4], %[kvp] offset:128\n\t"
            "s_mov_b32 %[sv], m0\n\t" DMA4 "s_mov_b32 m0, %[sv]"
            : "=&{v[4:7]}"(q1a), "=&{v[8:11]}"(q1b), "=&{v[12:13]}"(q2), "=&{v14}"(ga), "=&{v15}"(gb), "=&{v[168:171]}"(k1f), "=&{v[172:175]}"(k2f),
              [sv] "=&s"(sv), [dv] "+v"(dv), [dm] "+s"(dm)
            : [kvp] "s"(ws), [a0] "v"(qo + l * 16), [a1] "v"(qo + r32 * 16 + h * 8), [a2] "v"(kvo + l * 16), [a3] "v"(kvo + r32 * 16), [a4] "v"((unsigned)WS_G + qc * 256 + r32 * 4)
            : "memory", "scc");
    }
    const int mb = xcd * 4 + (ii & 3), nb = ii >> 2;
    const char* wp = ws + WS_WO + (size_t)(nb * 8 + w) * 25600 + 2048;
    const char* cp = ws + WS_CNT + (((mb >> 3) * 8 + w) * 8 + nb) * 128;
    const char* sgp = ws + WS_CNT + (qb * 8 + xcd) * 8 * 128;
    const char* a1 = ws + WS_O1 + (size_t)w * 8 * 65536 + mb * 2048;
    const char* a2 = ws + WS_O2 + (size_t)w * 4 * 32768 + mb * 1024;
    const char* op = out + (size_t)mb * 64 * 1280 + nb * 160;
    const char* po1 = ws + WS_O1 + (size_t)slot * 65536 + qc * 2048;
    const char* po2 = ws + WS_O2 + (size_t)(slot >> 1) * 32768 + qc * 1024 + (slot & 1) * 8;
    f32x16 oa, ob; u32x16 vini;
#pragma unroll
    for (int i = 0; i < 16; ++i) { oa[i] = 0.f; ob[i] = 0.f; vini[i] = (r32 & 27) == 24 ? 0x3f803f80u : 0u; }
    unsigned ak1 = lds0 + l * 16, ak2 = lds0 + 2048 + r32 * 16, av = lds0 + 2560 + h * 320 + r32 * 16, wv = l * 16;
    unsigned cnt, inc, svm, dm = __builtin_amdgcn_readfirstlane(lds0 + w * 4096 + 32768);
    u32x16 wt0, wt1, wt2, wt3, wt4;
    asm volatile(
        "s_mov_b32 %[sv], m0\n\t"
        ".irp n,24,25,26,27,28,29,30,31\n\tv_mov_b32 v[" STR(S1R) "+\\n], 0\n\t.endr\n\t"
        "s_mov_b32 %[cnt], 0\n\ts_mov_b32 %[inc], 20480\n\ts_mov_b64 vcc, -1\n\t"
        "s_waitcnt vmcnt(6)\n\t"
        "v_mfma_f32_32x32x4_2b_bf16 v[" STR(S0R) ":" STR(S0R) "+31], " KB0_ ", %[q2], 0\n\ts_nop 15\n\ts_nop 1\n\t"
        M2_(S0R, 0, q1a) M2_(S0R, 16, q1b)
        "s_nop 10\n\t"
        MX3(0, S0R + 0, 1) MX3(16, S0R + 16, 17) MX3(0, NMR, 3) MX3(16, NMR + 16, 19) MX3(0, NMR, 5) MX3(16, NMR + 16, 21) MX3(0, NMR, 7) MX3(16, NMR + 16, 23)
        MX3(0, NMR, 9) MX3(16, NMR + 16, 25) MX3(0, NMR, 11) MX3(16, NMR + 16, 27) MX3(0, NMR, 13) MX3(16, NMR + 16, 29) MX3(0, NMR, 14) MX3(16, NMR + 16, 30)
        "v_mov_b32 v[" STR(NMR) "+1], v[" STR(NMR) "]\n\tv_mov_b32 v[" STR(NMR) "+17], v[" STR(NMR) "+16]\n\ts_nop 1\n\t"
        "v_permlane32_swap_b32 v[" STR(NMR) "], v[" STR(NMR) "+1]\n\tv_permlane32_swap_b32 v[" STR(NMR) "+16], v[" STR(NMR) "+17]\n\ts_nop 1\n\t"
        "v_min_f32 v[" STR(NMR) "], -v[" STR(NMR) "], -v[" STR(NMR) "+1]\n\tv_min_f32 v[" STR(NMR) "+16], -v[" STR(NMR) "+16], -v[" STR(NMR) "+17]\n\t"
        "v_mov_b32 v[" STR(NMR) "+1], v[" STR(NMR) "]\n\tv_mov_b32 v[" STR(NMR) "+17], v[" STR(NMR) "+16]\n\t"
        ".irp n,0,2,4,6,8,10,12,14\n\tv_pk_add_f32 v[" STR(S0R) "+\\n:" STR(S0R) "+\\n+1], v[" STR(S0R) "+\\n:" STR(S0R) "+\\n+1], v[" STR(NMR) ":" STR(NMR) "+1]\n\t"
        "v_pk_add_f32 v[" STR(S0R) "+16+\\n:" STR(S0R) "+17+\\n], v[" STR(S0R) "+16+\\n:" STR(S0R) "+17+\\n], v[" STR(NMR) "+16:" STR(NMR) "+17]\n\t.endr\n\t"
        ".irp n,2,4,6,8,10,12,14\n\tv_mov_b64 v[" STR(NMR) "+\\n:" STR(NMR) "+\\n+1], v[" STR(NMR) ":" STR(NMR) "+1]\n\t"
        "v_mov_b64 v[" STR(NMR) "+16+\\n:" STR(NMR) "+17+\\n], v[" STR(NMR) "+16:" STR(NMR) "+17]\n\t.endr\n\t"
        "s_waitcnt vmcnt(0)\n\ts_barrier\n\t"
        RDV(VAR, 0) RDKA(1024)
        "1:\n\t"
        TILE(S0R, S1R, VAR, VBR, 5120, KB1_, RDKB(5120), 1280, PRIO(3) "s_cbranch_vccnz 11f\n\t21:\n\t")
        TILE(S1R, S0R, VBR, VAR, 5120 + 1024, KB0_, "", 5120, "s_cbranch_vccnz 12f\n\t22:\n\t")
        TILE(S0R, S1R, VAR, VBR, 10240, KB1_, RDKB(10240), 5120 + 1280, "s_cbranch_vccnz 13f\n\t23:\n\t")
        TILE(S1R, S0R, VBR, VAR, 10240 + 1024, KB0_, "", 10240, "s_cbranch_vccnz 14f\n\t24:\n\t")
        TILE(S0R, S1R, VAR, VBR, 15360, KB1_, RDKB(15360), 10240 + 1280, "")
        TILE(S1R, S0R, VBR, VAR, 15360 + 1024, KB0_, "", 15360, PRIO(2) "s_cmp_eq_u32 %[cnt], 7\n\ts_cselect_b32 %[inc], -143360, %[inc]\n\t")
        TILE(S0R, S1R, VAR, VBR, 0, KB1_, RDKB(0), 15360 + 1280, PRIO(1) "v_add_u32 %[ak1], %[inc], %[ak1]\n\tv_add_u32 %[ak2], %[inc], %[ak2]\n\t")
        TILE(S1R, S0R, VBR, VAR, 1024, KB0_, "", 0, PRIO(0) "v_add_u32 %[av], %[inc], %[av]\n\t")
        "s_add_u32 %[cnt], %[cnt], 1\n\t"
        "s_cmp_eq_u32 %[cnt], 1\n\ts_cbranch_scc1 2f\n\t"
        "s_cmp_eq_u32 %[cnt], 3\n\ts_cbranch_scc1 3f\n\t"
        "s_cmp_eq_u32 %[cnt], 4\n\ts_cbranch_scc1 5f\n\t"
        "s_cmp_eq_u32 %[cnt], 5\n\ts_cbranch_scc1 6f\n\t"
        "s_cmp_eq_u32 %[cnt], 6\n\ts_cbranch_scc1 8f\n\t"
        "s_cmp_lt_u32 %[cnt], 8\n\ts_cbranch_scc1 1b\n\t"
        "s_branch 4f\n\t"
        "11:\n\t" DMA4 "s_branch 21b\n\t"
        "12:\n\t" DMA4 "s_branch 22b\n\t"
        "13:\n\t" DMA4 "s_branch 23b\n\t"
        "14:\n\t" DMA4 "s_branch 24b\n\t"
        "2:\n\ts_mov_b64 vcc, 0\n\ts_waitcnt vmcnt(0)\n\ts_barrier\n\ts_branch 1b\n\t"
        "3:\n\t" WLD(0)
        "5:\n\t" WLD(1)
        "6:\n\t" WLD(2)
        "8:\n\tv_and_b32 %[dv], 0x3ff, %[wv]\n\tv_lshlrev_b32 %[dv], 3, %[dv]\n\tv_min_u32 %[dv], 0x1380, %[dv]\n\tv_add_u32 %[dv], 0x5000, %[dv]\n\t"
        "global_load_dword %[dv], %[dv], %[wp] offset:-2048\n\t" WLD(3)
        "4:\n\ts_nop 2\n\t"
        PV_(ob, VBR, 4, S1R, 24)
        "s_mov_b32 m0, %[sv]\n\ts_waitcnt vmcnt(0) lgkmcnt(0)\n\ts_nop 15\n\ts_nop 7"
        : [oa] "+{v[24:39]}"(oa), [ob] "+{v[40:55]}"(ob), [ak1] "+v"(ak1), [ak2] "+v"(ak2), [av] "+v"(av), [dv] "+v"(dv), [wv] "+v"(wv),
          "+{v[152:167]}"(vini), "+{v[168:171]}"(k1f), "+{v[172:175]}"(k2f),
          [q1a] "+{v[4:7]}"(q1a), [q1b] "+{v[8:11]}"(q1b), [q2] "+{v[12:13]}"(q2), "+{v14}"(ga), "+{v15}"(gb),
          "=&{v[176:191]}"(wt0), "=&{v[192:207]}"(wt1), "=&{v[208:223]}"(wt2), "=&{v[224:239]}"(wt3), "=&{v[240:255]}"(wt4),
          [cnt] "=&s"(cnt), [inc] "=&s"(inc), [sv] "=&s"(svm), [dm] "+s"(dm)
        : [vmask] "s"(0x000fffff000fffffull), [kvp] "s"(ws), [wp] "s"(wp), [idx] "v"(0xddddu),
          "s"(cp), "s"(sgp), "s"(a1), "s"(a2), "s"(op), "s"(po1), "s"(po2)
        : "memory", "scc", "vcc", "v56", "v57", "v58", "v59", CL10(6), CL10(7), CL10(8), CL10(9), CL10(10), CL10(11), CL10(12), CL10(13), CL10(14), "v150", "v151"
          );
    asm volatile("v_mbcnt_lo_u32_b32 %0, -1, 0\n\tv_mbcnt_hi_u32_b32 %0, -1, %0" : "=v"(l));
    r32 = l & 31; h = l >> 5;
    bf16x8 wl[5], af[5][4]; unsigned seen;
    {
        if (head >= 32) { ga = 1.0f - ga; gb = 1.0f - gb; }
        u32x4 va, vb; u32x2 vt;
        attn_pack(oa, ob, ga, gb, va, vb, vt);
        if (__builtin_expect(!__all(attn_ok(oa, ob)), 0)) {
#pragma unroll 1
            for (int t = 0; t < 2; ++t) {
                f32x16 ro; u32x4 v0, v1; u32x2 v2;
                attn_fallback(lds, l, ws + qo, t, ro);
                attn_pack(ro, ro, t ? gb : ga, t ? gb : ga, v0, v1, v2);
                if (t == 0) va = v0; else vb = v0;
                if (h == t) vt = v2;
            }
        }
        asm volatile(
            "global_store_dwordx4 %[lo], %[va], %[o1] sc1\n\tglobal_store_dwordx4 %[lo], %[vb], %[o1] offset:1024 sc1\n\tglobal_store_dwordx2 %[lo], %[vt], %[o2] sc1\n\t"
            "s_nop 0"
            :: [lo] "v"(l * 16), [va] "v"(va), [vb] "v"(vb), [vt] "v"(vt), [o1] "s"(po1), [o2] "s"(po2)
            : "memory");
    }
    const int r = l & 15, g = l >> 4, r8 = l & 7, g2 = (l >> 3) & 3;
    unsigned o1 = (g >> 1) * 65536 + (g & 1) * 512 + r * 16, o2 = g * 32768 + r * 16;
    unsigned wbo = w * 20480 + (g * 16 + (r ^ ((g >> 1) << 3))) * 16;
    unsigned rbo = ((w >> 1) * 5 + h) * 1024 + (g2 * 16 + ((8 * (w & 1) + r8) ^ ((g2 >> 1) << 3))) * 16;
    unsigned opo = (8 * w + r8) * 1280 + h * 32 + g2 * 8, sgo = l * 128;
    asm volatile("s_waitcnt vmcnt(0)\n\ts_barrier" : "+v"(o1), "+v"(o2), "+v"(wbo), "+v"(rbo), "+v"(opo), "+v"(sgo) :: "memory");
    if (w == (xcd ^ 1) && l < 8) __hip_atomic_fetch_add((__attribute__((address_space(1))) unsigned*)(sgp + sgo), 1u, __ATOMIC_RELAXED, __HIP_MEMORY_SCOPE_AGENT);
    asm volatile(
        "global_load_dwordx4 %0, %6, %7 offset:-2048\n\tglobal_load_dwordx4 %1, %6, %7 offset:-1024\n\tglobal_load_dwordx4 %2, %6, %7\n\t"
        "global_load_dwordx4 %3, %6, %7 offset:1024\n\tglobal_load_dwordx4 %4, %6, %7 offset:2048\n\tglobal_load_dword %5, %8, %9 sc1\n\ts_waitcnt vmcnt(0)"
        : "=&v"(wl[0]), "=&v"(wl[1]), "=&v"(wl[2]), "=&v"(wl[3]), "=&v"(wl[4]), "=&v"(seen) : "v"(wv), "s"(wp), "v"(0), "s"(cp) : "memory");
#pragma unroll 1
    for (int spin = 0; spin < 400000 && __builtin_amdgcn_readfirstlane(seen) < 8; ++spin) {
        __builtin_amdgcn_s_sleep(4);
        asm volatile("global_load_dword %0, %1, %2 sc1\n\ts_waitcnt vmcnt(0)" : "=v"(seen) : "v"(0), "s"(cp) : "memory");
    }
#define ALD(s, base, off, d1, d2) asm volatile( \
        "global_load_dwordx4 %0, %4, %5 sc1\n\tglobal_load_dwordx4 %1, %4, %5 offset:" #d1 " sc1\n\t" \
        "global_load_dwordx4 %2, %4, %5 offset:" #d2 " sc1\n\tglobal_load_dwordx4 %3, %4, %5 offset:" #d1 "+" #d2 " sc1" \
        : "=&v"(af[s][0]), "=&v"(af[s][1]), "=&v"(af[s][2]), "=&v"(af[s][3]) : "v"(off), "s"(base) : "memory")
#define AWAIT(s, N) asm volatile("s_waitcnt vmcnt(" STR(N) ")" : "+v"(af[s][0]), "+v"(af[s][1]), "+v"(af[s][2]), "+v"(af[s][3]) :: "memory")
#define OMM(s, w0, w1, w2, w3, w4) do { const bf16x8 wf_[5] = {w0, w1, w2, w3, w4}; \
        _Pragma("unroll") for (int j = 0; j < 5; ++j) _Pragma("unroll") for (int i = 0; i < 4; ++i) acc[i][j] = MFMA16(wf_[j], af[s][i], acc[i][j]); \
        __builtin_amdgcn_sched_barrier(0); } while (0)
    f32x4 acc[4][5];
#pragma unroll
    for (int i = 0; i < 4; ++i)
#pragma unroll
        for (int j = 0; j < 5; ++j) acc[i][j] = f32x4{0.f, 0.f, 0.f, 0.f};
    ALD(0, a1, o1, 256, 1024); ALD(1, a1 + 131072, o1, 256, 1024); ALD(2, a1 + 2 * 131072, o1, 256, 1024);
    AWAIT(0, 8); ALD(3, a1 + 3 * 131072, o1, 256, 1024); OMM(0, WSUB(wt0, 0), WSUB(wt0, 1), WSUB(wt0, 2), WSUB(wt0, 3), WSUB(wt1, 0));
    AWAIT(1, 8); ALD(4, a2, o2, 256, 512); OMM(1, WSUB(wt1, 1), WSUB(wt1, 2), WSUB(wt1, 3), WSUB(wt2, 0), WSUB(wt2, 1));
    AWAIT(2, 8); OMM(2, WSUB(wt2, 2), WSUB(wt2, 3), WSUB(wt3, 0), WSUB(wt3, 1), WSUB(wt3, 2));
    AWAIT(3, 4); OMM(3, WSUB(wt3, 3), WSUB(wt4, 0), WSUB(wt4, 1), WSUB(wt4, 2), WSUB(wt4, 3));
    AWAIT(4, 0);
#pragma unroll
    for (int j = 0; j < 6; ++j) {
        if (j < 5) {
#pragma unroll
            for (int i = 0; i < 4; ++i) acc[i][j] = MFMA16(wl[j], af[4][i], acc[i][j]);
        }
        if (j > 0) {
#pragma unroll
            for (int i = 0; i < 4; ++i) *(f32x4*)(lds + wbo + (i * 5 + j - 1) * 1024) = acc[i][j - 1];
        }
        __builtin_amdgcn_sched_barrier(0);
    }
    {
        __syncthreads();
        f32x4 t[3][8];
#pragma unroll
        for (int u = 0; u < 8; ++u)
#pragma unroll
            for (int p = 0; p < 3; ++p) t[p][u] = *(const f32x4*)(lds + rbo + u * 20480 + (p < 2 ? p * 2048 : 4096 - h * 1024));
        __builtin_amdgcn_sched_barrier(0);
#pragma unroll
        for (int p = 0; p < 3; ++p) {
            const f32x4 z = ((t[p][0] + t[p][1]) + (t[p][2] + t[p][3])) + ((t[p][4] + t[p][5]) + (t[p][6] + t[p][7]));
            const u32x2 v = {pk_bf16(z[0], z[1]), pk_bf16(z[2], z[3])};
            if (p < 2 || h == 0) asm volatile("global_store_dwordx2 %0, %1, %2 offset:%3" :: "v"(opo), "v"(v), "s"(op), "i"(p * 64) : "memory");
        }
    }
}

extern "C" void kernel_launch(void* const* d_in, const int* in_sizes, int n_in, void* d_out, int out_size, void* d_ws, size_t ws_size,
                              hipStream_t stream) {
    const char* x = (const char*)d_in[0];
    const char* wq = (const char*)d_in[1];
    const char* wk = (const char*)d_in[2];
    const char* wv = (const char*)d_in[3];
    const char* woa = (const char*)d_in[4];
    const char* wqkv = (const char*)d_in[6];
    const char* wob = (const char*)d_in[7];
    const char* gw = (const char*)d_in[8];
    char* ws = (char*)d_ws;
    k_proj<<<256, 768, 0, stream>>>(x, wq, wqkv, wk, wv, gw, woa, wob, ws);
    k_attn<<<256, 512, 0, stream>>>(ws, (char*)d_out);
}
```
